```python
import torch

def _softmax(x, axis=-1):
    return torch.softmax(x, dim=axis)
def _gelu(x):
    return torch.nn.functional.gelu(x, approximate="tanh")
def _silu(x):
    return torch.nn.functional.silu(x)
def _sigmoid(x):
    return torch.sigmoid(x)
def _layernorm(x, axis=-1, eps=1e-5):
    m = x.mean(dim=axis, keepdim=True)
    v = x.var(dim=axis, keepdim=True, unbiased=False)
    return (x - m) / torch.sqrt(v + eps)
def _scatter_add(tgt, idx, src, axis=0):
    return tgt.clone().index_add_(axis, idx, src)
def _segment_sum(src, seg, n):
    out = torch.zeros((n,) + src.shape[1:], dtype=src.dtype)
    return out.index_add_(0, seg, src)
def _pad(x, pw):
    flat = tuple(v for pair in reversed(pw) for v in pair)
    return torch.nn.functional.pad(x, flat)

kernel_name = 'sequential_preLN__gqa_block_cached__geglu_ffn__decode'

def setup_inputs():

    _w = (lambda a: (lambda t: t.to(torch.bfloat16) if t.is_floating_point() else t)(torch.as_tensor(a)))
    def _A_setup():
        B, Sq, D = 4, 1, 640
        Hq, Hkv, Sk, Dh = 32, 32, 4096, 128
        assert Hq % Hkv == 0, (Hq, Hkv)
        return {
            "x": _w((lambda *s: torch.randn(*s, dtype=torch.bfloat16) * 0.05)(B, Sq, D)),
            "k_cache": _w((lambda *s: torch.randn(*s, dtype=torch.bfloat16) * 3.2)(B, Hkv, Sk, Dh)),
            "v_cache": _w((lambda *s: torch.randn(*s, dtype=torch.bfloat16) * 0.4)(B, Hkv, Sk, Dh)),
            "wq": _w((lambda *s: torch.randn(*s, dtype=torch.bfloat16) * 3.2)(D, Hq * Dh) * (D ** -0.5)),
            "wo": _w((lambda *s: torch.randn(*s, dtype=torch.bfloat16) * 0.4)(Hq * Dh, D) * ((Hq * Dh) ** -0.5)),
        }
    _A_in = _A_setup()

    def _B_setup():
        B, S, D, F = 4, 1, 640, 1728
        return {
            "x": _w((lambda *s: torch.randn(*s, dtype=torch.bfloat16) * 0.05)(B, S, D)),
            "w_gate": _w((lambda *s: torch.randn(*s, dtype=torch.bfloat16) * 0.05)(D, F)),
            "w_up": _w((lambda *s: torch.randn(*s, dtype=torch.bfloat16) * 0.05)(D, F)),
            "w_down": _w((lambda *s: torch.randn(*s, dtype=torch.bfloat16) * 0.05)(F, D)),
        }
    _B_in = _B_setup()

    return {
        "A_x": _A_in["x"],
        "A_k_cache": _A_in["k_cache"],
        "A_v_cache": _A_in["v_cache"],
        "A_wq": _A_in["wq"],
        "A_wo": _A_in["wo"],
        "B_x": _B_in["x"],
        "B_w_gate": _B_in["w_gate"],
        "B_w_up": _B_in["w_up"],
        "B_w_down": _B_in["w_down"],
    }

def reference(A_x, A_k_cache, A_v_cache, A_wq, A_wo, B_x, B_w_gate, B_w_up, B_w_down):
    def _A_ref(x, k_cache, v_cache, wq, wo):
        B, Sq, D = x.shape
        Hq, Dh = 32, 128
        Hkv = k_cache.shape[1]
        rep = Hq // Hkv
        q = (lambda x, *d: x.permute(*d))((x @ wq).reshape(B, Sq, Hq, Dh), 0, 2, 1, 3)
        k = (lambda x, n, axis: torch.repeat_interleave(x, n, dim=axis))(k_cache, rep, axis=1)
        v = (lambda x, n, axis: torch.repeat_interleave(x, n, dim=axis))(v_cache, rep, axis=1)
        scores = torch.einsum("bhqd,bhkd->bhqk", q, k) * (Dh ** -0.5)
        p = _softmax(scores, axis=-1)
        o = torch.einsum("bhqk,bhkd->bhqd", p, v)
        return (lambda x, *d: x.permute(*d))(o, 0, 2, 1, 3).reshape(B, Sq, Hq * Dh) @ wo

    def _B_ref(x, w_gate, w_up, w_down):
        return (_gelu(x @ w_gate) * (x @ w_up)) @ w_down

    x = A_x
    h = x + _A_ref(_layernorm(x, axis=-1), A_k_cache, A_v_cache, A_wq, A_wo)
    h2 = h + _B_ref(_layernorm(h, axis=-1), B_w_gate, B_w_up, B_w_down)
    return h2
```

```cpp
#include <hip/hip_runtime.h>
#include <hip/hip_bf16.h>
#include <stdint.h>
#define MIMIC 0
typedef unsigned short u16;
typedef unsigned v4u __attribute__((ext_vector_type(4)));
constexpr int B = 4, D = 640, H = 32, SK = 4096, DH = 128, F = 1728, NSPLIT = 8;

__device__ __forceinline__ float bf2f(u16 b) { return __builtin_bit_cast(float, (unsigned)b << 16); }
__device__ __forceinline__ u16 f2bf(float f) { __hip_bfloat16 h = __float2bfloat16(f); return __builtin_bit_cast(u16, h); }
__device__ __forceinline__ float rnd(float f) { return MIMIC ? bf2f(f2bf(f)) : f; }

__device__ void ln_row(const float* in, float* out, float* red) {
  int t = threadIdx.x;
  float s = 0.f;
  for (int i = t; i < D; i += 256) s += in[i];
  red[t] = s; __syncthreads();
  for (int o = 128; o > 0; o >>= 1) { if (t < o) red[t] += red[t + o]; __syncthreads(); }
  float mean = red[0] / D; __syncthreads();
  s = 0.f;
  for (int i = t; i < D; i += 256) { float d = in[i] - mean; s += d * d; }
  red[t] = s; __syncthreads();
  for (int o = 128; o > 0; o >>= 1) { if (t < o) red[t] += red[t + o]; __syncthreads(); }
  float var = red[0] / D; __syncthreads();
  float m = rnd(mean), v = rnd(var);
  float den = rnd(sqrtf(rnd(v + 1e-5f)));
  for (int i = t; i < D; i += 256) out[i] = rnd(rnd(in[i] - m) / den);
  __syncthreads();
}

__global__ void k_q(const u16* x, const u16* wq, float* q) {
  __shared__ float xs[D], xn[B][D], red[256];
  for (int b = 0; b < B; ++b) {
    for (int i = threadIdx.x; i < D; i += 256) xs[i] = bf2f(x[b * D + i]);
    __syncthreads();
    ln_row(xs, xn[b], red);
  }
  int c = blockIdx.x * 256 + threadIdx.x;
  float acc[B] = {0, 0, 0, 0};
  for (int k = 0; k < D; ++k) { float w = bf2f(wq[(size_t)k * (H * DH) + c]);
    for (int b = 0; b < B; ++b) acc[b] += xn[b][k] * w; }
  for (int b = 0; b < B; ++b) q[b * H * DH + c] = rnd(acc[b]);
}

__global__ void k_attn(const float* q, const u16* kc, const u16* vc, float* pm, float* pl, float* po) {
  constexpr int KS = SK / NSPLIT;
  __shared__ float sc[KS], qs[DH], red[256], oacc[16][DH];
  int s = blockIdx.x, h = blockIdx.y, b = blockIdx.z, t = threadIdx.x;
  if (t < DH) qs[t] = q[b * H * DH + h * DH + t];
  __syncthreads();
  const u16* kb = kc + ((size_t)(b * H + h) * SK + s * KS) * DH;
  const u16* vb = vc + ((size_t)(b * H + h) * SK + s * KS) * DH;
  int r = t >> 4, c = t & 15;
  for (int k0 = 0; k0 < KS; k0 += 16) {
    v4u kv = *(const v4u*)(kb + (size_t)(k0 + r) * DH + c * 8);
    float d = 0.f;
    for (int j = 0; j < 4; ++j) { d += bf2f(kv[j] & 0xffff) * qs[c * 8 + 2 * j] + bf2f(kv[j] >> 16) * qs[c * 8 + 2 * j + 1]; }
    for (int o = 8; o > 0; o >>= 1) d += __shfl_xor(d, o);
    if (c == 0) sc[k0 + r] = rnd(rnd(d) * 0.08838834764831845f);
  }
  __syncthreads();
  float m = -INFINITY;
  for (int i = t; i < KS; i += 256) m = fmaxf(m, sc[i]);
  red[t] = m; __syncthreads();
  for (int o = 128; o > 0; o >>= 1) { if (t < o) red[t] = fmaxf(red[t], red[t + o]); __syncthreads(); }
  m = red[0]; __syncthreads();
  float l = 0.f;
  for (int i = t; i < KS; i += 256) { float p = __expf(sc[i] - m); sc[i] = p; l += p; }
  red[t] = l; __syncthreads();
  for (int o = 128; o > 0; o >>= 1) { if (t < o) red[t] += red[t + o]; __syncthreads(); }
  l = red[0]; __syncthreads();
  float acc[8] = {0, 0, 0, 0, 0, 0, 0, 0};
  for (int k0 = 0; k0 < KS; k0 += 16) {
    v4u vv = *(const v4u*)(vb + (size_t)(k0 + r) * DH + c * 8);
    float p = sc[k0 + r];
    for (int j = 0; j < 4; ++j) { acc[2 * j] += p * bf2f(vv[j] & 0xffff); acc[2 * j + 1] += p * bf2f(vv[j] >> 16); }
  }
  for (int j = 0; j < 8; ++j) oacc[r][c * 8 + j] = acc[j];
  __syncthreads();
  int slot = (b * H + h) * NSPLIT + s;
  if (t < DH) { float o = 0.f; for (int i = 0; i < 16; ++i) o += oacc[i][t]; po[slot * DH + t] = o; }
  if (t == 0) { pm[slot] = m; pl[slot] = l; }
}

__global__ void k_comb(const float* pm, const float* pl, const float* po, float* o) {
  int bh = blockIdx.x, t = threadIdx.x;
  float M = -INFINITY;
  for (int s = 0; s < NSPLIT; ++s) M = fmaxf(M, pm[bh * NSPLIT + s]);
  float L = 0.f, a = 0.f;
  for (int s = 0; s < NSPLIT; ++s) { float w = __expf(pm[bh * NSPLIT + s] - M); L += w * pl[bh * NSPLIT + s]; a += w * po[(bh * NSPLIT + s) * DH + t]; }
  o[bh * DH + t] = rnd(a / L);
}

__global__ void k_wo(const float* o, const u16* wo, const u16* x, float* h) {
  __shared__ float red[4][64];
  int b = blockIdx.y, c = blockIdx.x * 64 + (threadIdx.x & 63), g = threadIdx.x >> 6;
  float a = 0.f;
  for (int k = g; k < H * DH; k += 4) a += o[b * H * DH + k] * bf2f(wo[(size_t)k * D + c]);
  red[g][threadIdx.x & 63] = a; __syncthreads();
  if (g == 0) { a = red[0][threadIdx.x] + red[1][threadIdx.x] + red[2][threadIdx.x] + red[3][threadIdx.x];
    h[b * D + c] = rnd(bf2f(x[b * D + c]) + rnd(a)); }
}

__device__ __forceinline__ float gelu_tanh(float x) {
  const float k0 = 0.7978845608028654f, k1 = 0.044715f;
  float inner = k0 * (x + k1 * x * x * x);
  return 0.5f * x * (1.f + tanhf(inner));
}
__global__ void k_gu(const float* h, const u16* wg, const u16* wu, float* a) {
  __shared__ float xs[D], xn[B][D], red[256], rg[4][B][64], ru[4][B][64];
  for (int b = 0; b < B; ++b) {
    for (int i = threadIdx.x; i < D; i += 256) xs[i] = h[b * D + i];
    __syncthreads();
    ln_row(xs, xn[b], red);
  }
  int c = blockIdx.x * 64 + (threadIdx.x & 63), g = threadIdx.x >> 6;
  float ag[B] = {0, 0, 0, 0}, au[B] = {0, 0, 0, 0};
  for (int k = g; k < D; k += 4) { float w1 = bf2f(wg[(size_t)k * F + c]), w2 = bf2f(wu[(size_t)k * F + c]);
    for (int b = 0; b < B; ++b) { ag[b] += xn[b][k] * w1; au[b] += xn[b][k] * w2; } }
  for (int b = 0; b < B; ++b) { rg[g][b][threadIdx.x & 63] = ag[b]; ru[g][b][threadIdx.x & 63] = au[b]; }
  __syncthreads();
  { int b = g, l = threadIdx.x & 63;
    float gg = rnd(rg[0][b][l] + rg[1][b][l] + rg[2][b][l] + rg[3][b][l]);
    float uu = rnd(ru[0][b][l] + ru[1][b][l] + ru[2][b][l] + ru[3][b][l]);
    a[b * F + c] = rnd(rnd(gelu_tanh(gg)) * uu); }
}
__global__ void k_down(const float* a, const u16* wd, const float* h, u16* out) {
  __shared__ float red[4][64];
  int b = blockIdx.y, c = blockIdx.x * 64 + (threadIdx.x & 63), g = threadIdx.x >> 6;
  float s = 0.f;
  for (int k = g; k < F; k += 4) s += a[b * F + k] * bf2f(wd[(size_t)k * D + c]);
  red[g][threadIdx.x & 63] = s; __syncthreads();
  if (g == 0) { s = red[0][threadIdx.x] + red[1][threadIdx.x] + red[2][threadIdx.x] + red[3][threadIdx.x];
    out[b * D + c] = f2bf(h[b * D + c] + rnd(s)); }
}

extern "C" void kernel_launch(void* const* d_in, const int* in_sizes, int n_in, void* d_out, int out_size, void* d_ws, size_t ws_size, hipStream_t stream) {
  const u16* x = (const u16*)d_in[0]; const u16* kc = (const u16*)d_in[1]; const u16* vc = (const u16*)d_in[2];
  const u16* wq = (const u16*)d_in[3]; const u16* wo = (const u16*)d_in[4];
  const u16* wg = (const u16*)d_in[6]; const u16* wu = (const u16*)d_in[7]; const u16* wd = (const u16*)d_in[8];
  float* ws = (float*)d_ws;
  float* q = ws; float* pm = q + B * H * DH; float* pl = pm + B * H * NSPLIT; float* po = pl + B * H * NSPLIT;
  float* o = po + B * H * NSPLIT * DH; float* h = o + B * H * DH; float* a = h + B * D;
  k_q<<<H * DH / 256, 256, 0, stream>>>(x, wq, q);
  k_attn<<<dim3(NSPLIT, H, B), 256, 0, stream>>>(q, kc, vc, pm, pl, po);
  k_comb<<<B * H, 128, 0, stream>>>(pm, pl, po, o);
  k_wo<<<dim3(D / 64, B), 256, 0, stream>>>(o, wo, x, h);
  k_gu<<<F / 64, 256, 0, stream>>>(h, wg, wu, a);
  k_down<<<dim3(D / 64, B), 256, 0, stream>>>(a, wd, h, (u16*)d_out);
}
```

```cpp
#define AUX_WQ 2
#define AUX_WO 0
#define AUX_FW 0
#define DRY 0
#include <hip/hip_runtime.h>
#include <hip/hip_bf16.h>
#include <stdint.h>

typedef unsigned short u16;
typedef unsigned v4u __attribute__((ext_vector_type(4)));
typedef unsigned v2u __attribute__((ext_vector_type(2)));
typedef float f32x2 __attribute__((ext_vector_type(2)));
typedef float f32x4 __attribute__((ext_vector_type(4)));
typedef __bf16 bf16x2 __attribute__((ext_vector_type(2)));
typedef __bf16 bf16x8 __attribute__((ext_vector_type(8)));
typedef __amdgpu_buffer_rsrc_t rsrc_t;
#define GAS __attribute__((address_space(1)))

#define START_BAR 1
#define NPRE 4

constexpr int NB = 4, DM = 640, NH = 32, SK = 4096, DH = 128, FF = 1728;
constexpr int NSW = 4, NHW = 5, NT = 64 * (NSW + NHW), NWG = 256;
constexpr int NRED = 160, NFFN = 108;
constexpr int NTW = 32;
constexpr int WS_EPOCH = 0;
constexpr int WS_ML = 4096;
constexpr int WS_H = 8192;
constexpr int WS_A = 20480;
constexpr int WS_Y = 65536;
constexpr int WS_END = WS_Y + NB * 40 * 64 * 128;
constexpr int WS_STAMP = WS_END;
constexpr int AUX_NT = 2, AUX_SC1 = 16;
constexpr unsigned SPIN_CAP = 1u << 18;
constexpr float LOG2E = 1.4426950408889634f;

#define STAMPC(cond, i) do { } while (0)

__device__ __forceinline__ rsrc_t mk_rsrc(const void* p, int bytes) { return __builtin_amdgcn_make_buffer_rsrc((void*)p, 0, bytes, 0x00020000); }
__device__ __forceinline__ float as_f(unsigned u) { return __builtin_bit_cast(float, u); }
__device__ __forceinline__ unsigned as_u(float f) { return __builtin_bit_cast(unsigned, f); }
__device__ __forceinline__ unsigned pk_bf16(float lo, float hi) { f32x2 v = {lo, hi}; return __builtin_bit_cast(unsigned, __builtin_convertvector(v, bf16x2)); }
__device__ __forceinline__ float rbf(float x) { return as_f(pk_bf16(0.f, x)); }
__device__ __forceinline__ float lo_f(unsigned u) { return as_f(u << 16); }
__device__ __forceinline__ float hi_f(unsigned u) { return as_f(u & 0xffff0000u); }
__device__ __forceinline__ float dot2(unsigned a, unsigned b, float c) { return __builtin_amdgcn_fdot2_f32_bf16(__builtin_bit_cast(bf16x2, a), __builtin_bit_cast(bf16x2, b), c, false); }
template <int CTRL> __device__ __forceinline__ unsigned dppu(unsigned x) { return __builtin_amdgcn_mov_dpp((int)x, CTRL, 0xf, 0xf, true); }
template <int CTRL> __device__ __forceinline__ float dppf(float x) { return as_f(dppu<CTRL>(as_u(x))); }
constexpr int ROR1 = 0x121, ROR2 = 0x122, ROR4 = 0x124, ROR8 = 0x128, XOR1 = 0xB1, XOR2 = 0x4E, XOR7 = 0x141, BCAST = 0x150;
__device__ __forceinline__ unsigned perm_lo(unsigned a, unsigned b) { return __builtin_amdgcn_perm(b, a, 0x05040100u); }
__device__ __forceinline__ unsigned perm_hi(unsigned a, unsigned b) { return __builtin_amdgcn_perm(b, a, 0x07060302u); }
__device__ __forceinline__ float rs16(float a, float b) { auto s = __builtin_amdgcn_permlane16_swap(as_u(a), as_u(b), false, false); return as_f(s[0]) + as_f(s[1]); }
__device__ __forceinline__ float rs32(float a, float b) { auto s = __builtin_amdgcn_permlane32_swap(as_u(a), as_u(b), false, false); return as_f(s[0]) + as_f(s[1]); }
__device__ __forceinline__ float xrow_sum(float x) { x = rs16(x, x); return rs32(x, x); }
__device__ __forceinline__ float xrow_max(float x) {
  auto s = __builtin_amdgcn_permlane16_swap(as_u(x), as_u(x), false, false); x = fmaxf(as_f(s[0]), as_f(s[1]));
  auto t = __builtin_amdgcn_permlane32_swap(as_u(x), as_u(x), false, false); return fmaxf(as_f(t[0]), as_f(t[1]));
}
__device__ __forceinline__ float wave_sum(float x) {
  x += dppf<ROR8>(x); x += dppf<ROR4>(x); x += dppf<ROR2>(x); x += dppf<ROR1>(x);
  return xrow_sum(x);
}
__device__ __forceinline__ void st128_sc1(v4u v, rsrc_t rs, int voff, int soff) {
  __builtin_amdgcn_raw_buffer_store_b128(v, rs, voff, soff, 16);
  asm volatile("s_nop 1" :: "v"(v) : "memory");
}
#define SB() __builtin_amdgcn_sched_barrier(0)
#define WAVE_SYNC() do { __builtin_amdgcn_fence(__ATOMIC_RELEASE, "wavefront"); __builtin_amdgcn_wave_barrier(); __builtin_amdgcn_fence(__ATOMIC_ACQUIRE, "wavefront"); } while (0)
__device__ __forceinline__ int fresh_lane() { int l; asm volatile("v_mbcnt_lo_u32_b32 %0, -1, 0\n\tv_mbcnt_hi_u32_b32 %0, -1, %0" : "=v"(l)); return l; }
#define FRESH_IDS() int lane = fresh_lane(); const int r = lane >> 4, c = lane & 15, htid = w * 64 + lane; (void)r; (void)c; (void)htid
#define TAGBAD(g) (((g)[1] ^ tag) | ((g)[3] ^ tag))

constexpr int L_QP = 0;
constexpr int L_OP = 2560;
constexpr int L_ML = L_OP + 2048;
constexpr int L_OB = L_ML + 64;
constexpr int L_RED = L_OB + 1280;
constexpr int L_XR = L_RED + 256;
constexpr int L_RED2 = L_RED + 320;
constexpr int L_GP = L_RED2 + 320;
constexpr int HN_STRIDE = 1344;
constexpr int L_HN = L_GP + 2560;
constexpr int L_BG = L_HN + 4 * HN_STRIDE;
constexpr int L_WD = L_BG + 5 * 8 * 1024;
constexpr int L_END = L_WD + 5 * 12 * 1024;
static_assert(L_BG % 16 == 0 && L_HN % 16 == 0 && L_OB % 16 == 0, "align");

__global__ void __launch_bounds__(NT, 3) fused_block(const u16* __restrict__ x, const u16* __restrict__ kc, const u16* __restrict__ vc,
                                                     const u16* __restrict__ wq, const u16* __restrict__ wo, const u16* __restrict__ wg,
                                                     const u16* __restrict__ wu, const u16* __restrict__ wd, u16* __restrict__ out, void* ws) {
  __shared__ __attribute__((aligned(16))) unsigned char lds[L_END];
  const int tid = threadIdx.x, lane = tid & 63, wv = __builtin_amdgcn_readfirstlane(tid >> 6);
  const int bid = blockIdx.x, xs = bid & 7, rr = bid >> 3;
  const int head = xs + 8 * (rr & 3), b = (rr >> 2) & 3, half = rr >> 4;
  const int r = lane >> 4, c = lane & 15;
  STAMPC(tid == 0, 0);

  if (wv >= NHW) {
    const int w = wv - NHW;
    __builtin_amdgcn_s_barrier();
    const rsrc_t rk = mk_rsrc(kc, NB * NH * SK * DH * 2), rv = mk_rsrc(vc, NB * NH * SK * DH * 2);
    const int kvbase = ((b * NH + head) * SK + half * 2048) * (DH * 2) + w * 4096;
    const int voff = lane * 16;
    v4u K[NPRE][4], V[NPRE][4];
#pragma unroll
    for (int t = 0; t < NPRE; ++t) {
#pragma unroll
      for (int j = 0; j < 4; ++j) K[t][j] = __builtin_amdgcn_raw_buffer_load_b128(rk, voff, kvbase + t * (NSW * 4096) + j * 1024, AUX_NT);
#pragma unroll
      for (int j = 0; j < 4; ++j) V[t][j] = __builtin_amdgcn_raw_buffer_load_b128(rv, voff, kvbase + t * (NSW * 4096) + j * 1024, AUX_NT);
    }
    SB();
    __builtin_amdgcn_s_barrier();
    __builtin_amdgcn_fence(__ATOMIC_ACQUIRE, "workgroup");
    v4u q;
    {
      f32x2 s2 = {0.f, 0.f};
#pragma unroll
      for (int ww = 0; ww < NHW; ++ww) s2 += *(const f32x2*)(lds + L_QP + ww * 512 + (8 * c + 2 * r) * 4);
      const unsigned qp = pk_bf16(s2.x, s2.y);
      auto s16 = __builtin_amdgcn_permlane16_swap(qp, qp, false, false);
      auto e0 = __builtin_amdgcn_permlane32_swap(s16[0], s16[0], false, false);
      auto e1 = __builtin_amdgcn_permlane32_swap(s16[1], s16[1], false, false);
      q = v4u{e0[0], e1[0], e0[1], e1[1]};
    }
    STAMPC(tid == NHW * 64, 1);
    float m = -INFINITY, l = 0.f, acc[8];
#pragma unroll
    for (int e = 0; e < 8; ++e) acc[e] = 0.f;

#define TILE_BODY(T, RELOAD, NEXT_SO) { \
    float sx[4]; \
    _Pragma("unroll") for (int j = 0; j < 4; ++j) { \
      float d = dot2(K[T][j][0], q[0], 0.f); d = dot2(K[T][j][1], q[1], d); d = dot2(K[T][j][2], q[2], d); d = dot2(K[T][j][3], q[3], d); \
      asm volatile("" : "+v"(d)); sx[j] = d; SB(); \
      if (RELOAD) { K[T][j] = __builtin_amdgcn_raw_buffer_load_b128(rk, voff, (NEXT_SO) + j * 1024, AUX_NT); SB(); } } \
    { const float a0 = sx[0] + dppf<ROR8>(sx[0]), a2 = sx[2] + dppf<ROR8>(sx[2]), a1 = sx[1] + dppf<ROR8>(sx[1]), a3 = sx[3] + dppf<ROR8>(sx[3]); \
      const float b0 = (c & 8) ? a2 : a0, b1 = (c & 8) ? a3 : a1; \
      const float c0 = b0 + dppf<XOR7>(b0), c1 = b1 + dppf<XOR7>(b1); \
      float s = (c & 4) ? c1 : c0; \
      s += dppf<XOR2>(s); s += dppf<XOR1>(s); \
      s = rbf(rbf(s) * 0.08838834764831845f) * LOG2E; \
      float pm = fmaxf(s, dppf<ROR4>(s)); pm = fmaxf(pm, dppf<ROR8>(pm)); \
      const float mn = fmaxf(m, pm); \
      const float alpha = __builtin_amdgcn_exp2f(m - mn), p = __builtin_amdgcn_exp2f(s - mn); \
      l = fmaf(l, alpha, p); m = mn; \
      _Pragma("unroll") for (int e = 0; e < 8; ++e) acc[e] *= alpha; \
      const unsigned plo = pk_bf16(p, 0.f), phi = pk_bf16(0.f, p); \
      PV(T, 0, RELOAD, NEXT_SO) PV(T, 1, RELOAD, NEXT_SO) PV(T, 2, RELOAD, NEXT_SO) PV(T, 3, RELOAD, NEXT_SO) } }
#define PV(T, j, RELOAD, NEXT_SO) { const unsigned pl = dppu<BCAST + 4 * (j)>(plo), ph = dppu<BCAST + 4 * (j)>(phi); \
      _Pragma("unroll") for (int d = 0; d < 4; ++d) { acc[2 * d] = dot2(V[T][j][d], pl, acc[2 * d]); acc[2 * d + 1] = dot2(V[T][j][d], ph, acc[2 * d + 1]); } \
      asm volatile("" : "+v"(acc[0]), "+v"(acc[1]), "+v"(acc[2]), "+v"(acc[3]), "+v"(acc[4]), "+v"(acc[5]), "+v"(acc[6]), "+v"(acc[7])); SB(); \
      if (RELOAD) { V[T][j] = __builtin_amdgcn_raw_buffer_load_b128(rv, voff, (NEXT_SO) + (j) * 1024, AUX_NT); SB(); } }

    int so = kvbase + NPRE * (NSW * 4096);
    TILE_BODY(0, true, so)
    TILE_BODY(1, false, 0)
    TILE_BODY(2, false, 0)
    TILE_BODY(3, false, 0)
    for (int it = NPRE; it < NTW; ++it) { so = it + 1 < NTW ? so + NSW * 4096 : 0x40000000; TILE_BODY(0, true, so) }
#undef TILE_BODY
#undef PV
    STAMPC(lane == 0, 16 + w);
    {
      const float Mw = xrow_max(m);
      const float f = __builtin_amdgcn_exp2f(m - Mw);
      float lr = l + dppf<ROR4>(l); lr += dppf<ROR8>(lr);
      const float Lw = xrow_sum(lr * f);
      float t[4];
#pragma unroll
      for (int j = 0; j < 4; ++j) t[j] = rs32(acc[j] * f, acc[j + 4] * f);
      const f32x2 o2 = {rs16(t[0], t[2]), rs16(t[1], t[3])};
      *(f32x2*)(lds + L_OP + w * 512 + (8 * c + 2 * r) * 4) = o2;
      if (lane == 0) *(f32x2*)(lds + L_ML + w * 8) = f32x2{Mw, Lw};
    }
    __builtin_amdgcn_fence(__ATOMIC_RELEASE, "workgroup");
    __builtin_amdgcn_s_barrier();
    return;
  }

  const int w = wv, htid = tid;
  const rsrc_t rx = mk_rsrc(x, NB * DM * 2);
  unsigned epoch_v, xres;
  {
    const rsrc_t rwq = mk_rsrc(wq, DM * NH * DH * 2), rws0 = mk_rsrc(ws, WS_END);
    unsigned xr[5];
#pragma unroll
    for (int j = 0; j < 5; ++j) xr[j] = __builtin_amdgcn_raw_buffer_load_b32(rx, lane * 4 + j * 256, b * DM * 2, 0);
    const unsigned xa = __builtin_amdgcn_raw_buffer_load_b16(rx, (8 * c + r) * 2, (b * DM + 128 * w) * 2, 0);
    const unsigned xb = __builtin_amdgcn_raw_buffer_load_b16(rx, (8 * c + 4 + r) * 2, (b * DM + 128 * w) * 2, 0);
    SB();
    v4u W[32];
#pragma unroll
    for (int n = 0; n < 32; ++n) W[n] = __builtin_amdgcn_raw_buffer_load_b128(rwq, r * 8192 + c * 16, (128 * w + 4 * n) * 8192 + head * 256, AUX_WQ);
    epoch_v = __builtin_amdgcn_raw_buffer_load_b32(rws0, 0, WS_EPOCH, AUX_SC1);
    xres = __builtin_amdgcn_raw_buffer_load_b32(rx, (lane & 7) * 4, bid * 32, 0);
    SB();
    __builtin_amdgcn_s_barrier();
    SB();
    unsigned xn_pair;
    {
      float s = 0.f;
#pragma unroll
      for (int j = 0; j < 5; ++j) s += lo_f(xr[j]) + hi_f(xr[j]);
      const float mean = wave_sum(s) * (1.0f / DM);
      float q2 = 0.f;
#pragma unroll
      for (int j = 0; j < 5; ++j) { const float d0 = lo_f(xr[j]) - mean, d1 = hi_f(xr[j]) - mean; q2 = fmaf(d0, d0, q2); q2 = fmaf(d1, d1, q2); }
      const float var = wave_sum(q2) * (1.0f / DM);
      const float mb = rbf(mean), den = rbf(sqrtf(rbf(rbf(var) + 1e-5f)));
      xn_pair = pk_bf16(rbf(lo_f(xa) - mb) / den, rbf(lo_f(xb) - mb) / den);
    }
    asm volatile("" : "+v"(xn_pair));
    SB();
    STAMPC(htid == 0, 8);
    float a[8];
#pragma unroll
    for (int e = 0; e < 8; ++e) a[e] = 0.f;
#define QSTEP(u) { const unsigned xp = dppu<BCAST + (u)>(xn_pair); \
      _Pragma("unroll") for (int d = 0; d < 4; ++d) { a[2 * d] = dot2(perm_lo(W[2 * (u)][d], W[2 * (u) + 1][d]), xp, a[2 * d]); a[2 * d + 1] = dot2(perm_hi(W[2 * (u)][d], W[2 * (u) + 1][d]), xp, a[2 * d + 1]); } SB(); }
    QSTEP(0) QSTEP(1) QSTEP(2) QSTEP(3) QSTEP(4) QSTEP(5) QSTEP(6) QSTEP(7)
    QSTEP(8) QSTEP(9) QSTEP(10) QSTEP(11) QSTEP(12) QSTEP(13) QSTEP(14) QSTEP(15)
#undef QSTEP
    float t[4];
#pragma unroll
    for (int j = 0; j < 4; ++j) t[j] = rs32(a[j], a[j + 4]);
    const f32x2 o2 = {rs16(t[0], t[2]), rs16(t[1], t[3])};
    *(f32x2*)(lds + L_QP + w * 512 + (8 * c + 2 * r) * 4) = o2;
    if (htid < 8) *(unsigned*)(lds + L_XR + htid * 4) = xres;
    STAMPC(lane == 0, 24 + w);
    asm volatile("s_waitcnt lgkmcnt(0)" ::: "memory");
    __builtin_amdgcn_s_barrier();
  }
  const unsigned tag0 = __builtin_amdgcn_readfirstlane(epoch_v) + 1u;

  const bool is_red = bid < NRED;
  const int rb = bid / 40, cb = bid % 40;
  int fi = -1;
  { const int s = bid - (NWG - 128); if (s >= 0) { const int k = (s & 7) + 8 * (3 - (s >> 5)), f = 4 * k + ((s >> 3) & 3); if (k < 27) fi = f; } }
  const bool is_ffn = fi >= 0;

  if (is_ffn) {
    FRESH_IDS();
    unsigned GU[32];
    const GAS unsigned char* gp = (const GAS unsigned char*)(c < 8 ? wg : wu) + (size_t)((128 * w + 8 * r) * (FF * 2) + fi * 32 + (c & 7) * 4);
#pragma unroll
    for (int n = 0; n < 32; ++n) GU[n] = AUX_FW ? __builtin_nontemporal_load((const GAS unsigned*)(gp + (32 * (n >> 3) + (n & 7)) * (FF * 2))) : *(const GAS unsigned*)(gp + (32 * (n >> 3) + (n & 7)) * (FF * 2));
    SB();
#pragma unroll
    for (int s = 0; s < 4; ++s) {
      v4u f0, f1;
#pragma unroll
      for (int d = 0; d < 4; ++d) { f0[d] = perm_lo(GU[8 * s + 2 * d], GU[8 * s + 2 * d + 1]); f1[d] = perm_hi(GU[8 * s + 2 * d], GU[8 * s + 2 * d + 1]); }
      *(v4u*)(lds + L_BG + (w * 8 + 2 * s) * 1024 + lane * 16) = f0;
      *(v4u*)(lds + L_BG + (w * 8 + 2 * s + 1) * 1024 + lane * 16) = f1;
    }
  }
  if (is_red) {
    FRESH_IDS();
    const rsrc_t rd = mk_rsrc(wd, DM * FF * 2);
    v4u D[12];
#pragma unroll
    for (int n = 0; n < 12; ++n) D[n] = __builtin_amdgcn_raw_buffer_load_b128(rd, (4 * (lane >> 1)) * (DM * 2) + (lane & 1) * 16, (128 * (w + 5 * (n >> 2)) + (n & 3)) * (DM * 2) + cb * 32, AUX_FW);
    SB();
#pragma unroll
    for (int u = 0; u < 6; ++u) {
      v4u f0, f1;
#pragma unroll
      for (int d = 0; d < 4; ++d) { f0[d] = perm_lo(D[2 * u][d], D[2 * u + 1][d]); f1[d] = perm_hi(D[2 * u][d], D[2 * u + 1][d]); }
      *(v4u*)(lds + L_WD + (w * 12 + 2 * u) * 1024 + lane * 16) = f0;
      *(v4u*)(lds + L_WD + (w * 12 + 2 * u + 1) * 1024 + lane * 16) = f1;
    }
  }
  STAMPC(w == 0 && fresh_lane() == 0, 9);

  int pass = DRY ? 0 : 1;
  asm volatile("" : "+s"(pass));
#pragma nounroll
  for (; pass < 2; ++pass) {
    const bool real = pass != 0;
    unsigned tag = tag0; asm volatile("" : "+s"(tag));
    const rsrc_t rws = mk_rsrc(ws, real ? WS_END : 0), rwo = mk_rsrc(wo, real ? DM * NH * DH * 2 : 0), rout = mk_rsrc(out, real ? NB * DM * 2 : 0);
#define BAR() do { asm volatile("s_waitcnt lgkmcnt(0)" ::: "memory"); if (real) __builtin_amdgcn_s_barrier(); asm volatile("" ::: "memory"); } while (0)
    {
      FRESH_IDS();
      v4u W[32];
      const int wo_voff = (2 * (lane & 3)) * (DM * 2) + (lane >> 2) * 16;
      const int wo_soff = head * DH * (DM * 2) + w * 256;
#pragma unroll
      for (int n = 0; n < 32; ++n) W[n] = __builtin_amdgcn_raw_buffer_load_b128(rwo, wo_voff, wo_soff + (8 * (n >> 1) + (n & 1)) * (DM * 2), AUX_WO);
      SB();
#pragma unroll
      for (int u = 0; u < 16; ++u)
#pragma unroll
        for (int d = 0; d < 4; ++d) { const unsigned a = W[2 * u][d], bb = W[2 * u + 1][d]; W[2 * u][d] = perm_lo(a, bb); W[2 * u + 1][d] = perm_hi(a, bb); }
#pragma unroll
      for (int n = 0; n < 32; ++n) asm volatile("" : "+v"(W[n]));
      STAMPC(w == 0 && fresh_lane() == 0 && real, 10);
      BAR();
      lane = fresh_lane();
      STAMPC(w == 0 && fresh_lane() == 0 && real, 2);
      f32x2 ml[NSW], op[NSW];
#pragma unroll
      for (int ww = 0; ww < NSW; ++ww) { ml[ww] = *(const f32x2*)(lds + L_ML + ww * 8); op[ww] = *(const f32x2*)(lds + L_OP + ww * 512 + lane * 8); }
      const float M = fmaxf(fmaxf(ml[0].x, ml[1].x), fmaxf(ml[2].x, ml[3].x));
      float L = 0.f; f32x2 o = {0.f, 0.f};
#pragma unroll
      for (int ww = 0; ww < NSW; ++ww) { const float fw = __builtin_amdgcn_exp2f(ml[ww].x - M); L = fmaf(fw, ml[ww].y, L); o += fw * op[ww]; }
      *(unsigned*)(lds + L_OB + w * 256 + ((lane & 3) * 16 + (lane >> 2)) * 4) = pk_bf16(o.x, o.y);
      WAVE_SYNC();
      v4u ob[4];
#pragma unroll
      for (int j = 0; j < 4; ++j) ob[j] = *(const v4u*)(lds + L_OB + w * 256 + (lane & 3) * 64 + j * 16);
      float y[8];
#pragma unroll
      for (int e = 0; e < 8; ++e) y[e] = 0.f;
#pragma unroll
      for (int u = 0; u < 16; ++u)
#pragma unroll
        for (int d = 0; d < 4; ++d) { y[2 * d] = dot2(W[2 * u][d], ob[u >> 2][u & 3], y[2 * d]); y[2 * d + 1] = dot2(W[2 * u + 1][d], ob[u >> 2][u & 3], y[2 * d + 1]); }
      float z4[4], z2[2];
#pragma unroll
      for (int j = 0; j < 4; ++j) { const float lo = y[j] + dppf<XOR2>(y[j]), hi = y[j + 4] + dppf<XOR2>(y[j + 4]); z4[j] = (lane & 2) ? hi : lo; }
#pragma unroll
      for (int j = 0; j < 2; ++j) { const float lo = z4[j] + dppf<XOR1>(z4[j]), hi = z4[j + 2] + dppf<XOR1>(z4[j + 2]); z2[j] = (lane & 1) ? hi : lo; }
      const int p = head * 2 + half;
      st128_sc1(v4u{as_u(z2[0]), tag, as_u(z2[1]), tag}, rws, (lane >> 3) * 8192 + (lane & 7) * 16, WS_Y + ((b * 40 + 8 * w) * 64 + p) * 128);
      if (w == 0 && lane == 0) st128_sc1(v4u{as_u(M), tag, as_u(L), tag}, rws, 0, WS_ML + (b * 64 + p) * 16);
    }
    STAMPC(w == 0 && fresh_lane() == 0 && real, 3);

    float h0 = 0.f, h1 = 0.f;
    if (is_red) {
      FRESH_IDS();
      if (w < 4) {
        const int hp = htid >> 3, cp = htid & 7;
        const int yo = (2 * hp) * 128 + cp * 16, ys = WS_Y + (rb * 40 + cb) * 8192, mo = 2 * hp * 16, ms = WS_ML + rb * 1024;
        v4u y0, y1, m0, m1;
        for (unsigned spin = 0;; ++spin) {
          y0 = __builtin_amdgcn_raw_buffer_load_b128(rws, yo, ys, AUX_SC1);
          y1 = __builtin_amdgcn_raw_buffer_load_b128(rws, yo + 128, ys, AUX_SC1);
          m0 = __builtin_amdgcn_raw_buffer_load_b128(rws, mo, ms, AUX_SC1);
          m1 = __builtin_amdgcn_raw_buffer_load_b128(rws, mo + 16, ms, AUX_SC1);
          SB();
          const unsigned bad = TAGBAD(y0) | TAGBAD(y1) | TAGBAD(m0) | TAGBAD(m1);
          if (__all(bad == 0) || !real || spin > SPIN_CAP) break;
          asm volatile("" ::: "memory");
        }
        STAMPC(w == 0 && fresh_lane() == 0 && real, 12);
        const float M = fmaxf(as_f(m0[0]), as_f(m1[0]));
        const float w0 = __builtin_amdgcn_exp2f(as_f(m0[0]) - M), w1 = __builtin_amdgcn_exp2f(as_f(m1[0]) - M);
        const float inv = __builtin_amdgcn_rcpf(fmaf(w0, as_f(m0[2]), w1 * as_f(m1[2])));
        float v0 = fmaf(w0, as_f(y0[0]), w1 * as_f(y1[0])) * inv, v1 = fmaf(w0, as_f(y0[2]), w1 * as_f(y1[2])) * inv;
        v0 += dppf<ROR8>(v0); v1 += dppf<ROR8>(v1);
        v0 = xrow_sum(v0); v1 = xrow_sum(v1);
        if (lane < 8) *(f32x2*)(lds + L_RED + w * 64 + lane * 8) = f32x2{v0, v1};
      }
      BAR();
      if (htid < 8) {
        f32x2 s = *(const f32x2*)(lds + L_RED + htid * 8);
#pragma unroll
        for (int ww = 1; ww < 4; ++ww) s += *(const f32x2*)(lds + L_RED + ww * 64 + htid * 8);
        const unsigned xres = *(const unsigned*)(lds + L_XR + htid * 4);
        const unsigned hp2 = pk_bf16(lo_f(xres) + rbf(s.x), hi_f(xres) + rbf(s.y));
        h0 = lo_f(hp2); h1 = hi_f(hp2);
        __builtin_amdgcn_raw_buffer_store_b64(v2u{hp2, tag}, rws, htid * 8, WS_H + (rb * 320 + cb * 8) * 8, AUX_SC1);
      }
    }
    STAMPC(w == 0 && fresh_lane() == 0 && real, 4);

    if (is_ffn) {
      FRESH_IDS();
      v4u BG[8];
#pragma unroll
      for (int n = 0; n < 8; ++n) BG[n] = *(const v4u*)(lds + L_BG + (w * 8 + n) * 1024 + lane * 16);
      if (w < 4) {
        v4u g0, g1, g2;
        const int hs = WS_H + w * 2560;
        for (unsigned spin = 0;; ++spin) {
          g0 = __builtin_amdgcn_raw_buffer_load_b128(rws, lane * 16, hs, AUX_SC1);
          g1 = __builtin_amdgcn_raw_buffer_load_b128(rws, lane * 16 + 1024, hs, AUX_SC1);
          g2 = __builtin_amdgcn_raw_buffer_load_b128(rws, (lane & 31) * 16 + 2048, hs, AUX_SC1);
          SB();
          const unsigned bad = TAGBAD(g0) | TAGBAD(g1) | TAGBAD(g2);
          if (__all(bad == 0) || !real || spin > SPIN_CAP) break;
          asm volatile("" ::: "memory");
        }
        STAMPC(w == 0 && fresh_lane() == 0 && real, 13);
        const unsigned hv[6] = {g0[0], g0[2], g1[0], g1[2], g2[0], g2[2]};
        const float msk = lane < 32 ? 1.f : 0.f;
        float s = 0.f, s3 = 0.f;
#pragma unroll
        for (int j = 0; j < 4; ++j) s += lo_f(hv[j]) + hi_f(hv[j]);
#pragma unroll
        for (int j = 4; j < 6; ++j) s3 += lo_f(hv[j]) + hi_f(hv[j]);
        const float mean = wave_sum(fmaf(s3, msk, s)) * (1.0f / DM);
        float q2 = 0.f, q3 = 0.f;
#pragma unroll
        for (int j = 0; j < 4; ++j) { const float d0 = lo_f(hv[j]) - mean, d1 = hi_f(hv[j]) - mean; q2 = fmaf(d0, d0, q2); q2 = fmaf(d1, d1, q2); }
#pragma unroll
        for (int j = 4; j < 6; ++j) { const float d0 = lo_f(hv[j]) - mean, d1 = hi_f(hv[j]) - mean; q3 = fmaf(d0, d0, q3); q3 = fmaf(d1, d1, q3); }
        const float var = wave_sum(fmaf(q3, msk, q2)) * (1.0f / DM);
        const float mb = rbf(mean), den = rbf(sqrtf(rbf(rbf(var) + 1e-5f)));
        unsigned hn[6];
#pragma unroll
        for (int j = 0; j < 6; ++j) hn[j] = pk_bf16(rbf(lo_f(hv[j]) - mb) / den, rbf(hi_f(hv[j]) - mb) / den);
        unsigned char* hrow = lds + L_HN + w * HN_STRIDE;
        *(v2u*)(hrow + lane * 8) = v2u{hn[0], hn[1]};
        *(v2u*)(hrow + 512 + lane * 8) = v2u{hn[2], hn[3]};
        if (lane < 32) *(v2u*)(hrow + 1024 + lane * 8) = v2u{hn[4], hn[5]};
      }
      BAR();
      STAMPC(w == 0 && fresh_lane() == 0 && real, 5);
      f32x4 d0 = {0.f, 0.f, 0.f, 0.f}, d1 = {0.f, 0.f, 0.f, 0.f};
#pragma unroll
      for (int s = 0; s < 4; ++s) {
        const v4u a = *(const v4u*)(lds + L_HN + (c >> 2) * HN_STRIDE + (128 * w + 32 * s + 8 * r) * 2);
        d0 = __builtin_amdgcn_mfma_f32_16x16x32_bf16(__builtin_bit_cast(bf16x8, a), __builtin_bit_cast(bf16x8, BG[2 * s]), d0, 0, 0, 0);
        d1 = __builtin_amdgcn_mfma_f32_16x16x32_bf16(__builtin_bit_cast(bf16x8, a), __builtin_bit_cast(bf16x8, BG[2 * s + 1]), d1, 0, 0, 0);
      }
      *(f32x2*)(lds + L_GP + w * 512 + (r * 32 + 2 * c) * 4) = f32x2{d0[0], d1[0]};
      BAR();
      if (w == 0) {
        float G = 0.f, U = 0.f;
        const int o = (r * 32 + c) * 4;
#pragma unroll
        for (int ww = 0; ww < NHW; ++ww) { G += *(const float*)(lds + L_GP + ww * 512 + o); U += *(const float*)(lds + L_GP + ww * 512 + o + 64); }
        G = rbf(G); U = rbf(U);
        const float u2 = 2.0f * 0.7978845608028654f * LOG2E * (G + 0.044715f * G * G * G);
        const float ge = rbf(G * __builtin_amdgcn_rcpf(1.0f + __builtin_amdgcn_exp2f(-u2)));
        const float av = ge * U;
        const unsigned ap = pk_bf16(av, dppf<XOR1>(av));
        __builtin_amdgcn_raw_buffer_store_b64(v2u{ap, tag}, rws, (c & 1) ? 0x40000000 : r * 6912 + (c >> 1) * 8, WS_A + fi * 64, AUX_SC1);
      }
    }
    STAMPC(w == 0 && fresh_lane() == 0 && real, 6);

    if (is_red) {
      FRESH_IDS();
      v4u D[12];
#pragma unroll
      for (int n = 0; n < 12; ++n) D[n] = *(const v4u*)(lds + L_WD + (w * 12 + n) * 1024 + lane * 16);
      v4u g[3];
      const int as = WS_A + rb * 6912;
      int ao[3];
#pragma unroll
      for (int k = 0; k < 3; ++k) { const int o = (w + 5 * k) * 512 + (lane >> 1) * 16; ao[k] = o < 6912 ? o : 0; }
      for (unsigned spin = 0;; ++spin) {
#pragma unroll
        for (int k = 0; k < 3; ++k) g[k] = __builtin_amdgcn_raw_buffer_load_b128(rws, ao[k], as, AUX_SC1);
        SB();
        const unsigned bad = TAGBAD(g[0]) | TAGBAD(g[1]) | TAGBAD(g[2]);
        if (__all(bad == 0) || !real || spin > SPIN_CAP) break;
        asm volatile("" ::: "memory");
      }
      STAMPC(w == 0 && fresh_lane() == 0 && real, 14);
      float y[8];
#pragma unroll
      for (int e = 0; e < 8; ++e) y[e] = 0.f;
#pragma unroll
      for (int u = 0; u < 6; ++u)
#pragma unroll
        for (int d = 0; d < 4; ++d) { y[2 * d] = dot2(D[2 * u][d], g[u >> 1][2 * (u & 1)], y[2 * d]); y[2 * d + 1] = dot2(D[2 * u + 1][d], g[u >> 1][2 * (u & 1)], y[2 * d + 1]); }
      float t[4];
#pragma unroll
      for (int j = 0; j < 4; ++j) t[j] = rs32(y[j], y[j + 4]);
      float v0 = rs16(t[0], t[2]), v1 = rs16(t[1], t[3]);
      v0 += dppf<ROR8>(v0); v1 += dppf<ROR8>(v1);
      v0 += dppf<ROR4>(v0); v1 += dppf<ROR4>(v1);
      v0 += dppf<ROR2>(v0); v1 += dppf<ROR2>(v1);
      if (c < 2) *(f32x2*)(lds + L_RED2 + w * 64 + (4 * c + r) * 8) = f32x2{v0, v1};
      BAR();
      if (htid < 8) {
        f32x2 s = *(const f32x2*)(lds + L_RED2 + htid * 8);
#pragma unroll
        for (int ww = 1; ww < NHW; ++ww) s += *(const f32x2*)(lds + L_RED2 + ww * 64 + htid * 8);
        __builtin_amdgcn_raw_buffer_store_b32(pk_bf16(h0 + rbf(s.x), h1 + rbf(s.y)), rout, htid * 4, bid * 32, 0);
        if (bid == 0 && htid == 0) __builtin_amdgcn_raw_buffer_store_b32(tag, rws, 0, WS_EPOCH, AUX_SC1);
      }
    }
    STAMPC(w == 0 && fresh_lane() == 0, real ? 7 : 11);
  }
}

extern "C" void kernel_launch(void* const* d_in, const int* in_sizes, int n_in, void* d_out, int out_size, void* d_ws, size_t ws_size, hipStream_t stream) {
  fused_block<<<NWG, NT, 0, stream>>>((const u16*)d_in[0], (const u16*)d_in[1], (const u16*)d_in[2], (const u16*)d_in[3], (const u16*)d_in[4],
                                      (const u16*)d_in[6], (const u16*)d_in[7], (const u16*)d_in[8], (u16*)d_out, d_ws);
}
```

```cpp
#define LAGN 2
#define LAGF 2
#define SKEW 1
#include <hip/hip_runtime.h>
#include <hip/hip_bf16.h>
#include <stdint.h>

typedef unsigned short u16;
typedef unsigned v4u __attribute__((ext_vector_type(4)));
typedef unsigned v2u __attribute__((ext_vector_type(2)));
typedef float f32x2 __attribute__((ext_vector_type(2)));
typedef float f32x4 __attribute__((ext_vector_type(4)));
typedef __bf16 bf16x2 __attribute__((ext_vector_type(2)));
typedef __bf16 bf16x8 __attribute__((ext_vector_type(8)));
typedef __amdgpu_buffer_rsrc_t rsrc_t;
#define GAS __attribute__((address_space(1)))
#define LAS __attribute__((address_space(3)))

#define PW 2
#define PG 1
#define GU_FIRST 0
#define AUX_X 0
#define DPRE 8
#define QDELAY 0
#define AUX_QP 16
#define AUX_EP 16
#define ABL 0
#define PRE0 4
constexpr int NPRE = 4;

constexpr int NB = 4, DM = 640, NH = 32, SK = 4096, DH = 128, FF = 1728;
constexpr int NSW = 4, NHW = 5, NT = 64 * (NSW + NHW), NWG = 256;
constexpr int NRED = 160, NFFN = 54;
constexpr int WS_EPOCH = 0;
constexpr int WS_ML = 4096;
constexpr int WS_H = 8192;
constexpr int WS_Q = 65536;
constexpr int WS_Y = WS_Q + NB * NH * 4096;
constexpr int WS_Z = WS_Y + NB * 40 * 64 * 128;
constexpr int WS_END = WS_Z + NB * 40 * NFFN * 128;
constexpr int WS_STAMP = WS_END;
constexpr int AUX_NT = 2, AUX_SC1 = 16;
constexpr unsigned SPIN_CAP = 1u << 18;
constexpr float LOG2E = 1.4426950408889634f;

#define STAMPC(cond, i) do { } while (0)

__device__ __forceinline__ rsrc_t mk_rsrc(const void* p, int bytes) { return __builtin_amdgcn_make_buffer_rsrc((void*)p, 0, bytes, 0x00020000); }
__device__ __forceinline__ float as_f(unsigned u) { return __builtin_bit_cast(float, u); }
__device__ __forceinline__ unsigned as_u(float f) { return __builtin_bit_cast(unsigned, f); }
__device__ __forceinline__ unsigned pk_bf16(float lo, float hi) { f32x2 v = {lo, hi}; return __builtin_bit_cast(unsigned, __builtin_convertvector(v, bf16x2)); }
__device__ __forceinline__ float rbf(float x) { return as_f(pk_bf16(0.f, x)); }
__device__ __forceinline__ float lo_f(unsigned u) { return as_f(u << 16); }
__device__ __forceinline__ float hi_f(unsigned u) { return as_f(u & 0xffff0000u); }
__device__ __forceinline__ float dot2(unsigned a, unsigned b, float c) { return __builtin_amdgcn_fdot2_f32_bf16(__builtin_bit_cast(bf16x2, a), __builtin_bit_cast(bf16x2, b), c, false); }
__device__ __forceinline__ f32x4 mfma(v4u a, v4u b, f32x4 c) { return __builtin_amdgcn_mfma_f32_16x16x32_bf16(__builtin_bit_cast(bf16x8, a), __builtin_bit_cast(bf16x8, b), c, 0, 0, 0); }
template <int CTRL> __device__ __forceinline__ unsigned dppu(unsigned x) { return __builtin_amdgcn_mov_dpp((int)x, CTRL, 0xf, 0xf, true); }
template <int CTRL> __device__ __forceinline__ float dppf(float x) { return as_f(dppu<CTRL>(as_u(x))); }
constexpr int ROR1 = 0x121, ROR2 = 0x122, ROR4 = 0x124, ROR8 = 0x128, XOR1 = 0xB1, XOR2 = 0x4E, XOR7 = 0x141, BCAST = 0x150;
__device__ __forceinline__ unsigned perm_lo(unsigned a, unsigned b) { return __builtin_amdgcn_perm(b, a, 0x05040100u); }
__device__ __forceinline__ unsigned perm_hi(unsigned a, unsigned b) { return __builtin_amdgcn_perm(b, a, 0x07060302u); }
__device__ __forceinline__ float rs16(float a, float b) { auto s = __builtin_amdgcn_permlane16_swap(as_u(a), as_u(b), false, false); return as_f(s[0]) + as_f(s[1]); }
__device__ __forceinline__ float rs32(float a, float b) { auto s = __builtin_amdgcn_permlane32_swap(as_u(a), as_u(b), false, false); return as_f(s[0]) + as_f(s[1]); }
__device__ __forceinline__ float xrow_sum(float x) { x = rs16(x, x); return rs32(x, x); }
__device__ __forceinline__ float xrow_max(float x) {
  auto s = __builtin_amdgcn_permlane16_swap(as_u(x), as_u(x), false, false); x = fmaxf(as_f(s[0]), as_f(s[1]));
  auto t = __builtin_amdgcn_permlane32_swap(as_u(x), as_u(x), false, false); return fmaxf(as_f(t[0]), as_f(t[1]));
}
__device__ __forceinline__ void st128_sc1(v4u v, rsrc_t rs, int voff, int soff) {
  __builtin_amdgcn_raw_buffer_store_b128(v, rs, voff, soff, AUX_SC1);
  asm volatile("s_nop 1" :: "v"(v) : "memory");
}
__device__ __forceinline__ int fresh_lane() { int l; asm volatile("v_mbcnt_lo_u32_b32 %0, -1, 0\n\tv_mbcnt_hi_u32_b32 %0, -1, %0" : "=v"(l)); return l; }
__device__ __forceinline__ unsigned ln_pair(unsigned v, float mb, float rden) { return pk_bf16(rbf(lo_f(v) - mb) * rden, rbf(hi_f(v) - mb) * rden); }
#define SB() __builtin_amdgcn_sched_barrier(0)
#define VM0() do { SB(); asm volatile("s_waitcnt vmcnt(0)" ::: "memory"); SB(); } while (0)
#define BAR() do { asm volatile("s_waitcnt lgkmcnt(0)" ::: "memory"); __builtin_amdgcn_s_barrier(); asm volatile("" ::: "memory"); } while (0)
#define TAGBAD(g) (((g)[1] ^ tag) | ((g)[3] ^ tag))
#define FRESH_IDS() const int lane = fresh_lane(); const int r = lane >> 4, c = lane & 15, htid = w * 64 + lane; (void)r; (void)c; (void)htid

constexpr int L_FLAG = 0;
constexpr int L_XR = 16;
constexpr int L_ML = 64;
constexpr int L_Q = 128;
constexpr int L_RED = 384;
constexpr int L_RED2 = L_RED + 320;
constexpr int OP_S = 320;
constexpr int L_OP = L_RED2 + 320;
constexpr int GP_S = 384;
constexpr int L_GP = L_OP + 4 * OP_S;
constexpr int HN_S = 1344;
constexpr int L_HN = L_GP + 5 * 4 * GP_S;
constexpr int L_BG = L_HN + 4 * HN_S;
constexpr int L_WD = L_BG + 640 * 128;
constexpr int L_END = L_WD + 5 * 32 * 256;
static_assert(L_BG % 16 == 0 && L_HN % 16 == 0 && L_OP % 16 == 0 && L_GP % 16 == 0, "align");

__device__ __forceinline__ int rank_of(int bid) { return (bid & 7) + 8 * (bid >> 4); }
__device__ __forceinline__ int ffn_of_rank(int j) { const int s = j - 32; if (s < 0) return -1; const int k = (s & 7) + 8 * (s >> 4); return k < 27 ? 2 * k + ((s >> 3) & 1) : -1; }

__global__ void __launch_bounds__(NT, 3) fused_block(const u16* __restrict__ x, const u16* __restrict__ kc, const u16* __restrict__ vc,
                                                     const u16* __restrict__ wq, const u16* __restrict__ wo, const u16* __restrict__ wg,
                                                     const u16* __restrict__ wu, const u16* __restrict__ wd, u16* __restrict__ out, void* ws) {
  __shared__ __attribute__((aligned(16))) unsigned char lds[L_END + 256];
  const int wv = __builtin_amdgcn_readfirstlane(threadIdx.x >> 6);
  const int bid = blockIdx.x, xs = bid & 7, rr = bid >> 3;
  const int head = xs + 8 * (rr >> 3), b = (rr >> 1) & 3, half = rr & 1;
  STAMPC(wv == 0, 0);

  if (wv >= NHW) {
    const int w = wv - NHW;
    const int lane = threadIdx.x & 63, r = lane >> 4, c = lane & 15;
    const rsrc_t rk = mk_rsrc(kc, NB * NH * SK * DH * 2), rv = mk_rsrc(vc, NB * NH * SK * DH * 2);
    const int n0 = 32 + (ffn_of_rank(rank_of(bid)) >= 0 ? SKEW : 0);
    const int nr = half ? 64 - n0 : n0;
    const int kvbase = (b * NH + head) * (SK * DH * 2) + (half ? n0 * (NSW * 4096) : 0) + w * 4096;
    const int voff = lane * 16;
    STAMPC(w == 0, 13);
    if (PRE0 == 0) __builtin_amdgcn_s_barrier();
    STAMPC(w == 0, 31);
    v4u B[NPRE][8];
#define LD(i, so) (((i) & 4) ? __builtin_amdgcn_raw_buffer_load_b128(rv, voff, (so) + ((i) & 3) * 1024, AUX_NT) : __builtin_amdgcn_raw_buffer_load_b128(rk, voff, (so) + ((i) & 3) * 1024, AUX_NT))
#pragma unroll
    for (int t = 0; t < NPRE; ++t)
#pragma unroll
      for (int i = 0; i < 8; ++i) { B[t][i] = LD(i, kvbase + t * (NSW * 4096)); if (PRE0 && 8 * t + i + 1 == PRE0) { SB(); __builtin_amdgcn_s_barrier(); SB(); } if (DPRE && 8 * t + i + 1 >= DPRE) { SB(); asm volatile("s_waitcnt vmcnt(%0)" :: "i"(DPRE - 1) : "memory"); SB(); } if (i == 7 && t < 3) STAMPC(w == 0, 28 + t); }
    SB();
    STAMPC(w == 0, 11);
    {
      unsigned spin = 0;
      while (*(volatile LAS unsigned*)(LAS unsigned char*)(lds + L_FLAG + 4) == 0u && ++spin < (SPIN_CAP << 4)) __builtin_amdgcn_s_sleep(1);
      asm volatile("" ::: "memory");
    }
    const v4u q = *(const v4u*)(lds + L_Q + c * 16);
    STAMPC(w == 0, 1);
    float m = -INFINITY, l = 0.f, acc[8];
#pragma unroll
    for (int e = 0; e < 8; ++e) acc[e] = 0.f;

    int thr = (half && ffn_of_rank(rank_of(bid)) >= 0) ? LAGF : LAGN;
#define THROTTLE() asm volatile("s_cmp_lt_u32 %0, 1\n\ts_cbranch_scc1 1f\n\ts_waitcnt vmcnt(6)\n\ts_cmp_lt_u32 %0, 2\n\ts_cbranch_scc1 1f\n\ts_waitcnt vmcnt(5)\n1:" :: "s"(thr) : "scc", "memory")
#define REFILL(i, DO_CUR, CUR, DO_NXT, NXT) if (DO_NXT) { if (DO_CUR) THROTTLE(); B[0][i] = LD(i, NXT); SB(); }
#define QK(T, j, DO_CUR, CUR, DO_NXT, NXT) { \
      float d = dot2(B[T][j][0], q[0], 0.f); d = dot2(B[T][j][1], q[1], d); d = dot2(B[T][j][2], q[2], d); d = dot2(B[T][j][3], q[3], d); \
      asm volatile("" : "+v"(d)); sx[j] = d; SB(); REFILL(j, DO_CUR, CUR, DO_NXT, NXT) }
#define PV(T, j, DO_CUR, CUR, DO_NXT, NXT) { const unsigned pl = dppu<BCAST + 4 * (j)>(plo), ph = dppu<BCAST + 4 * (j)>(phi); \
      _Pragma("unroll") for (int d = 0; d < 4; ++d) { acc[2 * d] = dot2(B[T][4 + j][d], pl, acc[2 * d]); acc[2 * d + 1] = dot2(B[T][4 + j][d], ph, acc[2 * d + 1]); } \
      asm volatile("" : "+v"(acc[0]), "+v"(acc[1]), "+v"(acc[2]), "+v"(acc[3]), "+v"(acc[4]), "+v"(acc[5]), "+v"(acc[6]), "+v"(acc[7])); SB(); \
      REFILL(4 + j, DO_CUR, CUR, DO_NXT, NXT) }
#define TILE_BODY(T, DO_CUR, CUR, DO_NXT, NXT) { \
    float sx[4]; \
    QK(T, 0, DO_CUR, CUR, DO_NXT, NXT) QK(T, 1, DO_CUR, CUR, DO_NXT, NXT) QK(T, 2, DO_CUR, CUR, DO_NXT, NXT) QK(T, 3, DO_CUR, CUR, DO_NXT, NXT) \
    const float a0 = sx[0] + dppf<ROR8>(sx[0]), a2 = sx[2] + dppf<ROR8>(sx[2]), a1 = sx[1] + dppf<ROR8>(sx[1]), a3 = sx[3] + dppf<ROR8>(sx[3]); \
    const float b0 = (c & 8) ? a2 : a0, b1 = (c & 8) ? a3 : a1; \
    const float c0 = b0 + dppf<XOR7>(b0), c1 = b1 + dppf<XOR7>(b1); \
    float s = (c & 4) ? c1 : c0; \
    s += dppf<XOR2>(s); s += dppf<XOR1>(s); \
    s = rbf(rbf(s) * 0.08838834764831845f) * LOG2E; \
    float pm = fmaxf(s, dppf<ROR4>(s)); pm = fmaxf(pm, dppf<ROR8>(pm)); \
    const float mn = fmaxf(m, pm); \
    const float alpha = __builtin_amdgcn_exp2f(m - mn), p = __builtin_amdgcn_exp2f(s - mn); \
    l = fmaf(l, alpha, p); m = mn; \
    _Pragma("unroll") for (int e = 0; e < 8; ++e) acc[e] *= alpha; \
    const unsigned plo = pk_bf16(p, 0.f), phi = pk_bf16(0.f, p); \
    PV(T, 0, DO_CUR, CUR, DO_NXT, NXT) PV(T, 1, DO_CUR, CUR, DO_NXT, NXT) PV(T, 2, DO_CUR, CUR, DO_NXT, NXT) PV(T, 3, DO_CUR, CUR, DO_NXT, NXT) }

    int so = kvbase + NPRE * (NSW * 4096);
    TILE_BODY(0, false, 0, true, so)
    TILE_BODY(1, false, 0, false, 0)
    TILE_BODY(2, false, 0, false, 0)
    TILE_BODY(3, false, 0, false, 0)
    for (int it = NPRE; it < nr; ++it) {
      const bool more = it + 1 < nr;
      so = more ? so + NSW * 4096 : 0x40000000;
      thr = more ? thr : 0;
      TILE_BODY(0, true, 0, true, so)
      STAMPC(w == 0 && (it & 7) == 7, 20 + (it >> 3));
    }
    STAMPC(true, 16 + w);
    {
      const float Mw = xrow_max(m);
      const float f = __builtin_amdgcn_exp2f(m - Mw);
      float lr = l + dppf<ROR4>(l); lr += dppf<ROR8>(lr);
      float t[4];
#pragma unroll
      for (int j = 0; j < 4; ++j) t[j] = rs32(acc[j] * f, acc[j + 4] * f);
      *(unsigned*)(lds + L_OP + w * OP_S + (4 * c + r) * 4) = pk_bf16(rs16(t[0], t[2]), rs16(t[1], t[3]));
      const float Lw = xrow_sum(lr * f);
      if (lane == 0) *(f32x2*)(lds + L_ML + w * 8) = f32x2{Mw, Lw};
    }
    BAR();
    return;
  }

  const int w = wv;
  const int ri = half * 128 + rank_of(bid);
  __builtin_amdgcn_s_setprio(2);
  const rsrc_t rws = mk_rsrc(ws, WS_END);
  unsigned tag;
  {
    FRESH_IDS();
    const rsrc_t rx = mk_rsrc(x, ABL & 1 ? 0 : NB * DM * 2), rwq = mk_rsrc(wq, ABL & 2 ? 0 : DM * NH * DH * 2);
    v4u xs5[5];
#pragma unroll
    for (int n = 0; n < 5; ++n) xs5[n] = __builtin_amdgcn_raw_buffer_load_b128(rx, (c >> 2) * (DM * 2) + (4 * r + (c & 3)) * 16, n * 256, AUX_X);
    const v4u xa = __builtin_amdgcn_raw_buffer_load_b128(rx, (c >> 2) * (DM * 2) + r * 16, (160 * b + 32 * w) * 2, AUX_X);
    v2u Wq[8];
#pragma unroll
    for (int e = 0; e < 8; ++e) Wq[e] = __builtin_amdgcn_raw_buffer_load_b64(rwq, (8 * r) * 8192 + c * 8, (160 * b + 32 * w + e) * 8192 + head * 256 + half * 128, AUX_NT);
    const unsigned epoch_v = __builtin_amdgcn_raw_buffer_load_b32(rws, ABL & 8 ? 0x40000000 : 0, WS_EPOCH, AUX_EP);
    const unsigned xres = __builtin_amdgcn_raw_buffer_load_b32(rx, (lane & 7) * 4, ri * 32, AUX_X);
    if (htid < 2) *(unsigned*)(lds + L_FLAG + htid * 4) = 0u;
    SB();
    BAR();
    SB();
    STAMPC(w == 0, 27);
    const float piv = dppf<0x00>(xrow_max(r == 0 ? lo_f(xs5[0][0]) : -INFINITY));
    float s1 = 0.f, s2 = 0.f;
#pragma unroll
    for (int n = 0; n < 5; ++n)
#pragma unroll
      for (int d = 0; d < 4; ++d) { const float d0 = lo_f(xs5[n][d]) - piv, d1 = hi_f(xs5[n][d]) - piv; s1 += d0 + d1; s2 = fmaf(d0, d0, s2); s2 = fmaf(d1, d1, s2); }
    s1 += dppf<XOR1>(s1); s2 += dppf<XOR1>(s2); s1 += dppf<XOR2>(s1); s2 += dppf<XOR2>(s2);
    s1 = xrow_sum(s1) * (1.0f / DM); s2 = xrow_sum(s2) * (1.0f / DM);
    const float var = fmaxf(fmaf(-s1, s1, s2), 0.f);
    const float mb = rbf(piv + s1), rden = __builtin_amdgcn_rcpf(rbf(__builtin_amdgcn_sqrtf(rbf(rbf(var) + 1e-5f))));
    const v4u af = {ln_pair(xa[0], mb, rden), ln_pair(xa[1], mb, rden), ln_pair(xa[2], mb, rden), ln_pair(xa[3], mb, rden)};
    STAMPC(w == 0, 8);
    const f32x4 zero = {0.f, 0.f, 0.f, 0.f};
    f32x4 dq[4];
#pragma unroll
    for (int f = 0; f < 4; ++f) {
      v4u bf;
#pragma unroll
      for (int d = 0; d < 4; ++d) bf[d] = (f & 1) ? perm_hi(Wq[2 * d][f >> 1], Wq[2 * d + 1][f >> 1]) : perm_lo(Wq[2 * d][f >> 1], Wq[2 * d + 1][f >> 1]);
      dq[f] = mfma(af, bf, zero);
    }
    *(f32x4*)(lds + L_GP + w * 1024 + lane * 16) = f32x4{dq[0][0], dq[1][0], dq[2][0], dq[3][0]};
    if (htid < 8) *(unsigned*)(lds + L_XR + htid * 4) = xres;
    tag = __builtin_amdgcn_readfirstlane(epoch_v) + 1u;
    asm volatile("s_waitcnt lgkmcnt(0)" ::: "memory");
    if (lane == 0) __hip_atomic_fetch_add((LAS unsigned*)(LAS unsigned char*)(lds + L_FLAG), 1u, __ATOMIC_RELAXED, __HIP_MEMORY_SCOPE_WORKGROUP);
    STAMPC(w == 0, 24);
    if (w < 2 && !(ABL & 4)) {
      unsigned spin = 0;
      while (*(volatile LAS unsigned*)(LAS unsigned char*)(lds + L_FLAG) < (unsigned)NHW && ++spin < (SPIN_CAP << 4)) { }
      asm volatile("" ::: "memory");
      f32x2 s = {0.f, 0.f};
#pragma unroll
      for (int ww = 0; ww < NHW; ++ww) s += *(const f32x2*)(lds + L_GP + ww * 1024 + htid * 8);
      st128_sc1(v4u{as_u(s.x), tag, as_u(s.y), tag}, rws, (htid >> 5) * (NH * 4096) + (htid & 31) * 16, WS_Q + head * 4096 + b * 1024 + half * 512);
      STAMPC(w == 0, 25);
    }
    if (w == 2) {
      v4u g[4];
      if (QDELAY) { unsigned spin = 0; while (*(volatile LAS unsigned*)(LAS unsigned char*)(lds + L_FLAG) < (unsigned)NHW && ++spin < (SPIN_CAP << 4)) { }
        __builtin_amdgcn_s_sleep(QDELAY); }
      if (ABL & 4) { for (int k = 0; k < 4; ++k) g[k] = v4u{0, 0, 0, 0}; __builtin_amdgcn_s_sleep(100); } else
      for (unsigned spin = 0;; ++spin) {
#pragma unroll
        for (int k = 0; k < 4; ++k) g[k] = __builtin_amdgcn_raw_buffer_load_b128(rws, lane * 16, WS_Q + (b * NH + head) * 4096 + k * 1024, AUX_QP);
        SB();
        const unsigned bad = TAGBAD(g[0]) | TAGBAD(g[1]) | TAGBAD(g[2]) | TAGBAD(g[3]);
        if (__all(bad == 0) || spin > SPIN_CAP) break;
        asm volatile("" ::: "memory");
      }
      *(unsigned*)(lds + L_Q + lane * 4) = pk_bf16((as_f(g[0][0]) + as_f(g[1][0])) + (as_f(g[2][0]) + as_f(g[3][0])), (as_f(g[0][2]) + as_f(g[1][2])) + (as_f(g[2][2]) + as_f(g[3][2])));
      asm volatile("s_waitcnt lgkmcnt(0)" ::: "memory");
      *(volatile LAS unsigned*)(LAS unsigned char*)(lds + L_FLAG + 4) = 1u;
      STAMPC(true, 26);
    } else {
      unsigned spin = 0;
      while (*(volatile LAS unsigned*)(LAS unsigned char*)(lds + L_FLAG + 4) == 0u && ++spin < (SPIN_CAP << 4)) __builtin_amdgcn_s_sleep(2);
      asm volatile("" ::: "memory");
    }
  }

  const bool is_red = ri < NRED;
  const int rb = ri / 40, cb = ri % 40;
  const int fi = half ? ffn_of_rank(rank_of(bid)) : -1;
  const bool is_ffn = fi >= 0;

#define STAGE_GU() if (is_ffn) { \
    const int ln = fresh_lane(); \
    const GAS unsigned char* gp = (const GAS unsigned char*)((ln & 4) ? wu : wg) + (size_t)((128 * w + (ln >> 3)) * (FF * 2) + fi * 64 + (ln & 3) * 16); \
    _Pragma("nounroll") for (int s = 0; s < 16 / PG; ++s) { \
      v4u GU[PG]; \
      _Pragma("unroll") for (int n = 0; n < PG; ++n) GU[n] = __builtin_nontemporal_load((const GAS v4u*)(gp + 8 * n * (FF * 2))); \
      gp += 8 * PG * (FF * 2); \
      _Pragma("unroll") for (int n = 0; n < PG; ++n) *(v4u*)(lds + L_BG + (w * 16 + s * PG + n) * 1024 + ln * 16) = GU[n]; \
    } }
  {
    v4u F[32];
    {
      FRESH_IDS();
      const rsrc_t rwo = mk_rsrc(wo, DM * NH * DH * 2);
      const int vo = (8 * r) * (DM * 2) + c * 16, so = head * DH * (DM * 2) + w * 256;
#pragma unroll
      for (int s = 0; s < 4; ++s) {
        v4u T[8];
#pragma unroll
        for (int e = 0; e < 8; ++e) { T[e] = __builtin_amdgcn_raw_buffer_load_b128(rwo, vo, so + (32 * s + e) * (DM * 2), AUX_NT); if (e % PW == PW - 1) VM0(); }
#pragma unroll
        for (int f = 0; f < 8; ++f)
#pragma unroll
          for (int d = 0; d < 4; ++d) F[8 * s + f][d] = (f & 1) ? perm_hi(T[2 * d][f >> 1], T[2 * d + 1][f >> 1]) : perm_lo(T[2 * d][f >> 1], T[2 * d + 1][f >> 1]);
#pragma unroll
        for (int f = 0; f < 8; ++f) asm volatile("" : "+v"(F[8 * s + f]));
        SB();
      }
    }
    STAMPC(w == 0, 9);
    STAGE_GU()
    STAMPC(w == 0, 10);
    BAR();
    STAMPC(w == 0, 2);
    FRESH_IDS();
    v4u a[4];
#pragma unroll
    for (int s = 0; s < 4; ++s) a[s] = *(const v4u*)(lds + L_OP + (c >> 2) * OP_S + s * 64 + r * 16);
    const f32x4 m01 = *(const f32x4*)(lds + L_ML), m23 = *(const f32x4*)(lds + L_ML + 16);
    const float mown = *(const float*)(lds + L_ML + r * 8);
    const f32x4 zero = {0.f, 0.f, 0.f, 0.f};
    f32x4 d[8];
#pragma unroll
    for (int f = 0; f < 8; ++f) d[f] = mfma(a[0], F[f], zero);
#pragma unroll
    for (int s = 1; s < 4; ++s)
#pragma unroll
      for (int f = 0; f < 8; ++f) d[f] = mfma(a[s], F[8 * s + f], d[f]);
    const float M = fmaxf(fmaxf(m01[0], m01[2]), fmaxf(m23[0], m23[2]));
    const float fo = __builtin_amdgcn_exp2f(mown - M);
    const int p = head * 2 + half;
    if (w == 0) {
      const float L = fmaf(__builtin_amdgcn_exp2f(m01[0] - M), m01[1], __builtin_amdgcn_exp2f(m01[2] - M) * m01[3]) + fmaf(__builtin_amdgcn_exp2f(m23[0] - M), m23[1], __builtin_amdgcn_exp2f(m23[2] - M) * m23[3]);
      st128_sc1(v4u{as_u(M), tag, as_u(L), tag}, rws, lane == 0 ? 0 : 0x40000000, WS_ML + (b * 64 + p) * 16);
    }
    float t[4];
#pragma unroll
    for (int j = 0; j < 4; ++j) t[j] = rs32(d[j][0] * fo, d[j + 4][0] * fo);
    const float y0 = rs16(t[0], t[2]), y1 = rs16(t[1], t[3]);
    st128_sc1(v4u{as_u(y0), tag, as_u(y1), tag}, rws, (c >> 1) * 8192 + ((c & 1) * 4 + r) * 16, WS_Y + ((b * 40 + 8 * w) * 64 + p) * 128);
  }
  STAMPC(w == 0, 3);

  float h0 = 0.f, h1 = 0.f;
  if (is_red) {
    FRESH_IDS();
    if (w < 4) {
      const int hp = htid >> 3, cp = htid & 7;
      const int yo = (2 * hp) * 128 + cp * 16, ys = WS_Y + (rb * 40 + cb) * 8192, mo = 2 * hp * 16, ms = WS_ML + rb * 1024;
      v4u y0, y1, m0, m1;
      for (unsigned spin = 0;; ++spin) {
        y0 = __builtin_amdgcn_raw_buffer_load_b128(rws, yo, ys, AUX_SC1);
        y1 = __builtin_amdgcn_raw_buffer_load_b128(rws, yo + 128, ys, AUX_SC1);
        m0 = __builtin_amdgcn_raw_buffer_load_b128(rws, mo, ms, AUX_SC1);
        m1 = __builtin_amdgcn_raw_buffer_load_b128(rws, mo + 16, ms, AUX_SC1);
        SB();
        const unsigned bad = TAGBAD(y0) | TAGBAD(y1) | TAGBAD(m0) | TAGBAD(m1);
        if (__all(bad == 0) || spin > SPIN_CAP) break;
        asm volatile("" ::: "memory");
      }
      STAMPC(w == 0, 12);
      const float M = fmaxf(as_f(m0[0]), as_f(m1[0]));
      const float w0 = __builtin_amdgcn_exp2f(as_f(m0[0]) - M), w1 = __builtin_amdgcn_exp2f(as_f(m1[0]) - M);
      const float inv = __builtin_amdgcn_rcpf(fmaf(w0, as_f(m0[2]), w1 * as_f(m1[2])));
      float v0 = fmaf(w0, as_f(y0[0]), w1 * as_f(y1[0])) * inv, v1 = fmaf(w0, as_f(y0[2]), w1 * as_f(y1[2])) * inv;
      v0 += dppf<ROR8>(v0); v1 += dppf<ROR8>(v1);
      float vv = rs16(v0, v1);
      vv = rs32(vv, vv);
      if (lane < 32 && c < 8) *(float*)(lds + L_RED + w * 64 + c * 8 + r * 4) = vv;
    }
    BAR();
    if (htid < 8) {
      f32x2 s = *(const f32x2*)(lds + L_RED + htid * 8);
#pragma unroll
      for (int ww = 1; ww < 4; ++ww) s += *(const f32x2*)(lds + L_RED + ww * 64 + htid * 8);
      const unsigned xres = *(const unsigned*)(lds + L_XR + htid * 4);
      const unsigned hp2 = pk_bf16(lo_f(xres) + rbf(s.x), hi_f(xres) + rbf(s.y));
      h0 = lo_f(hp2); h1 = hi_f(hp2);
      __builtin_amdgcn_raw_buffer_store_b64(v2u{hp2, tag}, rws, htid * 8, WS_H + (rb * 320 + cb * 8) * 8, AUX_SC1);
    }
  }
  STAMPC(w == 0, 4);

  if (is_ffn) {
    FRESH_IDS();
    v4u BG[16], BD[8];
    {
      const rsrc_t rd = mk_rsrc(wd, DM * FF * 2);
      v4u D[8];
#pragma unroll
      for (int n = 0; n < 8; ++n) D[n] = __builtin_amdgcn_raw_buffer_load_b128(rd, r * (DM * 2) + c * 16, (fi * 32 + 4 * n) * (DM * 2) + w * 256, AUX_NT);
      SB();
#pragma unroll
      for (int s = 0; s < 4; ++s) {
        v2u t[8];
#pragma unroll
        for (int e = 0; e < 8; ++e) t[e] = *(const v2u*)(lds + L_BG + (128 * w + 32 * s + 8 * r + e) * 128 + c * 8);
#pragma unroll
        for (int d = 0; d < 4; ++d) {
          BG[4 * s + 0][d] = perm_lo(t[2 * d][0], t[2 * d + 1][0]); BG[4 * s + 1][d] = perm_hi(t[2 * d][0], t[2 * d + 1][0]);
          BG[4 * s + 2][d] = perm_lo(t[2 * d][1], t[2 * d + 1][1]); BG[4 * s + 3][d] = perm_hi(t[2 * d][1], t[2 * d + 1][1]);
        }
      }
#pragma unroll
      for (int n = 0; n < 16; ++n) asm volatile("" : "+v"(BG[n]));
      SB();
#pragma unroll
      for (int n = 0; n < 8; ++n) *(v4u*)(lds + L_WD + (w * 8 + n) * 1024 + lane * 16) = D[n];
      asm volatile("" ::: "memory");
#pragma unroll
      for (int s = 0; s < 4; ++s) {
        unsigned t[8];
#pragma unroll
        for (int e = 0; e < 8; ++e) t[e] = *(const unsigned*)(lds + L_WD + w * 8192 + (8 * r + e) * 256 + s * 64 + c * 4);
#pragma unroll
        for (int d = 0; d < 4; ++d) { BD[2 * s][d] = perm_lo(t[2 * d], t[2 * d + 1]); BD[2 * s + 1][d] = perm_hi(t[2 * d], t[2 * d + 1]); }
      }
#pragma unroll
      for (int n = 0; n < 8; ++n) asm volatile("" : "+v"(BD[n]));
    }
    STAMPC(w == 0, 15);
    if (w < 4) {
      v4u g0, g1, g2;
      const int hs = WS_H + w * 2560;
      for (unsigned spin = 0;; ++spin) {
        g0 = __builtin_amdgcn_raw_buffer_load_b128(rws, lane * 16, hs, AUX_SC1);
        g1 = __builtin_amdgcn_raw_buffer_load_b128(rws, lane * 16 + 1024, hs, AUX_SC1);
        g2 = __builtin_amdgcn_raw_buffer_load_b128(rws, (lane & 31) * 16 + 2048, hs, AUX_SC1);
        SB();
        const unsigned bad = TAGBAD(g0) | TAGBAD(g1) | TAGBAD(g2);
        if (__all(bad == 0) || spin > SPIN_CAP) break;
        asm volatile("" ::: "memory");
      }
      const unsigned hv[5] = {g0[0], g0[2], g1[0], g1[2], lane < 32 ? g2[0] : g2[2]};
      const float piv = as_f(__builtin_amdgcn_readfirstlane(hv[0] << 16));
      float s1 = 0.f, s2 = 0.f;
#pragma unroll
      for (int j = 0; j < 5; ++j) { const float d0 = lo_f(hv[j]) - piv, d1 = hi_f(hv[j]) - piv; s1 += d0 + d1; s2 = fmaf(d0, d0, s2); s2 = fmaf(d1, d1, s2); }
      float ss = rs32(s1, s2);
      ss = rs16(ss, ss);
      ss += dppf<ROR8>(ss); ss += dppf<ROR4>(ss); ss += dppf<ROR2>(ss); ss += dppf<ROR1>(ss);
      const float e1 = as_f(__builtin_amdgcn_readlane(as_u(ss), 0)) * (1.0f / DM), e2 = as_f(__builtin_amdgcn_readlane(as_u(ss), 32)) * (1.0f / DM);
      const float var = fmaxf(fmaf(-e1, e1, e2), 0.f);
      const float mb = rbf(piv + e1), rden = __builtin_amdgcn_rcpf(rbf(__builtin_amdgcn_sqrtf(rbf(rbf(var) + 1e-5f))));
      unsigned char* hrow = lds + L_HN + w * HN_S;
      *(v2u*)(hrow + lane * 8) = v2u{ln_pair(hv[0], mb, rden), ln_pair(hv[1], mb, rden)};
      *(v2u*)(hrow + 512 + lane * 8) = v2u{ln_pair(hv[2], mb, rden), ln_pair(hv[3], mb, rden)};
      *(unsigned*)(hrow + 1024 + (lane & 31) * 8 + (lane >> 5) * 4) = ln_pair(hv[4], mb, rden);
    }
    BAR();
    STAMPC(w == 0, 5);
    const f32x4 zero = {0.f, 0.f, 0.f, 0.f};
    f32x4 dd[4] = {zero, zero, zero, zero};
    v4u a[4];
#pragma unroll
    for (int s = 0; s < 4; ++s) a[s] = *(const v4u*)(lds + L_HN + (c >> 2) * HN_S + (128 * w + 32 * s + 8 * r) * 2);
    SB();
#pragma unroll
    for (int s = 0; s < 4; ++s)
#pragma unroll
      for (int f = 0; f < 4; ++f) dd[f] = mfma(a[s], BG[4 * s + f], dd[f]);
    {
      unsigned char* gp = lds + L_GP + (w * 4 + r) * GP_S + (c & 7) * 32 + (c >> 3) * 8;
      *(f32x2*)gp = f32x2{dd[0][0], dd[1][0]};
      *(f32x2*)(gp + 16) = f32x2{dd[2][0], dd[3][0]};
    }
    BAR();
    f32x4 gu[NHW];
#pragma unroll
    for (int ww = 0; ww < NHW; ++ww) gu[ww] = *(const f32x4*)(lds + L_GP + (ww * 4 + (c >> 2)) * GP_S + (4 * r + (c & 3)) * 16);
    SB();
    const f32x4 GU = ((gu[0] + gu[1]) + (gu[2] + gu[3])) + gu[4];
    float av[2];
#pragma unroll
    for (int j = 0; j < 2; ++j) {
      const float g = rbf(GU[j]), u = rbf(GU[2 + j]);
      const float u2 = 2.0f * 0.7978845608028654f * LOG2E * (g + 0.044715f * g * g * g);
      av[j] = rbf(g * __builtin_amdgcn_rcpf(1.0f + __builtin_amdgcn_exp2f(-u2))) * u;
    }
    const unsigned ap = pk_bf16(av[0], av[1]);
    const v4u af = {dppu<0x00>(ap), dppu<0x55>(ap), dppu<0xAA>(ap), dppu<0xFF>(ap)};
    f32x4 z[8];
#pragma unroll
    for (int f = 0; f < 8; ++f) z[f] = mfma(af, BD[f], zero);
#pragma unroll
    for (int s = 0; s < 4; ++s)
      st128_sc1(v4u{as_u(z[2 * s][0]), tag, as_u(z[2 * s + 1][0]), tag}, rws, (r * 40 + (c >> 3)) * (NFFN * 128) + (c & 7) * 16, WS_Z + ((8 * w + 2 * s) * NFFN + fi) * 128);
  }
  STAMPC(w == 0, 6);

  if (is_red) {
    FRESH_IDS();
    v4u z0, z1;
    const int zs = WS_Z + (rb * 40 + cb) * (NFFN * 128);
    const bool has2 = htid + 320 < NFFN * 8;
    for (unsigned spin = 0;; ++spin) {
      z0 = __builtin_amdgcn_raw_buffer_load_b128(rws, htid * 16, zs, AUX_SC1);
      z1 = __builtin_amdgcn_raw_buffer_load_b128(rws, has2 ? htid * 16 + 5120 : htid * 16, zs, AUX_SC1);
      SB();
      const unsigned bad = TAGBAD(z0) | TAGBAD(z1);
      if (__all(bad == 0) || spin > SPIN_CAP) break;
      asm volatile("" ::: "memory");
    }
    STAMPC(w == 0, 14);
    float v0 = as_f(z0[0]) + (has2 ? as_f(z1[0]) : 0.f), v1 = as_f(z0[2]) + (has2 ? as_f(z1[2]) : 0.f);
    v0 += dppf<ROR8>(v0); v1 += dppf<ROR8>(v1);
    float vv = rs16(v0, v1);
    vv = rs32(vv, vv);
    if (lane < 32 && c < 8) *(float*)(lds + L_RED2 + w * 64 + c * 8 + r * 4) = vv;
    BAR();
    if (htid < 8) {
      f32x2 s = *(const f32x2*)(lds + L_RED2 + htid * 8);
#pragma unroll
      for (int ww = 1; ww < NHW; ++ww) s += *(const f32x2*)(lds + L_RED2 + ww * 64 + htid * 8);
      *(unsigned*)(out + ri * 16 + htid * 2) = pk_bf16(h0 + rbf(s.x), h1 + rbf(s.y));
      if (bid == 0 && htid == 0) __builtin_amdgcn_raw_buffer_store_b32(tag, rws, 0, WS_EPOCH, AUX_SC1);
    }
  }
  STAMPC(w == 0, 7);
}

extern "C" void kernel_launch(void* const* d_in, const int* in_sizes, int n_in, void* d_out, int out_size, void* d_ws, size_t ws_size, hipStream_t stream) {
  fused_block<<<NWG, NT, 0, stream>>>((const u16*)d_in[0], (const u16*)d_in[1], (const u16*)d_in[2], (const u16*)d_in[3], (const u16*)d_in[4],
                                      (const u16*)d_in[6], (const u16*)d_in[7], (const u16*)d_in[8], (u16*)d_out, d_ws);
}
```

```cpp
#define AUX_K 2
#define AUX_V 2
#define AUX_W 0
#include <hip/hip_runtime.h>
#include <hip/hip_bf16.h>
#include <stdint.h>

typedef unsigned short u16;
typedef unsigned v4u __attribute__((ext_vector_type(4)));
typedef unsigned v2u __attribute__((ext_vector_type(2)));
typedef float f32x2 __attribute__((ext_vector_type(2)));
typedef float f32x4 __attribute__((ext_vector_type(4)));
typedef __bf16 bf16x2 __attribute__((ext_vector_type(2)));
typedef __bf16 bf16x8 __attribute__((ext_vector_type(8)));
typedef __amdgpu_buffer_rsrc_t rsrc_t;
#define GAS __attribute__((address_space(1)))
#define LAS __attribute__((address_space(3)))

#define LAGN 2
#define LAGF 0
#define SKEW 0
#define PW 2
#define PG 1
#define GU_FIRST 0
#define AUX_X 0
#define DPRE 8
#define QDELAY 0
#define AUX_QP 16
#define AUX_EP 16
#define ABL 0
#define PRE0 4
constexpr int NPRE = 4;

constexpr int NB = 4, DM = 640, NH = 32, SK = 4096, DH = 128, FF = 1728;
constexpr int NSW = 4, NHW = 5, NT = 64 * (NSW + NHW), NWG = 256;
constexpr int NRED = 160, NFFN = 54;
constexpr int WS_EPOCH = 0;
constexpr int WS_ML = 4096;
constexpr int WS_H = 8192;
constexpr int WS_Q = 65536;
constexpr int WS_Y = WS_Q + NB * NH * 4096;
constexpr int WS_Z = WS_Y + NB * 40 * 64 * 128;
constexpr int WS_END = WS_Z + NB * 40 * NFFN * 128;
constexpr int WS_STAMP = WS_END;
constexpr int AUX_NT = 2, AUX_SC1 = 16;
constexpr unsigned SPIN_CAP = 1u << 18;
constexpr float LOG2E = 1.4426950408889634f;

#define STAMPC(cond, i) do { } while (0)

__device__ __forceinline__ rsrc_t mk_rsrc(const void* p, int bytes) { return __builtin_amdgcn_make_buffer_rsrc((void*)p, 0, bytes, 0x00020000); }
__device__ __forceinline__ float as_f(unsigned u) { return __builtin_bit_cast(float, u); }
__device__ __forceinline__ unsigned as_u(float f) { return __builtin_bit_cast(unsigned, f); }
__device__ __forceinline__ unsigned pk_bf16(float lo, float hi) { f32x2 v = {lo, hi}; return __builtin_bit_cast(unsigned, __builtin_convertvector(v, bf16x2)); }
__device__ __forceinline__ float rbf(float x) { return as_f(pk_bf16(0.f, x)); }
__device__ __forceinline__ float lo_f(unsigned u) { return as_f(u << 16); }
__device__ __forceinline__ float hi_f(unsigned u) { return as_f(u & 0xffff0000u); }
__device__ __forceinline__ float dot2(unsigned a, unsigned b, float c) { return __builtin_amdgcn_fdot2_f32_bf16(__builtin_bit_cast(bf16x2, a), __builtin_bit_cast(bf16x2, b), c, false); }
__device__ __forceinline__ f32x4 mfma(v4u a, v4u b, f32x4 c) { return __builtin_amdgcn_mfma_f32_16x16x32_bf16(__builtin_bit_cast(bf16x8, a), __builtin_bit_cast(bf16x8, b), c, 0, 0, 0); }
template <int CTRL> __device__ __forceinline__ unsigned dppu(unsigned x) { return __builtin_amdgcn_mov_dpp((int)x, CTRL, 0xf, 0xf, true); }
template <int CTRL> __device__ __forceinline__ float dppf(float x) { return as_f(dppu<CTRL>(as_u(x))); }
constexpr int ROR1 = 0x121, ROR2 = 0x122, ROR4 = 0x124, ROR8 = 0x128, XOR1 = 0xB1, XOR2 = 0x4E, XOR7 = 0x141, BCAST = 0x150;
__device__ __forceinline__ unsigned perm_lo(unsigned a, unsigned b) { return __builtin_amdgcn_perm(b, a, 0x05040100u); }
__device__ __forceinline__ unsigned perm_hi(unsigned a, unsigned b) { return __builtin_amdgcn_perm(b, a, 0x07060302u); }
__device__ __forceinline__ float rs16(float a, float b) { auto s = __builtin_amdgcn_permlane16_swap(as_u(a), as_u(b), false, false); return as_f(s[0]) + as_f(s[1]); }
__device__ __forceinline__ float rs32(float a, float b) { auto s = __builtin_amdgcn_permlane32_swap(as_u(a), as_u(b), false, false); return as_f(s[0]) + as_f(s[1]); }
__device__ __forceinline__ float xrow_sum(float x) { x = rs16(x, x); return rs32(x, x); }
__device__ __forceinline__ float xrow_max(float x) {
  auto s = __builtin_amdgcn_permlane16_swap(as_u(x), as_u(x), false, false); x = fmaxf(as_f(s[0]), as_f(s[1]));
  auto t = __builtin_amdgcn_permlane32_swap(as_u(x), as_u(x), false, false); return fmaxf(as_f(t[0]), as_f(t[1]));
}
__device__ __forceinline__ void st128_sc1(v4u v, rsrc_t rs, int voff, int soff) {
  __builtin_amdgcn_raw_buffer_store_b128(v, rs, voff, soff, AUX_SC1);
  asm volatile("s_nop 1" :: "v"(v) : "memory");
}
__device__ __forceinline__ int fresh_lane() { int l; asm volatile("v_mbcnt_lo_u32_b32 %0, -1, 0\n\tv_mbcnt_hi_u32_b32 %0, -1, %0" : "=v"(l)); return l; }
__device__ __forceinline__ unsigned ln_pair(unsigned v, float mb, float rden) { return pk_bf16(rbf(lo_f(v) - mb) * rden, rbf(hi_f(v) - mb) * rden); }
#define SB() __builtin_amdgcn_sched_barrier(0)
#define VM0() do { SB(); asm volatile("s_waitcnt vmcnt(0)" ::: "memory"); SB(); } while (0)
#define BAR() do { asm volatile("s_waitcnt lgkmcnt(0)" ::: "memory"); __builtin_amdgcn_s_barrier(); asm volatile("" ::: "memory"); } while (0)
#define TAGBAD(g) (((g)[1] ^ tag) | ((g)[3] ^ tag))
#define FRESH_IDS() const int lane = fresh_lane(); const int r = lane >> 4, c = lane & 15, htid = w * 64 + lane; (void)r; (void)c; (void)htid

constexpr int L_FLAG = 0;
constexpr int L_XR = 16;
constexpr int L_ML = 64;
constexpr int L_Q = 128;
constexpr int L_RED = 384;
constexpr int L_RED2 = L_RED + 320;
constexpr int OP_S = 320;
constexpr int L_OP = L_RED2 + 320;
constexpr int GP_S = 384;
constexpr int L_GP = L_OP + 4 * OP_S;
constexpr int HN_S = 1344;
constexpr int L_HN = L_GP + 5 * 4 * GP_S;
constexpr int L_BG = L_HN + 4 * HN_S;
constexpr int L_WD = L_BG + 640 * 128;
constexpr int L_END = L_WD + 5 * 32 * 256;
static_assert(L_BG % 16 == 0 && L_HN % 16 == 0 && L_OP % 16 == 0 && L_GP % 16 == 0, "align");

__device__ __forceinline__ int rank_of(int bid) { return (bid & 7) + 8 * (bid >> 4); }
__device__ __forceinline__ int ffn_of_rank(int j) { const int s = j - 32; if (s < 0) return -1; const int k = (s & 7) + 8 * (s >> 4); return k < 27 ? 2 * k + ((s >> 3) & 1) : -1; }

__global__ void __launch_bounds__(NT, 3) fused_block(const u16* __restrict__ x, const u16* __restrict__ kc, const u16* __restrict__ vc,
                                                     const u16* __restrict__ wq, const u16* __restrict__ wo, const u16* __restrict__ wg,
                                                     const u16* __restrict__ wu, const u16* __restrict__ wd, u16* __restrict__ out, void* ws) {
  __shared__ __attribute__((aligned(16))) unsigned char lds[L_END + 256];
  const int wv = __builtin_amdgcn_readfirstlane(threadIdx.x >> 6);
  const int bid = blockIdx.x, xs = bid & 7, rr = bid >> 3;
  const int head = xs + 8 * (rr >> 3), b = (rr >> 1) & 3, half = rr & 1;
  STAMPC(wv == 0, 0);

  if (wv >= NHW) {
    const int w = wv - NHW;
    const int lane = threadIdx.x & 63, r = lane >> 4, c = lane & 15;
    const rsrc_t rk = mk_rsrc(kc, NB * NH * SK * DH * 2), rv = mk_rsrc(vc, NB * NH * SK * DH * 2);
    const int n0 = 32 + (ffn_of_rank(rank_of(bid)) >= 0 ? SKEW : 0);
    const int nr = half ? 64 - n0 : n0;
    const int kvbase = (b * NH + head) * (SK * DH * 2) + (half ? n0 * (NSW * 4096) : 0) + w * 4096;
    const int voff = lane * 16;
    STAMPC(w == 0, 13);
    if (PRE0 == 0) __builtin_amdgcn_s_barrier();
    STAMPC(w == 0, 31);
    v4u B[NPRE][8];
#define LD(i, so) (((i) & 4) ? __builtin_amdgcn_raw_buffer_load_b128(rv, voff, (so) + ((i) & 3) * 1024, AUX_V) : __builtin_amdgcn_raw_buffer_load_b128(rk, voff, (so) + ((i) & 3) * 1024, AUX_K))
#pragma unroll
    for (int t = 0; t < NPRE; ++t)
#pragma unroll
      for (int i = 0; i < 8; ++i) { B[t][i] = LD(i, kvbase + t * (NSW * 4096)); if (PRE0 && 8 * t + i + 1 == PRE0) { SB(); __builtin_amdgcn_s_barrier(); SB(); } if (DPRE && 8 * t + i + 1 >= DPRE) { SB(); asm volatile("s_waitcnt vmcnt(%0)" :: "i"(DPRE - 1) : "memory"); SB(); } if (i == 7 && t < 3) STAMPC(w == 0, 28 + t); }
    SB();
    STAMPC(w == 0, 11);
    {
      unsigned spin = 0;
      while (*(volatile LAS unsigned*)(LAS unsigned char*)(lds + L_FLAG + 4) == 0u && ++spin < (SPIN_CAP << 4)) __builtin_amdgcn_s_sleep(1);
      asm volatile("" ::: "memory");
    }
    const v4u q = *(const v4u*)(lds + L_Q + c * 16);
    STAMPC(w == 0, 1);
    float m = -INFINITY, l = 0.f, acc[8];
#pragma unroll
    for (int e = 0; e < 8; ++e) acc[e] = 0.f;

    int thr = (half && ffn_of_rank(rank_of(bid)) >= 0) ? LAGF : LAGN;
#define THROTTLE() asm volatile("s_cmp_lt_u32 %0, 1\n\ts_cbranch_scc1 1f\n\ts_waitcnt vmcnt(6)\n\ts_cmp_lt_u32 %0, 2\n\ts_cbranch_scc1 1f\n\ts_waitcnt vmcnt(5)\n1:" :: "s"(thr) : "scc", "memory")
#define REFILL(i, DO_CUR, CUR, DO_NXT, NXT) if (DO_NXT) { if (DO_CUR) THROTTLE(); B[0][i] = LD(i, NXT); SB(); }
#define QK(T, j, DO_CUR, CUR, DO_NXT, NXT) { \
      float d = dot2(B[T][j][0], q[0], 0.f); d = dot2(B[T][j][1], q[1], d); d = dot2(B[T][j][2], q[2], d); d = dot2(B[T][j][3], q[3], d); \
      asm volatile("" : "+v"(d)); sx[j] = d; SB(); REFILL(j, DO_CUR, CUR, DO_NXT, NXT) }
#define PV(T, j, DO_CUR, CUR, DO_NXT, NXT) { const unsigned pl = dppu<BCAST + 4 * (j)>(plo), ph = dppu<BCAST + 4 * (j)>(phi); \
      _Pragma("unroll") for (int d = 0; d < 4; ++d) { acc[2 * d] = dot2(B[T][4 + j][d], pl, acc[2 * d]); acc[2 * d + 1] = dot2(B[T][4 + j][d], ph, acc[2 * d + 1]); } \
      asm volatile("" : "+v"(acc[0]), "+v"(acc[1]), "+v"(acc[2]), "+v"(acc[3]), "+v"(acc[4]), "+v"(acc[5]), "+v"(acc[6]), "+v"(acc[7])); SB(); \
      REFILL(4 + j, DO_CUR, CUR, DO_NXT, NXT) }
#define TILE_BODY(T, DO_CUR, CUR, DO_NXT, NXT) { \
    float sx[4]; \
    QK(T, 0, DO_CUR, CUR, DO_NXT, NXT) QK(T, 1, DO_CUR, CUR, DO_NXT, NXT) QK(T, 2, DO_CUR, CUR, DO_NXT, NXT) QK(T, 3, DO_CUR, CUR, DO_NXT, NXT) \
    const float a0 = sx[0] + dppf<ROR8>(sx[0]), a2 = sx[2] + dppf<ROR8>(sx[2]), a1 = sx[1] + dppf<ROR8>(sx[1]), a3 = sx[3] + dppf<ROR8>(sx[3]); \
    const float b0 = (c & 8) ? a2 : a0, b1 = (c & 8) ? a3 : a1; \
    const float c0 = b0 + dppf<XOR7>(b0), c1 = b1 + dppf<XOR7>(b1); \
    float s = (c & 4) ? c1 : c0; \
    s += dppf<XOR2>(s); s += dppf<XOR1>(s); \
    s = rbf(rbf(s) * 0.08838834764831845f) * LOG2E; \
    float pm = fmaxf(s, dppf<ROR4>(s)); pm = fmaxf(pm, dppf<ROR8>(pm)); \
    const float mn = fmaxf(m, pm); \
    const float alpha = __builtin_amdgcn_exp2f(m - mn), p = __builtin_amdgcn_exp2f(s - mn); \
    l = fmaf(l, alpha, p); m = mn; \
    _Pragma("unroll") for (int e = 0; e < 8; ++e) acc[e] *= alpha; \
    const unsigned plo = pk_bf16(p, 0.f), phi = pk_bf16(0.f, p); \
    PV(T, 0, DO_CUR, CUR, DO_NXT, NXT) PV(T, 1, DO_CUR, CUR, DO_NXT, NXT) PV(T, 2, DO_CUR, CUR, DO_NXT, NXT) PV(T, 3, DO_CUR, CUR, DO_NXT, NXT) }

    int so = kvbase + NPRE * (NSW * 4096);
    TILE_BODY(0, false, 0, true, so)
    TILE_BODY(1, false, 0, false, 0)
    TILE_BODY(2, false, 0, false, 0)
    TILE_BODY(3, false, 0, false, 0)
    for (int it = NPRE; it < nr; ++it) {
      const bool more = it + 1 < nr;
      so = more ? so + NSW * 4096 : 0x40000000;
      thr = more ? thr : 0;
      TILE_BODY(0, true, 0, true, so)
      STAMPC(w == 0 && (it & 7) == 7, 20 + (it >> 3));
    }
    STAMPC(true, 16 + w);
    {
      const float Mw = xrow_max(m);
      const float f = __builtin_amdgcn_exp2f(m - Mw);
      float lr = l + dppf<ROR4>(l); lr += dppf<ROR8>(lr);
      float t[4];
#pragma unroll
      for (int j = 0; j < 4; ++j) t[j] = rs32(acc[j] * f, acc[j + 4] * f);
      *(unsigned*)(lds + L_OP + w * OP_S + (4 * c + r) * 4) = pk_bf16(rs16(t[0], t[2]), rs16(t[1], t[3]));
      const float Lw = xrow_sum(lr * f);
      if (lane == 0) *(f32x2*)(lds + L_ML + w * 8) = f32x2{Mw, Lw};
    }
    BAR();
    return;
  }

  const int w = wv;
  const int ri = half * 128 + rank_of(bid);
  __builtin_amdgcn_s_setprio(2);
  const rsrc_t rws = mk_rsrc(ws, WS_END);
  unsigned tag;
  {
    FRESH_IDS();
    const rsrc_t rx = mk_rsrc(x, ABL & 1 ? 0 : NB * DM * 2), rwq = mk_rsrc(wq, ABL & 2 ? 0 : DM * NH * DH * 2);
    v4u xs5[5];
#pragma unroll
    for (int n = 0; n < 5; ++n) xs5[n] = __builtin_amdgcn_raw_buffer_load_b128(rx, (c >> 2) * (DM * 2) + (4 * r + (c & 3)) * 16, n * 256, AUX_X);
    const v4u xa = __builtin_amdgcn_raw_buffer_load_b128(rx, (c >> 2) * (DM * 2) + r * 16, (160 * b + 32 * w) * 2, AUX_X);
    v2u Wq[8];
#pragma unroll
    for (int e = 0; e < 8; ++e) Wq[e] = __builtin_amdgcn_raw_buffer_load_b64(rwq, (8 * r) * 8192 + c * 8, (160 * b + 32 * w + e) * 8192 + head * 256 + half * 128, AUX_W);
    const unsigned epoch_v = __builtin_amdgcn_raw_buffer_load_b32(rws, ABL & 8 ? 0x40000000 : 0, WS_EPOCH, AUX_EP);
    const unsigned xres = __builtin_amdgcn_raw_buffer_load_b32(rx, (lane & 7) * 4, ri * 32, AUX_X);
    if (htid < 2) *(unsigned*)(lds + L_FLAG + htid * 4) = 0u;
    SB();
    BAR();
    SB();
    STAMPC(w == 0, 27);
    const float piv = dppf<0x00>(xrow_max(r == 0 ? lo_f(xs5[0][0]) : -INFINITY));
    float s1 = 0.f, s2 = 0.f;
#pragma unroll
    for (int n = 0; n < 5; ++n)
#pragma unroll
      for (int d = 0; d < 4; ++d) { const float d0 = lo_f(xs5[n][d]) - piv, d1 = hi_f(xs5[n][d]) - piv; s1 += d0 + d1; s2 = fmaf(d0, d0, s2); s2 = fmaf(d1, d1, s2); }
    s1 += dppf<XOR1>(s1); s2 += dppf<XOR1>(s2); s1 += dppf<XOR2>(s1); s2 += dppf<XOR2>(s2);
    s1 = xrow_sum(s1) * (1.0f / DM); s2 = xrow_sum(s2) * (1.0f / DM);
    const float var = fmaxf(fmaf(-s1, s1, s2), 0.f);
    const float mb = rbf(piv + s1), rden = __builtin_amdgcn_rcpf(rbf(__builtin_amdgcn_sqrtf(rbf(rbf(var) + 1e-5f))));
    const v4u af = {ln_pair(xa[0], mb, rden), ln_pair(xa[1], mb, rden), ln_pair(xa[2], mb, rden), ln_pair(xa[3], mb, rden)};
    STAMPC(w == 0, 8);
    const f32x4 zero = {0.f, 0.f, 0.f, 0.f};
    f32x4 dq[4];
#pragma unroll
    for (int f = 0; f < 4; ++f) {
      v4u bf;
#pragma unroll
      for (int d = 0; d < 4; ++d) bf[d] = (f & 1) ? perm_hi(Wq[2 * d][f >> 1], Wq[2 * d + 1][f >> 1]) : perm_lo(Wq[2 * d][f >> 1], Wq[2 * d + 1][f >> 1]);
      dq[f] = mfma(af, bf, zero);
    }
    *(f32x4*)(lds + L_GP + w * 1024 + lane * 16) = f32x4{dq[0][0], dq[1][0], dq[2][0], dq[3][0]};
    if (htid < 8) *(unsigned*)(lds + L_XR + htid * 4) = xres;
    tag = __builtin_amdgcn_readfirstlane(epoch_v) + 1u;
    asm volatile("s_waitcnt lgkmcnt(0)" ::: "memory");
    if (lane == 0) __hip_atomic_fetch_add((LAS unsigned*)(LAS unsigned char*)(lds + L_FLAG), 1u, __ATOMIC_RELAXED, __HIP_MEMORY_SCOPE_WORKGROUP);
    STAMPC(w == 0, 24);
    if (w < 2 && !(ABL & 4)) {
      unsigned spin = 0;
      while (*(volatile LAS unsigned*)(LAS unsigned char*)(lds + L_FLAG) < (unsigned)NHW && ++spin < (SPIN_CAP << 4)) { }
      asm volatile("" ::: "memory");
      f32x2 s = {0.f, 0.f};
#pragma unroll
      for (int ww = 0; ww < NHW; ++ww) s += *(const f32x2*)(lds + L_GP + ww * 1024 + htid * 8);
      st128_sc1(v4u{as_u(s.x), tag, as_u(s.y), tag}, rws, (htid >> 5) * (NH * 4096) + (htid & 31) * 16, WS_Q + head * 4096 + b * 1024 + half * 512);
      STAMPC(w == 0, 25);
    }
    if (w == 2) {
      v4u g[4];
      if (QDELAY) { unsigned spin = 0; while (*(volatile LAS unsigned*)(LAS unsigned char*)(lds + L_FLAG) < (unsigned)NHW && ++spin < (SPIN_CAP << 4)) { }
        __builtin_amdgcn_s_sleep(QDELAY); }
      if (ABL & 4) { for (int k = 0; k < 4; ++k) g[k] = v4u{0, 0, 0, 0}; __builtin_amdgcn_s_sleep(100); } else
      for (unsigned spin = 0;; ++spin) {
#pragma unroll
        for (int k = 0; k < 4; ++k) g[k] = __builtin_amdgcn_raw_buffer_load_b128(rws, lane * 16, WS_Q + (b * NH + head) * 4096 + k * 1024, AUX_QP);
        SB();
        const unsigned bad = TAGBAD(g[0]) | TAGBAD(g[1]) | TAGBAD(g[2]) | TAGBAD(g[3]);
        if (__all(bad == 0) || spin > SPIN_CAP) break;
        asm volatile("" ::: "memory");
      }
      *(unsigned*)(lds + L_Q + lane * 4) = pk_bf16((as_f(g[0][0]) + as_f(g[1][0])) + (as_f(g[2][0]) + as_f(g[3][0])), (as_f(g[0][2]) + as_f(g[1][2])) + (as_f(g[2][2]) + as_f(g[3][2])));
      asm volatile("s_waitcnt lgkmcnt(0)" ::: "memory");
      *(volatile LAS unsigned*)(LAS unsigned char*)(lds + L_FLAG + 4) = 1u;
      STAMPC(true, 26);
    } else {
      unsigned spin = 0;
      while (*(volatile LAS unsigned*)(LAS unsigned char*)(lds + L_FLAG + 4) == 0u && ++spin < (SPIN_CAP << 4)) __builtin_amdgcn_s_sleep(2);
      asm volatile("" ::: "memory");
    }
  }

  const bool is_red = ri < NRED;
  const int rb = ri / 40, cb = ri % 40;
  const int fi = half ? ffn_of_rank(rank_of(bid)) : -1;
  const bool is_ffn = fi >= 0;

#define STAGE_GU() if (is_ffn) { \
    const int ln = fresh_lane(); \
    const GAS unsigned char* gp = (const GAS unsigned char*)((ln & 4) ? wu : wg) + (size_t)((128 * w + (ln >> 3)) * (FF * 2) + fi * 64 + (ln & 3) * 16); \
    _Pragma("nounroll") for (int s = 0; s < 16 / PG; ++s) { \
      v4u GU[PG]; \
      _Pragma("unroll") for (int n = 0; n < PG; ++n) GU[n] = AUX_W ? __builtin_nontemporal_load((const GAS v4u*)(gp + 8 * n * (FF * 2))) : *(const GAS v4u*)(gp + 8 * n * (FF * 2)); \
      gp += 8 * PG * (FF * 2); \
      _Pragma("unroll") for (int n = 0; n < PG; ++n) *(v4u*)(lds + L_BG + (w * 16 + s * PG + n) * 1024 + ln * 16) = GU[n]; \
    } }
  {
    v4u F[32];
    {
      FRESH_IDS();
      const rsrc_t rwo = mk_rsrc(wo, DM * NH * DH * 2);
      const int vo = (8 * r) * (DM * 2) + c * 16, so = head * DH * (DM * 2) + w * 256;
#pragma unroll
      for (int s = 0; s < 4; ++s) {
        v4u T[8];
#pragma unroll
        for (int e = 0; e < 8; ++e) { T[e] = __builtin_amdgcn_raw_buffer_load_b128(rwo, vo, so + (32 * s + e) * (DM * 2), AUX_W); if (e % PW == PW - 1) VM0(); }
#pragma unroll
        for (int f = 0; f < 8; ++f)
#pragma unroll
          for (int d = 0; d < 4; ++d) F[8 * s + f][d] = (f & 1) ? perm_hi(T[2 * d][f >> 1], T[2 * d + 1][f >> 1]) : perm_lo(T[2 * d][f >> 1], T[2 * d + 1][f >> 1]);
#pragma unroll
        for (int f = 0; f < 8; ++f) asm volatile("" : "+v"(F[8 * s + f]));
        SB();
      }
    }
    STAMPC(w == 0, 9);
    STAGE_GU()
    STAMPC(w == 0, 10);
    BAR();
    STAMPC(w == 0, 2);
    FRESH_IDS();
    v4u a[4];
#pragma unroll
    for (int s = 0; s < 4; ++s) a[s] = *(const v4u*)(lds + L_OP + (c >> 2) * OP_S + s * 64 + r * 16);
    const f32x4 m01 = *(const f32x4*)(lds + L_ML), m23 = *(const f32x4*)(lds + L_ML + 16);
    const float mown = *(const float*)(lds + L_ML + r * 8);
    const f32x4 zero = {0.f, 0.f, 0.f, 0.f};
    f32x4 d[8];
#pragma unroll
    for (int f = 0; f < 8; ++f) d[f] = mfma(a[0], F[f], zero);
#pragma unroll
    for (int s = 1; s < 4; ++s)
#pragma unroll
      for (int f = 0; f < 8; ++f) d[f] = mfma(a[s], F[8 * s + f], d[f]);
    const float M = fmaxf(fmaxf(m01[0], m01[2]), fmaxf(m23[0], m23[2]));
    const float fo = __builtin_amdgcn_exp2f(mown - M);
    const int p = head * 2 + half;
    if (w == 0) {
      const float L = fmaf(__builtin_amdgcn_exp2f(m01[0] - M), m01[1], __builtin_amdgcn_exp2f(m01[2] - M) * m01[3]) + fmaf(__builtin_amdgcn_exp2f(m23[0] - M), m23[1], __builtin_amdgcn_exp2f(m23[2] - M) * m23[3]);
      st128_sc1(v4u{as_u(M), tag, as_u(L), tag}, rws, lane == 0 ? 0 : 0x40000000, WS_ML + (b * 64 + p) * 16);
    }
    float t[4];
#pragma unroll
    for (int j = 0; j < 4; ++j) t[j] = rs32(d[j][0] * fo, d[j + 4][0] * fo);
    const float y0 = rs16(t[0], t[2]), y1 = rs16(t[1], t[3]);
    st128_sc1(v4u{as_u(y0), tag, as_u(y1), tag}, rws, (c >> 1) * 8192 + ((c & 1) * 4 + r) * 16, WS_Y + ((b * 40 + 8 * w) * 64 + p) * 128);
  }
  STAMPC(w == 0, 3);

  float h0 = 0.f, h1 = 0.f;
  if (is_red) {
    FRESH_IDS();
    if (w < 4) {
      const int hp = htid >> 3, cp = htid & 7;
      const int yo = (2 * hp) * 128 + cp * 16, ys = WS_Y + (rb * 40 + cb) * 8192, mo = 2 * hp * 16, ms = WS_ML + rb * 1024;
      v4u y0, y1, m0, m1;
      for (unsigned spin = 0;; ++spin) {
        y0 = __builtin_amdgcn_raw_buffer_load_b128(rws, yo, ys, AUX_SC1);
        y1 = __builtin_amdgcn_raw_buffer_load_b128(rws, yo + 128, ys, AUX_SC1);
        m0 = __builtin_amdgcn_raw_buffer_load_b128(rws, mo, ms, AUX_SC1);
        m1 = __builtin_amdgcn_raw_buffer_load_b128(rws, mo + 16, ms, AUX_SC1);
        SB();
        const unsigned bad = TAGBAD(y0) | TAGBAD(y1) | TAGBAD(m0) | TAGBAD(m1);
        if (__all(bad == 0) || spin > SPIN_CAP) break;
        asm volatile("" ::: "memory");
      }
      STAMPC(w == 0, 12);
      const float M = fmaxf(as_f(m0[0]), as_f(m1[0]));
      const float w0 = __builtin_amdgcn_exp2f(as_f(m0[0]) - M), w1 = __builtin_amdgcn_exp2f(as_f(m1[0]) - M);
      const float inv = __builtin_amdgcn_rcpf(fmaf(w0, as_f(m0[2]), w1 * as_f(m1[2])));
      float v0 = fmaf(w0, as_f(y0[0]), w1 * as_f(y1[0])) * inv, v1 = fmaf(w0, as_f(y0[2]), w1 * as_f(y1[2])) * inv;
      v0 += dppf<ROR8>(v0); v1 += dppf<ROR8>(v1);
      float vv = rs16(v0, v1);
      vv = rs32(vv, vv);
      if (lane < 32 && c < 8) *(float*)(lds + L_RED + w * 64 + c * 8 + r * 4) = vv;
    }
    BAR();
    if (htid < 8) {
      f32x2 s = *(const f32x2*)(lds + L_RED + htid * 8);
#pragma unroll
      for (int ww = 1; ww < 4; ++ww) s += *(const f32x2*)(lds + L_RED + ww * 64 + htid * 8);
      const unsigned xres = *(const unsigned*)(lds + L_XR + htid * 4);
      const unsigned hp2 = pk_bf16(lo_f(xres) + rbf(s.x), hi_f(xres) + rbf(s.y));
      h0 = lo_f(hp2); h1 = hi_f(hp2);
      __builtin_amdgcn_raw_buffer_store_b64(v2u{hp2, tag}, rws, htid * 8, WS_H + (rb * 320 + cb * 8) * 8, AUX_SC1);
    }
  }
  STAMPC(w == 0, 4);

  if (is_ffn) {
    FRESH_IDS();
    v4u BG[16], BD[8];
    {
      const rsrc_t rd = mk_rsrc(wd, DM * FF * 2);
      v4u D[8];
#pragma unroll
      for (int n = 0; n < 8; ++n) D[n] = __builtin_amdgcn_raw_buffer_load_b128(rd, r * (DM * 2) + c * 16, (fi * 32 + 4 * n) * (DM * 2) + w * 256, AUX_W);
      SB();
#pragma unroll
      for (int s = 0; s < 4; ++s) {
        v2u t[8];
#pragma unroll
        for (int e = 0; e < 8; ++e) t[e] = *(const v2u*)(lds + L_BG + (128 * w + 32 * s + 8 * r + e) * 128 + c * 8);
#pragma unroll
        for (int d = 0; d < 4; ++d) {
          BG[4 * s + 0][d] = perm_lo(t[2 * d][0], t[2 * d + 1][0]); BG[4 * s + 1][d] = perm_hi(t[2 * d][0], t[2 * d + 1][0]);
          BG[4 * s + 2][d] = perm_lo(t[2 * d][1], t[2 * d + 1][1]); BG[4 * s + 3][d] = perm_hi(t[2 * d][1], t[2 * d + 1][1]);
        }
      }
#pragma unroll
      for (int n = 0; n < 16; ++n) asm volatile("" : "+v"(BG[n]));
      SB();
#pragma unroll
      for (int n = 0; n < 8; ++n) *(v4u*)(lds + L_WD + (w * 8 + n) * 1024 + lane * 16) = D[n];
      asm volatile("" ::: "memory");
#pragma unroll
      for (int s = 0; s < 4; ++s) {
        unsigned t[8];
#pragma unroll
        for (int e = 0; e < 8; ++e) t[e] = *(const unsigned*)(lds + L_WD + w * 8192 + (8 * r + e) * 256 + s * 64 + c * 4);
#pragma unroll
        for (int d = 0; d < 4; ++d) { BD[2 * s][d] = perm_lo(t[2 * d], t[2 * d + 1]); BD[2 * s + 1][d] = perm_hi(t[2 * d], t[2 * d + 1]); }
      }
#pragma unroll
      for (int n = 0; n < 8; ++n) asm volatile("" : "+v"(BD[n]));
    }
    STAMPC(w == 0, 15);
    if (w < 4) {
      v4u g0, g1, g2;
      const int hs = WS_H + w * 2560;
      for (unsigned spin = 0;; ++spin) {
        g0 = __builtin_amdgcn_raw_buffer_load_b128(rws, lane * 16, hs, AUX_SC1);
        g1 = __builtin_amdgcn_raw_buffer_load_b128(rws, lane * 16 + 1024, hs, AUX_SC1);
        g2 = __builtin_amdgcn_raw_buffer_load_b128(rws, (lane & 31) * 16 + 2048, hs, AUX_SC1);
        SB();
        const unsigned bad = TAGBAD(g0) | TAGBAD(g1) | TAGBAD(g2);
        if (__all(bad == 0) || spin > SPIN_CAP) break;
        asm volatile("" ::: "memory");
      }
      const unsigned hv[5] = {g0[0], g0[2], g1[0], g1[2], lane < 32 ? g2[0] : g2[2]};
      const float piv = as_f(__builtin_amdgcn_readfirstlane(hv[0] << 16));
      float s1 = 0.f, s2 = 0.f;
#pragma unroll
      for (int j = 0; j < 5; ++j) { const float d0 = lo_f(hv[j]) - piv, d1 = hi_f(hv[j]) - piv; s1 += d0 + d1; s2 = fmaf(d0, d0, s2); s2 = fmaf(d1, d1, s2); }
      float ss = rs32(s1, s2);
      ss = rs16(ss, ss);
      ss += dppf<ROR8>(ss); ss += dppf<ROR4>(ss); ss += dppf<ROR2>(ss); ss += dppf<ROR1>(ss);
      const float e1 = as_f(__builtin_amdgcn_readlane(as_u(ss), 0)) * (1.0f / DM), e2 = as_f(__builtin_amdgcn_readlane(as_u(ss), 32)) * (1.0f / DM);
      const float var = fmaxf(fmaf(-e1, e1, e2), 0.f);
      const float mb = rbf(piv + e1), rden = __builtin_amdgcn_rcpf(rbf(__builtin_amdgcn_sqrtf(rbf(rbf(var) + 1e-5f))));
      unsigned char* hrow = lds + L_HN + w * HN_S;
      *(v2u*)(hrow + lane * 8) = v2u{ln_pair(hv[0], mb, rden), ln_pair(hv[1], mb, rden)};
      *(v2u*)(hrow + 512 + lane * 8) = v2u{ln_pair(hv[2], mb, rden), ln_pair(hv[3], mb, rden)};
      *(unsigned*)(hrow + 1024 + (lane & 31) * 8 + (lane >> 5) * 4) = ln_pair(hv[4], mb, rden);
    }
    BAR();
    STAMPC(w == 0, 5);
    const f32x4 zero = {0.f, 0.f, 0.f, 0.f};
    f32x4 dd[4] = {zero, zero, zero, zero};
    v4u a[4];
#pragma unroll
    for (int s = 0; s < 4; ++s) a[s] = *(const v4u*)(lds + L_HN + (c >> 2) * HN_S + (128 * w + 32 * s + 8 * r) * 2);
    SB();
#pragma unroll
    for (int s = 0; s < 4; ++s)
#pragma unroll
      for (int f = 0; f < 4; ++f) dd[f] = mfma(a[s], BG[4 * s + f], dd[f]);
    {
      unsigned char* gp = lds + L_GP + (w * 4 + r) * GP_S + (c & 7) * 32 + (c >> 3) * 8;
      *(f32x2*)gp = f32x2{dd[0][0], dd[1][0]};
      *(f32x2*)(gp + 16) = f32x2{dd[2][0], dd[3][0]};
    }
    BAR();
    f32x4 gu[NHW];
#pragma unroll
    for (int ww = 0; ww < NHW; ++ww) gu[ww] = *(const f32x4*)(lds + L_GP + (ww * 4 + (c >> 2)) * GP_S + (4 * r + (c & 3)) * 16);
    SB();
    const f32x4 GU = ((gu[0] + gu[1]) + (gu[2] + gu[3])) + gu[4];
    float av[2];
#pragma unroll
    for (int j = 0; j < 2; ++j) {
      const float g = rbf(GU[j]), u = rbf(GU[2 + j]);
      const float u2 = 2.0f * 0.7978845608028654f * LOG2E * (g + 0.044715f * g * g * g);
      av[j] = rbf(g * __builtin_amdgcn_rcpf(1.0f + __builtin_amdgcn_exp2f(-u2))) * u;
    }
    const unsigned ap = pk_bf16(av[0], av[1]);
    const v4u af = {dppu<0x00>(ap), dppu<0x55>(ap), dppu<0xAA>(ap), dppu<0xFF>(ap)};
    f32x4 z[8];
#pragma unroll
    for (int f = 0; f < 8; ++f) z[f] = mfma(af, BD[f], zero);
#pragma unroll
    for (int s = 0; s < 4; ++s)
      st128_sc1(v4u{as_u(z[2 * s][0]), tag, as_u(z[2 * s + 1][0]), tag}, rws, (r * 40 + (c >> 3)) * (NFFN * 128) + (c & 7) * 16, WS_Z + ((8 * w + 2 * s) * NFFN + fi) * 128);
  }
  STAMPC(w == 0, 6);

  if (is_red) {
    FRESH_IDS();
    v4u z0, z1;
    const int zs = WS_Z + (rb * 40 + cb) * (NFFN * 128);
    const bool has2 = htid + 320 < NFFN * 8;
    for (unsigned spin = 0;; ++spin) {
      z0 = __builtin_amdgcn_raw_buffer_load_b128(rws, htid * 16, zs, AUX_SC1);
      z1 = __builtin_amdgcn_raw_buffer_load_b128(rws, has2 ? htid * 16 + 5120 : htid * 16, zs, AUX_SC1);
      SB();
      const unsigned bad = TAGBAD(z0) | TAGBAD(z1);
      if (__all(bad == 0) || spin > SPIN_CAP) break;
      asm volatile("" ::: "memory");
    }
    STAMPC(w == 0, 14);
    float v0 = as_f(z0[0]) + (has2 ? as_f(z1[0]) : 0.f), v1 = as_f(z0[2]) + (has2 ? as_f(z1[2]) : 0.f);
    v0 += dppf<ROR8>(v0); v1 += dppf<ROR8>(v1);
    float vv = rs16(v0, v1);
    vv = rs32(vv, vv);
    if (lane < 32 && c < 8) *(float*)(lds + L_RED2 + w * 64 + c * 8 + r * 4) = vv;
    BAR();
    if (htid < 8) {
      f32x2 s = *(const f32x2*)(lds + L_RED2 + htid * 8);
#pragma unroll
      for (int ww = 1; ww < NHW; ++ww) s += *(const f32x2*)(lds + L_RED2 + ww * 64 + htid * 8);
      *(unsigned*)(out + ri * 16 + htid * 2) = pk_bf16(h0 + rbf(s.x), h1 + rbf(s.y));
      if (bid == 0 && htid == 0) __builtin_amdgcn_raw_buffer_store_b32(tag, rws, 0, WS_EPOCH, AUX_SC1);
    }
  }
  STAMPC(w == 0, 7);
}

extern "C" void kernel_launch(void* const* d_in, const int* in_sizes, int n_in, void* d_out, int out_size, void* d_ws, size_t ws_size, hipStream_t stream) {
  fused_block<<<NWG, NT, 0, stream>>>((const u16*)d_in[0], (const u16*)d_in[1], (const u16*)d_in[2], (const u16*)d_in[3], (const u16*)d_in[4],
                                      (const u16*)d_in[6], (const u16*)d_in[7], (const u16*)d_in[8], (u16*)d_out, d_ws);
}
```

```cpp
#define HOLD 0
#include <hip/hip_runtime.h>
#include <hip/hip_bf16.h>
#include <stdint.h>

typedef unsigned short u16;
typedef unsigned v4u __attribute__((ext_vector_type(4)));
typedef unsigned v2u __attribute__((ext_vector_type(2)));
typedef float f32x2 __attribute__((ext_vector_type(2)));
typedef float f32x4 __attribute__((ext_vector_type(4)));
typedef __bf16 bf16x2 __attribute__((ext_vector_type(2)));
typedef __bf16 bf16x8 __attribute__((ext_vector_type(8)));
typedef __amdgpu_buffer_rsrc_t rsrc_t;
#define GAS __attribute__((address_space(1)))
#define LAS __attribute__((address_space(3)))

#define LAGN 2
#define LAGF 0
#define SKEW 0
#define PW 2
#define PG 1
#define GU_FIRST 0
#define AUX_X 0
#define DPRE 8
#define QDELAY 0
#define AUX_QP 16
#define AUX_EP 16
#define ABL 0
#define PRE0 4
#define AUX_K 2
#define AUX_V 2
#define AUX_W 2
constexpr int NPRE = 4;

constexpr int NB = 4, DM = 640, NH = 32, SK = 4096, DH = 128, FF = 1728;
constexpr int NSW = 4, NHW = 5, NT = 64 * (NSW + NHW), NWG = 256;
constexpr int NRED = 160, NFFN = 54;
constexpr int WS_EPOCH = 0;
constexpr int WS_ML = 4096;
constexpr int WS_H = 8192;
constexpr int WS_Q = 65536;
constexpr int WS_Y = WS_Q + NB * NH * 4096;
constexpr int WS_Z = WS_Y + NB * 40 * 64 * 128;
constexpr int WS_END = WS_Z + NB * 40 * NFFN * 128;
constexpr int WS_STAMP = WS_END;
constexpr int AUX_NT = 2, AUX_SC1 = 16;
constexpr unsigned SPIN_CAP = 1u << 18;
constexpr float LOG2E = 1.4426950408889634f;

#define STAMPC(cond, i) do { } while (0)

__device__ __forceinline__ rsrc_t mk_rsrc(const void* p, int bytes) { return __builtin_amdgcn_make_buffer_rsrc((void*)p, 0, bytes, 0x00020000); }
__device__ __forceinline__ float as_f(unsigned u) { return __builtin_bit_cast(float, u); }
__device__ __forceinline__ unsigned as_u(float f) { return __builtin_bit_cast(unsigned, f); }
__device__ __forceinline__ unsigned pk_bf16(float lo, float hi) { f32x2 v = {lo, hi}; return __builtin_bit_cast(unsigned, __builtin_convertvector(v, bf16x2)); }
__device__ __forceinline__ float rbf(float x) { return as_f(pk_bf16(0.f, x)); }
__device__ __forceinline__ float lo_f(unsigned u) { return as_f(u << 16); }
__device__ __forceinline__ float hi_f(unsigned u) { return as_f(u & 0xffff0000u); }
__device__ __forceinline__ float dot2(unsigned a, unsigned b, float c) { return __builtin_amdgcn_fdot2_f32_bf16(__builtin_bit_cast(bf16x2, a), __builtin_bit_cast(bf16x2, b), c, false); }
__device__ __forceinline__ f32x4 mfma(v4u a, v4u b, f32x4 c) { return __builtin_amdgcn_mfma_f32_16x16x32_bf16(__builtin_bit_cast(bf16x8, a), __builtin_bit_cast(bf16x8, b), c, 0, 0, 0); }
template <int CTRL> __device__ __forceinline__ unsigned dppu(unsigned x) { return __builtin_amdgcn_mov_dpp((int)x, CTRL, 0xf, 0xf, true); }
template <int CTRL> __device__ __forceinline__ float dppf(float x) { return as_f(dppu<CTRL>(as_u(x))); }
constexpr int ROR1 = 0x121, ROR2 = 0x122, ROR4 = 0x124, ROR8 = 0x128, XOR1 = 0xB1, XOR2 = 0x4E, XOR7 = 0x141, BCAST = 0x150;
__device__ __forceinline__ unsigned perm_lo(unsigned a, unsigned b) { return __builtin_amdgcn_perm(b, a, 0x05040100u); }
__device__ __forceinline__ unsigned perm_hi(unsigned a, unsigned b) { return __builtin_amdgcn_perm(b, a, 0x07060302u); }
__device__ __forceinline__ float rs16(float a, float b) { auto s = __builtin_amdgcn_permlane16_swap(as_u(a), as_u(b), false, false); return as_f(s[0]) + as_f(s[1]); }
__device__ __forceinline__ float rs32(float a, float b) { auto s = __builtin_amdgcn_permlane32_swap(as_u(a), as_u(b), false, false); return as_f(s[0]) + as_f(s[1]); }
__device__ __forceinline__ float xrow_sum(float x) { x = rs16(x, x); return rs32(x, x); }
__device__ __forceinline__ float xrow_max(float x) {
  auto s = __builtin_amdgcn_permlane16_swap(as_u(x), as_u(x), false, false); x = fmaxf(as_f(s[0]), as_f(s[1]));
  auto t = __builtin_amdgcn_permlane32_swap(as_u(x), as_u(x), false, false); return fmaxf(as_f(t[0]), as_f(t[1]));
}
__device__ __forceinline__ void st128_sc1(v4u v, rsrc_t rs, int voff, int soff) {
  __builtin_amdgcn_raw_buffer_store_b128(v, rs, voff, soff, AUX_SC1);
  asm volatile("s_nop 1" :: "v"(v) : "memory");
}
__device__ __forceinline__ int fresh_lane() { int l; asm volatile("v_mbcnt_lo_u32_b32 %0, -1, 0\n\tv_mbcnt_hi_u32_b32 %0, -1, %0" : "=v"(l)); return l; }
__device__ __forceinline__ unsigned ln_pair(unsigned v, float mb, float rden) { return pk_bf16(rbf(lo_f(v) - mb) * rden, rbf(hi_f(v) - mb) * rden); }
#define SB() __builtin_amdgcn_sched_barrier(0)
#define VM0() do { SB(); asm volatile("s_waitcnt vmcnt(0)" ::: "memory"); SB(); } while (0)
#define BAR() do { asm volatile("s_waitcnt lgkmcnt(0)" ::: "memory"); __builtin_amdgcn_s_barrier(); asm volatile("" ::: "memory"); } while (0)
#define TAGBAD(g) (((g)[1] ^ tag) | ((g)[3] ^ tag))
#define FRESH_IDS() const int lane = fresh_lane(); const int r = lane >> 4, c = lane & 15, htid = w * 64 + lane; (void)r; (void)c; (void)htid

constexpr int L_FLAG = 0;
constexpr int L_XR = 16;
constexpr int L_ML = 64;
constexpr int L_Q = 128;
constexpr int L_RED = 384;
constexpr int L_RED2 = L_RED + 320;
constexpr int OP_S = 320;
constexpr int L_OP = L_RED2 + 320;
constexpr int GP_S = 384;
constexpr int L_GP = L_OP + 4 * OP_S;
constexpr int HN_S = 1344;
constexpr int L_HN = L_GP + 5 * 4 * GP_S;
constexpr int L_BG = L_HN + 4 * HN_S;
constexpr int L_WD = L_BG + 640 * 128;
constexpr int L_END = L_WD + 5 * 32 * 256;
static_assert(L_BG % 16 == 0 && L_HN % 16 == 0 && L_OP % 16 == 0 && L_GP % 16 == 0, "align");

__device__ __forceinline__ int rank_of(int bid) { return (bid & 7) + 8 * (bid >> 4); }
__device__ __forceinline__ int ffn_of_rank(int j) { const int s = j - 32; if (s < 0) return -1; const int k = (s & 7) + 8 * (s >> 4); return k < 27 ? 2 * k + ((s >> 3) & 1) : -1; }

__global__ void __launch_bounds__(NT, 3) fused_block(const u16* __restrict__ x, const u16* __restrict__ kc, const u16* __restrict__ vc,
                                                     const u16* __restrict__ wq, const u16* __restrict__ wo, const u16* __restrict__ wg,
                                                     const u16* __restrict__ wu, const u16* __restrict__ wd, u16* __restrict__ out, void* ws) {
  __shared__ __attribute__((aligned(16))) unsigned char lds[L_END + 256];
  const int wv = __builtin_amdgcn_readfirstlane(threadIdx.x >> 6);
  const int bid = blockIdx.x, xs = bid & 7, rr = bid >> 3;
  const int head = xs + 8 * (rr >> 3), b = (rr >> 1) & 3, half = rr & 1;
  STAMPC(wv == 0, 0);
  const unsigned long long t_begin = __builtin_amdgcn_s_memrealtime();
#define HOLD_UNTIL(k, us) do { if (HOLD == (k)) while (__builtin_amdgcn_s_memrealtime() - t_begin < (us) * 100ull) __builtin_amdgcn_s_sleep(1); } while (0)

  if (wv >= NHW) {
    const int w = wv - NHW;
    const int lane = threadIdx.x & 63, r = lane >> 4, c = lane & 15;
    const rsrc_t rk = mk_rsrc(kc, NB * NH * SK * DH * 2), rv = mk_rsrc(vc, NB * NH * SK * DH * 2);
    const int n0 = 32 + (ffn_of_rank(rank_of(bid)) >= 0 ? SKEW : 0);
    const int nr = half ? 64 - n0 : n0;
    const int kvbase = (b * NH + head) * (SK * DH * 2) + (half ? n0 * (NSW * 4096) : 0) + w * 4096;
    const int voff = lane * 16;
    STAMPC(w == 0, 13);
    if (PRE0 == 0) __builtin_amdgcn_s_barrier();
    STAMPC(w == 0, 31);
    v4u B[NPRE][8];
#define LD(i, so) (((i) & 4) ? __builtin_amdgcn_raw_buffer_load_b128(rv, voff, (so) + ((i) & 3) * 1024, AUX_V) : __builtin_amdgcn_raw_buffer_load_b128(rk, voff, (so) + ((i) & 3) * 1024, AUX_K))
#pragma unroll
    for (int t = 0; t < NPRE; ++t)
#pragma unroll
      for (int i = 0; i < 8; ++i) { B[t][i] = LD(i, kvbase + t * (NSW * 4096)); if (PRE0 && 8 * t + i + 1 == PRE0) { SB(); __builtin_amdgcn_s_barrier(); SB(); } if (DPRE && 8 * t + i + 1 >= DPRE) { SB(); asm volatile("s_waitcnt vmcnt(%0)" :: "i"(DPRE - 1) : "memory"); SB(); } if (i == 7 && t < 3) STAMPC(w == 0, 28 + t); }
    SB();
    STAMPC(w == 0, 11);
    {
      unsigned spin = 0;
      while (*(volatile LAS unsigned*)(LAS unsigned char*)(lds + L_FLAG + 4) == 0u && ++spin < (SPIN_CAP << 4)) __builtin_amdgcn_s_sleep(1);
      asm volatile("" ::: "memory");
    }
    HOLD_UNTIL(3, 40);
    const v4u q = *(const v4u*)(lds + L_Q + c * 16);
    STAMPC(w == 0, 1);
    float m = -INFINITY, l = 0.f, acc[8];
#pragma unroll
    for (int e = 0; e < 8; ++e) acc[e] = 0.f;

    int thr = (half && ffn_of_rank(rank_of(bid)) >= 0) ? LAGF : LAGN;
#define THROTTLE() asm volatile("s_cmp_lt_u32 %0, 1\n\ts_cbranch_scc1 1f\n\ts_waitcnt vmcnt(6)\n\ts_cmp_lt_u32 %0, 2\n\ts_cbranch_scc1 1f\n\ts_waitcnt vmcnt(5)\n1:" :: "s"(thr) : "scc", "memory")
#define REFILL(i, DO_CUR, CUR, DO_NXT, NXT) if (DO_NXT) { if (DO_CUR) THROTTLE(); B[0][i] = LD(i, NXT); SB(); }
#define QK(T, j, DO_CUR, CUR, DO_NXT, NXT) { \
      float d = dot2(B[T][j][0], q[0], 0.f); d = dot2(B[T][j][1], q[1], d); d = dot2(B[T][j][2], q[2], d); d = dot2(B[T][j][3], q[3], d); \
      asm volatile("" : "+v"(d)); sx[j] = d; SB(); REFILL(j, DO_CUR, CUR, DO_NXT, NXT) }
#define PV(T, j, DO_CUR, CUR, DO_NXT, NXT) { const unsigned pl = dppu<BCAST + 4 * (j)>(plo), ph = dppu<BCAST + 4 * (j)>(phi); \
      _Pragma("unroll") for (int d = 0; d < 4; ++d) { acc[2 * d] = dot2(B[T][4 + j][d], pl, acc[2 * d]); acc[2 * d + 1] = dot2(B[T][4 + j][d], ph, acc[2 * d + 1]); } \
      asm volatile("" : "+v"(acc[0]), "+v"(acc[1]), "+v"(acc[2]), "+v"(acc[3]), "+v"(acc[4]), "+v"(acc[5]), "+v"(acc[6]), "+v"(acc[7])); SB(); \
      REFILL(4 + j, DO_CUR, CUR, DO_NXT, NXT) }
#define TILE_BODY(T, DO_CUR, CUR, DO_NXT, NXT) { \
    float sx[4]; \
    QK(T, 0, DO_CUR, CUR, DO_NXT, NXT) QK(T, 1, DO_CUR, CUR, DO_NXT, NXT) QK(T, 2, DO_CUR, CUR, DO_NXT, NXT) QK(T, 3, DO_CUR, CUR, DO_NXT, NXT) \
    const float a0 = sx[0] + dppf<ROR8>(sx[0]), a2 = sx[2] + dppf<ROR8>(sx[2]), a1 = sx[1] + dppf<ROR8>(sx[1]), a3 = sx[3] + dppf<ROR8>(sx[3]); \
    const float b0 = (c & 8) ? a2 : a0, b1 = (c & 8) ? a3 : a1; \
    const float c0 = b0 + dppf<XOR7>(b0), c1 = b1 + dppf<XOR7>(b1); \
    float s = (c & 4) ? c1 : c0; \
    s += dppf<XOR2>(s); s += dppf<XOR1>(s); \
    s = rbf(rbf(s) * 0.08838834764831845f) * LOG2E; \
    float pm = fmaxf(s, dppf<ROR4>(s)); pm = fmaxf(pm, dppf<ROR8>(pm)); \
    const float mn = fmaxf(m, pm); \
    const float alpha = __builtin_amdgcn_exp2f(m - mn), p = __builtin_amdgcn_exp2f(s - mn); \
    l = fmaf(l, alpha, p); m = mn; \
    _Pragma("unroll") for (int e = 0; e < 8; ++e) acc[e] *= alpha; \
    const unsigned plo = pk_bf16(p, 0.f), phi = pk_bf16(0.f, p); \
    PV(T, 0, DO_CUR, CUR, DO_NXT, NXT) PV(T, 1, DO_CUR, CUR, DO_NXT, NXT) PV(T, 2, DO_CUR, CUR, DO_NXT, NXT) PV(T, 3, DO_CUR, CUR, DO_NXT, NXT) }

    int so = kvbase + NPRE * (NSW * 4096);
    TILE_BODY(0, false, 0, true, so)
    TILE_BODY(1, false, 0, false, 0)
    TILE_BODY(2, false, 0, false, 0)
    TILE_BODY(3, false, 0, false, 0)
    for (int it = NPRE; it < nr; ++it) {
      const bool more = it + 1 < nr;
      so = more ? so + NSW * 4096 : 0x40000000;
      thr = more ? thr : 0;
      TILE_BODY(0, true, 0, true, so)
      STAMPC(w == 0 && (it & 7) == 7, 20 + (it >> 3));
    }
    STAMPC(true, 16 + w);
    {
      const float Mw = xrow_max(m);
      const float f = __builtin_amdgcn_exp2f(m - Mw);
      float lr = l + dppf<ROR4>(l); lr += dppf<ROR8>(lr);
      float t[4];
#pragma unroll
      for (int j = 0; j < 4; ++j) t[j] = rs32(acc[j] * f, acc[j + 4] * f);
      *(unsigned*)(lds + L_OP + w * OP_S + (4 * c + r) * 4) = pk_bf16(rs16(t[0], t[2]), rs16(t[1], t[3]));
      const float Lw = xrow_sum(lr * f);
      if (lane == 0) *(f32x2*)(lds + L_ML + w * 8) = f32x2{Mw, Lw};
    }
    BAR();
    return;
  }

  const int w = wv;
  const int ri = half * 128 + rank_of(bid);
  __builtin_amdgcn_s_setprio(2);
  const rsrc_t rws = mk_rsrc(ws, WS_END);
  unsigned tag;
  {
    FRESH_IDS();
    const rsrc_t rx = mk_rsrc(x, ABL & 1 ? 0 : NB * DM * 2), rwq = mk_rsrc(wq, ABL & 2 ? 0 : DM * NH * DH * 2);
    v4u xs5[5];
#pragma unroll
    for (int n = 0; n < 5; ++n) xs5[n] = __builtin_amdgcn_raw_buffer_load_b128(rx, (c >> 2) * (DM * 2) + (4 * r + (c & 3)) * 16, n * 256, AUX_X);
    const v4u xa = __builtin_amdgcn_raw_buffer_load_b128(rx, (c >> 2) * (DM * 2) + r * 16, (160 * b + 32 * w) * 2, AUX_X);
    v2u Wq[8];
#pragma unroll
    for (int e = 0; e < 8; ++e) Wq[e] = __builtin_amdgcn_raw_buffer_load_b64(rwq, (8 * r) * 8192 + c * 8, (160 * b + 32 * w + e) * 8192 + head * 256 + half * 128, AUX_W);
    const unsigned epoch_v = __builtin_amdgcn_raw_buffer_load_b32(rws, ABL & 8 ? 0x40000000 : 0, WS_EPOCH, AUX_EP);
    const unsigned xres = __builtin_amdgcn_raw_buffer_load_b32(rx, (lane & 7) * 4, ri * 32, AUX_X);
    if (htid < 2) *(unsigned*)(lds + L_FLAG + htid * 4) = 0u;
    SB();
    BAR();
    SB();
    STAMPC(w == 0, 27);
    const float piv = dppf<0x00>(xrow_max(r == 0 ? lo_f(xs5[0][0]) : -INFINITY));
    float s1 = 0.f, s2 = 0.f;
#pragma unroll
    for (int n = 0; n < 5; ++n)
#pragma unroll
      for (int d = 0; d < 4; ++d) { const float d0 = lo_f(xs5[n][d]) - piv, d1 = hi_f(xs5[n][d]) - piv; s1 += d0 + d1; s2 = fmaf(d0, d0, s2); s2 = fmaf(d1, d1, s2); }
    s1 += dppf<XOR1>(s1); s2 += dppf<XOR1>(s2); s1 += dppf<XOR2>(s1); s2 += dppf<XOR2>(s2);
    s1 = xrow_sum(s1) * (1.0f / DM); s2 = xrow_sum(s2) * (1.0f / DM);
    const float var = fmaxf(fmaf(-s1, s1, s2), 0.f);
    const float mb = rbf(piv + s1), rden = __builtin_amdgcn_rcpf(rbf(__builtin_amdgcn_sqrtf(rbf(rbf(var) + 1e-5f))));
    const v4u af = {ln_pair(xa[0], mb, rden), ln_pair(xa[1], mb, rden), ln_pair(xa[2], mb, rden), ln_pair(xa[3], mb, rden)};
    STAMPC(w == 0, 8);
    const f32x4 zero = {0.f, 0.f, 0.f, 0.f};
    f32x4 dq[4];
#pragma unroll
    for (int f = 0; f < 4; ++f) {
      v4u bf;
#pragma unroll
      for (int d = 0; d < 4; ++d) bf[d] = (f & 1) ? perm_hi(Wq[2 * d][f >> 1], Wq[2 * d + 1][f >> 1]) : perm_lo(Wq[2 * d][f >> 1], Wq[2 * d + 1][f >> 1]);
      dq[f] = mfma(af, bf, zero);
    }
    *(f32x4*)(lds + L_GP + w * 1024 + lane * 16) = f32x4{dq[0][0], dq[1][0], dq[2][0], dq[3][0]};
    if (htid < 8) *(unsigned*)(lds + L_XR + htid * 4) = xres;
    tag = __builtin_amdgcn_readfirstlane(epoch_v) + 1u;
    asm volatile("s_waitcnt lgkmcnt(0)" ::: "memory");
    if (lane == 0) __hip_atomic_fetch_add((LAS unsigned*)(LAS unsigned char*)(lds + L_FLAG), 1u, __ATOMIC_RELAXED, __HIP_MEMORY_SCOPE_WORKGROUP);
    STAMPC(w == 0, 24);
    if (w < 2 && !(ABL & 4)) {
      unsigned spin = 0;
      while (*(volatile LAS unsigned*)(LAS unsigned char*)(lds + L_FLAG) < (unsigned)NHW && ++spin < (SPIN_CAP << 4)) { }
      asm volatile("" ::: "memory");
      f32x2 s = {0.f, 0.f};
#pragma unroll
      for (int ww = 0; ww < NHW; ++ww) s += *(const f32x2*)(lds + L_GP + ww * 1024 + htid * 8);
      st128_sc1(v4u{as_u(s.x), tag, as_u(s.y), tag}, rws, (htid >> 5) * (NH * 4096) + (htid & 31) * 16, WS_Q + head * 4096 + b * 1024 + half * 512);
      STAMPC(w == 0, 25);
    }
    if (w == 2) {
      v4u g[4];
      if (QDELAY) { unsigned spin = 0; while (*(volatile LAS unsigned*)(LAS unsigned char*)(lds + L_FLAG) < (unsigned)NHW && ++spin < (SPIN_CAP << 4)) { }
        __builtin_amdgcn_s_sleep(QDELAY); }
      if (ABL & 4) { for (int k = 0; k < 4; ++k) g[k] = v4u{0, 0, 0, 0}; __builtin_amdgcn_s_sleep(100); } else
      for (unsigned spin = 0;; ++spin) {
#pragma unroll
        for (int k = 0; k < 4; ++k) g[k] = __builtin_amdgcn_raw_buffer_load_b128(rws, lane * 16, WS_Q + (b * NH + head) * 4096 + k * 1024, AUX_QP);
        SB();
        const unsigned bad = TAGBAD(g[0]) | TAGBAD(g[1]) | TAGBAD(g[2]) | TAGBAD(g[3]);
        if (__all(bad == 0) || spin > SPIN_CAP) break;
        asm volatile("" ::: "memory");
      }
      *(unsigned*)(lds + L_Q + lane * 4) = pk_bf16((as_f(g[0][0]) + as_f(g[1][0])) + (as_f(g[2][0]) + as_f(g[3][0])), (as_f(g[0][2]) + as_f(g[1][2])) + (as_f(g[2][2]) + as_f(g[3][2])));
      asm volatile("s_waitcnt lgkmcnt(0)" ::: "memory");
      *(volatile LAS unsigned*)(LAS unsigned char*)(lds + L_FLAG + 4) = 1u;
      STAMPC(true, 26);
    } else {
      unsigned spin = 0;
      while (*(volatile LAS unsigned*)(LAS unsigned char*)(lds + L_FLAG + 4) == 0u && ++spin < (SPIN_CAP << 4)) __builtin_amdgcn_s_sleep(2);
      asm volatile("" ::: "memory");
    }
  }

  const bool is_red = ri < NRED;
  const int rb = ri / 40, cb = ri % 40;
  const int fi = half ? ffn_of_rank(rank_of(bid)) : -1;
  const bool is_ffn = fi >= 0;

#define STAGE_GU() if (is_ffn) { \
    const int ln = fresh_lane(); \
    const GAS unsigned char* gp = (const GAS unsigned char*)((ln & 4) ? wu : wg) + (size_t)((128 * w + (ln >> 3)) * (FF * 2) + fi * 64 + (ln & 3) * 16); \
    _Pragma("nounroll") for (int s = 0; s < 16 / PG; ++s) { \
      v4u GU[PG]; \
      _Pragma("unroll") for (int n = 0; n < PG; ++n) GU[n] = AUX_W ? __builtin_nontemporal_load((const GAS v4u*)(gp + 8 * n * (FF * 2))) : *(const GAS v4u*)(gp + 8 * n * (FF * 2)); \
      gp += 8 * PG * (FF * 2); \
      _Pragma("unroll") for (int n = 0; n < PG; ++n) *(v4u*)(lds + L_BG + (w * 16 + s * PG + n) * 1024 + ln * 16) = GU[n]; \
    } }
  {
    v4u F[32];
    {
      FRESH_IDS();
      const rsrc_t rwo = mk_rsrc(wo, DM * NH * DH * 2);
      const int vo = (8 * r) * (DM * 2) + c * 16, so = head * DH * (DM * 2) + w * 256;
#pragma unroll
      for (int s = 0; s < 4; ++s) {
        v4u T[8];
#pragma unroll
        for (int e = 0; e < 8; ++e) { T[e] = __builtin_amdgcn_raw_buffer_load_b128(rwo, vo, so + (32 * s + e) * (DM * 2), AUX_W); if (e % PW == PW - 1) VM0(); }
#pragma unroll
        for (int f = 0; f < 8; ++f)
#pragma unroll
          for (int d = 0; d < 4; ++d) F[8 * s + f][d] = (f & 1) ? perm_hi(T[2 * d][f >> 1], T[2 * d + 1][f >> 1]) : perm_lo(T[2 * d][f >> 1], T[2 * d + 1][f >> 1]);
#pragma unroll
        for (int f = 0; f < 8; ++f) asm volatile("" : "+v"(F[8 * s + f]));
        SB();
      }
    }
    STAMPC(w == 0, 9);
    STAGE_GU()
    STAMPC(w == 0, 10);
    HOLD_UNTIL(2, 80);
    BAR();
    STAMPC(w == 0, 2);
    FRESH_IDS();
    v4u a[4];
#pragma unroll
    for (int s = 0; s < 4; ++s) a[s] = *(const v4u*)(lds + L_OP + (c >> 2) * OP_S + s * 64 + r * 16);
    const f32x4 m01 = *(const f32x4*)(lds + L_ML), m23 = *(const f32x4*)(lds + L_ML + 16);
    const float mown = *(const float*)(lds + L_ML + r * 8);
    const f32x4 zero = {0.f, 0.f, 0.f, 0.f};
    f32x4 d[8];
#pragma unroll
    for (int f = 0; f < 8; ++f) d[f] = mfma(a[0], F[f], zero);
#pragma unroll
    for (int s = 1; s < 4; ++s)
#pragma unroll
      for (int f = 0; f < 8; ++f) d[f] = mfma(a[s], F[8 * s + f], d[f]);
    const float M = fmaxf(fmaxf(m01[0], m01[2]), fmaxf(m23[0], m23[2]));
    const float fo = __builtin_amdgcn_exp2f(mown - M);
    const int p = head * 2 + half;
    if (w == 0) {
      const float L = fmaf(__builtin_amdgcn_exp2f(m01[0] - M), m01[1], __builtin_amdgcn_exp2f(m01[2] - M) * m01[3]) + fmaf(__builtin_amdgcn_exp2f(m23[0] - M), m23[1], __builtin_amdgcn_exp2f(m23[2] - M) * m23[3]);
      st128_sc1(v4u{as_u(M), tag, as_u(L), tag}, rws, lane == 0 ? 0 : 0x40000000, WS_ML + (b * 64 + p) * 16);
    }
    float t[4];
#pragma unroll
    for (int j = 0; j < 4; ++j) t[j] = rs32(d[j][0] * fo, d[j + 4][0] * fo);
    const float y0 = rs16(t[0], t[2]), y1 = rs16(t[1], t[3]);
    st128_sc1(v4u{as_u(y0), tag, as_u(y1), tag}, rws, (c >> 1) * 8192 + ((c & 1) * 4 + r) * 16, WS_Y + ((b * 40 + 8 * w) * 64 + p) * 128);
  }
  STAMPC(w == 0, 3);

  float h0 = 0.f, h1 = 0.f;
  if (is_red) {
    FRESH_IDS();
    if (w < 4) {
      const int hp = htid >> 3, cp = htid & 7;
      const int yo = (2 * hp) * 128 + cp * 16, ys = WS_Y + (rb * 40 + cb) * 8192, mo = 2 * hp * 16, ms = WS_ML + rb * 1024;
      v4u y0, y1, m0, m1;
      for (unsigned spin = 0;; ++spin) {
        y0 = __builtin_amdgcn_raw_buffer_load_b128(rws, yo, ys, AUX_SC1);
        y1 = __builtin_amdgcn_raw_buffer_load_b128(rws, yo + 128, ys, AUX_SC1);
        m0 = __builtin_amdgcn_raw_buffer_load_b128(rws, mo, ms, AUX_SC1);
        m1 = __builtin_amdgcn_raw_buffer_load_b128(rws, mo + 16, ms, AUX_SC1);
        SB();
        const unsigned bad = TAGBAD(y0) | TAGBAD(y1) | TAGBAD(m0) | TAGBAD(m1);
        if (__all(bad == 0) || spin > SPIN_CAP) break;
        asm volatile("" ::: "memory");
      }
      STAMPC(w == 0, 12);
      const float M = fmaxf(as_f(m0[0]), as_f(m1[0]));
      const float w0 = __builtin_amdgcn_exp2f(as_f(m0[0]) - M), w1 = __builtin_amdgcn_exp2f(as_f(m1[0]) - M);
      const float inv = __builtin_amdgcn_rcpf(fmaf(w0, as_f(m0[2]), w1 * as_f(m1[2])));
      float v0 = fmaf(w0, as_f(y0[0]), w1 * as_f(y1[0])) * inv, v1 = fmaf(w0, as_f(y0[2]), w1 * as_f(y1[2])) * inv;
      v0 += dppf<ROR8>(v0); v1 += dppf<ROR8>(v1);
      float vv = rs16(v0, v1);
      vv = rs32(vv, vv);
      if (lane < 32 && c < 8) *(float*)(lds + L_RED + w * 64 + c * 8 + r * 4) = vv;
    }
    BAR();
    if (htid < 8) {
      f32x2 s = *(const f32x2*)(lds + L_RED + htid * 8);
#pragma unroll
      for (int ww = 1; ww < 4; ++ww) s += *(const f32x2*)(lds + L_RED + ww * 64 + htid * 8);
      const unsigned xres = *(const unsigned*)(lds + L_XR + htid * 4);
      const unsigned hp2 = pk_bf16(lo_f(xres) + rbf(s.x), hi_f(xres) + rbf(s.y));
      h0 = lo_f(hp2); h1 = hi_f(hp2);
      __builtin_amdgcn_raw_buffer_store_b64(v2u{hp2, tag}, rws, htid * 8, WS_H + (rb * 320 + cb * 8) * 8, AUX_SC1);
    }
  }
  STAMPC(w == 0, 4);

  if (is_ffn) {
    FRESH_IDS();
    v4u BG[16], BD[8];
    {
      const rsrc_t rd = mk_rsrc(wd, DM * FF * 2);
      v4u D[8];
#pragma unroll
      for (int n = 0; n < 8; ++n) D[n] = __builtin_amdgcn_raw_buffer_load_b128(rd, r * (DM * 2) + c * 16, (fi * 32 + 4 * n) * (DM * 2) + w * 256, AUX_W);
      SB();
#pragma unroll
      for (int s = 0; s < 4; ++s) {
        v2u t[8];
#pragma unroll
        for (int e = 0; e < 8; ++e) t[e] = *(const v2u*)(lds + L_BG + (128 * w + 32 * s + 8 * r + e) * 128 + c * 8);
#pragma unroll
        for (int d = 0; d < 4; ++d) {
          BG[4 * s + 0][d] = perm_lo(t[2 * d][0], t[2 * d + 1][0]); BG[4 * s + 1][d] = perm_hi(t[2 * d][0], t[2 * d + 1][0]);
          BG[4 * s + 2][d] = perm_lo(t[2 * d][1], t[2 * d + 1][1]); BG[4 * s + 3][d] = perm_hi(t[2 * d][1], t[2 * d + 1][1]);
        }
      }
#pragma unroll
      for (int n = 0; n < 16; ++n) asm volatile("" : "+v"(BG[n]));
      SB();
#pragma unroll
      for (int n = 0; n < 8; ++n) *(v4u*)(lds + L_WD + (w * 8 + n) * 1024 + lane * 16) = D[n];
      asm volatile("" ::: "memory");
#pragma unroll
      for (int s = 0; s < 4; ++s) {
        unsigned t[8];
#pragma unroll
        for (int e = 0; e < 8; ++e) t[e] = *(const unsigned*)(lds + L_WD + w * 8192 + (8 * r + e) * 256 + s * 64 + c * 4);
#pragma unroll
        for (int d = 0; d < 4; ++d) { BD[2 * s][d] = perm_lo(t[2 * d], t[2 * d + 1]); BD[2 * s + 1][d] = perm_hi(t[2 * d], t[2 * d + 1]); }
      }
#pragma unroll
      for (int n = 0; n < 8; ++n) asm volatile("" : "+v"(BD[n]));
    }
    STAMPC(w == 0, 15);
    if (w < 4) {
      v4u g0, g1, g2;
      const int hs = WS_H + w * 2560;
      for (unsigned spin = 0;; ++spin) {
        g0 = __builtin_amdgcn_raw_buffer_load_b128(rws, lane * 16, hs, AUX_SC1);
        g1 = __builtin_amdgcn_raw_buffer_load_b128(rws, lane * 16 + 1024, hs, AUX_SC1);
        g2 = __builtin_amdgcn_raw_buffer_load_b128(rws, (lane & 31) * 16 + 2048, hs, AUX_SC1);
        SB();
        const unsigned bad = TAGBAD(g0) | TAGBAD(g1) | TAGBAD(g2);
        if (__all(bad == 0) || spin > SPIN_CAP) break;
        asm volatile("" ::: "memory");
      }
      const unsigned hv[5] = {g0[0], g0[2], g1[0], g1[2], lane < 32 ? g2[0] : g2[2]};
      const float piv = as_f(__builtin_amdgcn_readfirstlane(hv[0] << 16));
      float s1 = 0.f, s2 = 0.f;
#pragma unroll
      for (int j = 0; j < 5; ++j) { const float d0 = lo_f(hv[j]) - piv, d1 = hi_f(hv[j]) - piv; s1 += d0 + d1; s2 = fmaf(d0, d0, s2); s2 = fmaf(d1, d1, s2); }
      float ss = rs32(s1, s2);
      ss = rs16(ss, ss);
      ss += dppf<ROR8>(ss); ss += dppf<ROR4>(ss); ss += dppf<ROR2>(ss); ss += dppf<ROR1>(ss);
      const float e1 = as_f(__builtin_amdgcn_readlane(as_u(ss), 0)) * (1.0f / DM), e2 = as_f(__builtin_amdgcn_readlane(as_u(ss), 32)) * (1.0f / DM);
      const float var = fmaxf(fmaf(-e1, e1, e2), 0.f);
      const float mb = rbf(piv + e1), rden = __builtin_amdgcn_rcpf(rbf(__builtin_amdgcn_sqrtf(rbf(rbf(var) + 1e-5f))));
      unsigned char* hrow = lds + L_HN + w * HN_S;
      *(v2u*)(hrow + lane * 8) = v2u{ln_pair(hv[0], mb, rden), ln_pair(hv[1], mb, rden)};
      *(v2u*)(hrow + 512 + lane * 8) = v2u{ln_pair(hv[2], mb, rden), ln_pair(hv[3], mb, rden)};
      *(unsigned*)(hrow + 1024 + (lane & 31) * 8 + (lane >> 5) * 4) = ln_pair(hv[4], mb, rden);
    }
    BAR();
    STAMPC(w == 0, 5);
    const f32x4 zero = {0.f, 0.f, 0.f, 0.f};
    f32x4 dd[4] = {zero, zero, zero, zero};
    v4u a[4];
#pragma unroll
    for (int s = 0; s < 4; ++s) a[s] = *(const v4u*)(lds + L_HN + (c >> 2) * HN_S + (128 * w + 32 * s + 8 * r) * 2);
    SB();
#pragma unroll
    for (int s = 0; s < 4; ++s)
#pragma unroll
      for (int f = 0; f < 4; ++f) dd[f] = mfma(a[s], BG[4 * s + f], dd[f]);
    {
      unsigned char* gp = lds + L_GP + (w * 4 + r) * GP_S + (c & 7) * 32 + (c >> 3) * 8;
      *(f32x2*)gp = f32x2{dd[0][0], dd[1][0]};
      *(f32x2*)(gp + 16) = f32x2{dd[2][0], dd[3][0]};
    }
    BAR();
    f32x4 gu[NHW];
#pragma unroll
    for (int ww = 0; ww < NHW; ++ww) gu[ww] = *(const f32x4*)(lds + L_GP + (ww * 4 + (c >> 2)) * GP_S + (4 * r + (c & 3)) * 16);
    SB();
    const f32x4 GU = ((gu[0] + gu[1]) + (gu[2] + gu[3])) + gu[4];
    float av[2];
#pragma unroll
    for (int j = 0; j < 2; ++j) {
      const float g = rbf(GU[j]), u = rbf(GU[2 + j]);
      const float u2 = 2.0f * 0.7978845608028654f * LOG2E * (g + 0.044715f * g * g * g);
      av[j] = rbf(g * __builtin_amdgcn_rcpf(1.0f + __builtin_amdgcn_exp2f(-u2))) * u;
    }
    const unsigned ap = pk_bf16(av[0], av[1]);
    const v4u af = {dppu<0x00>(ap), dppu<0x55>(ap), dppu<0xAA>(ap), dppu<0xFF>(ap)};
    f32x4 z[8];
#pragma unroll
    for (int f = 0; f < 8; ++f) z[f] = mfma(af, BD[f], zero);
#pragma unroll
    for (int s = 0; s < 4; ++s)
      st128_sc1(v4u{as_u(z[2 * s][0]), tag, as_u(z[2 * s + 1][0]), tag}, rws, (r * 40 + (c >> 3)) * (NFFN * 128) + (c & 7) * 16, WS_Z + ((8 * w + 2 * s) * NFFN + fi) * 128);
  }
  STAMPC(w == 0, 6);

  if (is_red) {
    FRESH_IDS();
    v4u z0, z1;
    const int zs = WS_Z + (rb * 40 + cb) * (NFFN * 128);
    const bool has2 = htid + 320 < NFFN * 8;
    for (unsigned spin = 0;; ++spin) {
      z0 = __builtin_amdgcn_raw_buffer_load_b128(rws, htid * 16, zs, AUX_SC1);
      z1 = __builtin_amdgcn_raw_buffer_load_b128(rws, has2 ? htid * 16 + 5120 : htid * 16, zs, AUX_SC1);
      SB();
      const unsigned bad = TAGBAD(z0) | TAGBAD(z1);
      if (__all(bad == 0) || spin > SPIN_CAP) break;
      asm volatile("" ::: "memory");
    }
    STAMPC(w == 0, 14);
    float v0 = as_f(z0[0]) + (has2 ? as_f(z1[0]) : 0.f), v1 = as_f(z0[2]) + (has2 ? as_f(z1[2]) : 0.f);
    v0 += dppf<ROR8>(v0); v1 += dppf<ROR8>(v1);
    float vv = rs16(v0, v1);
    vv = rs32(vv, vv);
    if (lane < 32 && c < 8) *(float*)(lds + L_RED2 + w * 64 + c * 8 + r * 4) = vv;
    BAR();
    if (htid < 8) {
      f32x2 s = *(const f32x2*)(lds + L_RED2 + htid * 8);
#pragma unroll
      for (int ww = 1; ww < NHW; ++ww) s += *(const f32x2*)(lds + L_RED2 + ww * 64 + htid * 8);
      *(unsigned*)(out + ri * 16 + htid * 2) = pk_bf16(h0 + rbf(s.x), h1 + rbf(s.y));
      if (bid == 0 && htid == 0) __builtin_amdgcn_raw_buffer_store_b32(tag, rws, 0, WS_EPOCH, AUX_SC1);
    }
  }
  STAMPC(w == 0, 7);
  HOLD_UNTIL(1, 100);
}

extern "C" void kernel_launch(void* const* d_in, const int* in_sizes, int n_in, void* d_out, int out_size, void* d_ws, size_t ws_size, hipStream_t stream) {
  fused_block<<<NWG, NT, 0, stream>>>((const u16*)d_in[0], (const u16*)d_in[1], (const u16*)d_in[2], (const u16*)d_in[3], (const u16*)d_in[4],
                                      (const u16*)d_in[6], (const u16*)d_in[7], (const u16*)d_in[8], (u16*)d_out, d_ws);
}
```

```cpp
#define HOLD 5
#include <hip/hip_runtime.h>
#include <hip/hip_bf16.h>
#include <stdint.h>

typedef unsigned short u16;
typedef unsigned v4u __attribute__((ext_vector_type(4)));
typedef unsigned v2u __attribute__((ext_vector_type(2)));
typedef float f32x2 __attribute__((ext_vector_type(2)));
typedef float f32x4 __attribute__((ext_vector_type(4)));
typedef __bf16 bf16x2 __attribute__((ext_vector_type(2)));
typedef __bf16 bf16x8 __attribute__((ext_vector_type(8)));
typedef __amdgpu_buffer_rsrc_t rsrc_t;
#define GAS __attribute__((address_space(1)))
#define LAS __attribute__((address_space(3)))

#define LAGN 2
#define LAGF 0
#define SKEW 0
#define PW 2
#define PG 1
#define GU_FIRST 0
#define AUX_X 0
#define DPRE 8
#define QDELAY 0
#define AUX_QP 16
#define AUX_EP 16
#define ABL 0
#define PRE0 4
#define AUX_K 2
#define AUX_V 2
#define AUX_W 2
constexpr int NPRE = 4;

constexpr int NB = 4, DM = 640, NH = 32, SK = 4096, DH = 128, FF = 1728;
constexpr int NSW = 4, NHW = 5, NT = 64 * (NSW + NHW), NWG = 256;
constexpr int NRED = 160, NFFN = 54;
constexpr int WS_EPOCH = 0;
constexpr int WS_ML = 4096;
constexpr int WS_H = 8192;
constexpr int WS_Q = 65536;
constexpr int WS_Y = WS_Q + NB * NH * 4096;
constexpr int WS_Z = WS_Y + NB * 40 * 64 * 128;
constexpr int WS_END = WS_Z + NB * 40 * NFFN * 128;
constexpr int WS_STAMP = WS_END;
constexpr int AUX_NT = 2, AUX_SC1 = 16;
constexpr unsigned SPIN_CAP = 1u << 18;
constexpr float LOG2E = 1.4426950408889634f;

#define STAMPC(cond, i) do { } while (0)

__device__ __forceinline__ rsrc_t mk_rsrc(const void* p, int bytes) { return __builtin_amdgcn_make_buffer_rsrc((void*)p, 0, bytes, 0x00020000); }
__device__ __forceinline__ float as_f(unsigned u) { return __builtin_bit_cast(float, u); }
__device__ __forceinline__ unsigned as_u(float f) { return __builtin_bit_cast(unsigned, f); }
__device__ __forceinline__ unsigned pk_bf16(float lo, float hi) { f32x2 v = {lo, hi}; return __builtin_bit_cast(unsigned, __builtin_convertvector(v, bf16x2)); }
__device__ __forceinline__ float rbf(float x) { return as_f(pk_bf16(0.f, x)); }
__device__ __forceinline__ float lo_f(unsigned u) { return as_f(u << 16); }
__device__ __forceinline__ float hi_f(unsigned u) { return as_f(u & 0xffff0000u); }
__device__ __forceinline__ float dot2(unsigned a, unsigned b, float c) { return __builtin_amdgcn_fdot2_f32_bf16(__builtin_bit_cast(bf16x2, a), __builtin_bit_cast(bf16x2, b), c, false); }
__device__ __forceinline__ f32x4 mfma(v4u a, v4u b, f32x4 c) { return __builtin_amdgcn_mfma_f32_16x16x32_bf16(__builtin_bit_cast(bf16x8, a), __builtin_bit_cast(bf16x8, b), c, 0, 0, 0); }
template <int CTRL> __device__ __forceinline__ unsigned dppu(unsigned x) { return __builtin_amdgcn_mov_dpp((int)x, CTRL, 0xf, 0xf, true); }
template <int CTRL> __device__ __forceinline__ float dppf(float x) { return as_f(dppu<CTRL>(as_u(x))); }
constexpr int ROR1 = 0x121, ROR2 = 0x122, ROR4 = 0x124, ROR8 = 0x128, XOR1 = 0xB1, XOR2 = 0x4E, XOR7 = 0x141, BCAST = 0x150;
__device__ __forceinline__ unsigned perm_lo(unsigned a, unsigned b) { return __builtin_amdgcn_perm(b, a, 0x05040100u); }
__device__ __forceinline__ unsigned perm_hi(unsigned a, unsigned b) { return __builtin_amdgcn_perm(b, a, 0x07060302u); }
__device__ __forceinline__ float rs16(float a, float b) { auto s = __builtin_amdgcn_permlane16_swap(as_u(a), as_u(b), false, false); return as_f(s[0]) + as_f(s[1]); }
__device__ __forceinline__ float rs32(float a, float b) { auto s = __builtin_amdgcn_permlane32_swap(as_u(a), as_u(b), false, false); return as_f(s[0]) + as_f(s[1]); }
__device__ __forceinline__ float xrow_sum(float x) { x = rs16(x, x); return rs32(x, x); }
__device__ __forceinline__ float xrow_max(float x) {
  auto s = __builtin_amdgcn_permlane16_swap(as_u(x), as_u(x), false, false); x = fmaxf(as_f(s[0]), as_f(s[1]));
  auto t = __builtin_amdgcn_permlane32_swap(as_u(x), as_u(x), false, false); return fmaxf(as_f(t[0]), as_f(t[1]));
}
__device__ __forceinline__ void st128_sc1(v4u v, rsrc_t rs, int voff, int soff) {
  __builtin_amdgcn_raw_buffer_store_b128(v, rs, voff, soff, AUX_SC1);
  asm volatile("s_nop 1" :: "v"(v) : "memory");
}
__device__ __forceinline__ int fresh_lane() { int l; asm volatile("v_mbcnt_lo_u32_b32 %0, -1, 0\n\tv_mbcnt_hi_u32_b32 %0, -1, %0" : "=v"(l)); return l; }
__device__ __forceinline__ unsigned ln_pair(unsigned v, float mb, float rden) { return pk_bf16(rbf(lo_f(v) - mb) * rden, rbf(hi_f(v) - mb) * rden); }
#define SB() __builtin_amdgcn_sched_barrier(0)
#define VM0() do { SB(); asm volatile("s_waitcnt vmcnt(0)" ::: "memory"); SB(); } while (0)
#define BAR() do { asm volatile("s_waitcnt lgkmcnt(0)" ::: "memory"); __builtin_amdgcn_s_barrier(); asm volatile("" ::: "memory"); } while (0)
#define TAGBAD(g) (((g)[1] ^ tag) | ((g)[3] ^ tag))
#define FRESH_IDS() const int lane = fresh_lane(); const int r = lane >> 4, c = lane & 15, htid = w * 64 + lane; (void)r; (void)c; (void)htid

constexpr int L_FLAG = 0;
constexpr int L_XR = 16;
constexpr int L_ML = 64;
constexpr int L_Q = 128;
constexpr int L_RED = 384;
constexpr int L_RED2 = L_RED + 320;
constexpr int OP_S = 320;
constexpr int L_OP = L_RED2 + 320;
constexpr int GP_S = 384;
constexpr int L_GP = L_OP + 4 * OP_S;
constexpr int HN_S = 1344;
constexpr int L_HN = L_GP + 5 * 4 * GP_S;
constexpr int L_BG = L_HN + 4 * HN_S;
constexpr int L_WD = L_BG + 640 * 128;
constexpr int L_END = L_WD + 5 * 32 * 256;
static_assert(L_BG % 16 == 0 && L_HN % 16 == 0 && L_OP % 16 == 0 && L_GP % 16 == 0, "align");

__device__ __forceinline__ int rank_of(int bid) { return (bid & 7) + 8 * (bid >> 4); }
__device__ __forceinline__ int ffn_of_rank(int j) { const int s = j - 32; if (s < 0) return -1; const int k = (s & 7) + 8 * (s >> 4); return k < 27 ? 2 * k + ((s >> 3) & 1) : -1; }

__global__ void __launch_bounds__(NT, 3) fused_block(const u16* __restrict__ x, const u16* __restrict__ kc, const u16* __restrict__ vc,
                                                     const u16* __restrict__ wq, const u16* __restrict__ wo, const u16* __restrict__ wg,
                                                     const u16* __restrict__ wu, const u16* __restrict__ wd, u16* __restrict__ out, void* ws) {
  __shared__ __attribute__((aligned(16))) unsigned char lds[L_END + 256];
  const int wv = __builtin_amdgcn_readfirstlane(threadIdx.x >> 6);
  const int bid = blockIdx.x, xs = bid & 7, rr = bid >> 3;
  const int head = xs + 8 * (rr >> 3), b = (rr >> 1) & 3, half = rr & 1;
  STAMPC(wv == 0, 0);
  const unsigned long long t_begin = __builtin_amdgcn_s_memrealtime();
#define HOLD_UNTIL(k, us) do { if (HOLD == (k)) while (__builtin_amdgcn_s_memrealtime() - t_begin < (us) * 100ull) __builtin_amdgcn_s_sleep(1); } while (0)

  if (wv >= NHW) {
    const int w = wv - NHW;
    const int lane = threadIdx.x & 63, r = lane >> 4, c = lane & 15;
    const rsrc_t rk = mk_rsrc(kc, NB * NH * SK * DH * 2), rv = mk_rsrc(vc, NB * NH * SK * DH * 2);
    const int n0 = 32 + (ffn_of_rank(rank_of(bid)) >= 0 ? SKEW : 0);
    const int nr = half ? 64 - n0 : n0;
    const int kvbase = (b * NH + head) * (SK * DH * 2) + (half ? n0 * (NSW * 4096) : 0) + w * 4096;
    const int voff = lane * 16;
    STAMPC(w == 0, 13);
    if (PRE0 == 0) __builtin_amdgcn_s_barrier();
    STAMPC(w == 0, 31);
    v4u B[NPRE][8];
#define LD(i, so) (((i) & 4) ? __builtin_amdgcn_raw_buffer_load_b128(rv, voff, (so) + ((i) & 3) * 1024, AUX_V) : __builtin_amdgcn_raw_buffer_load_b128(rk, voff, (so) + ((i) & 3) * 1024, AUX_K))
#pragma unroll
    for (int t = 0; t < NPRE; ++t)
#pragma unroll
      for (int i = 0; i < 8; ++i) { B[t][i] = LD(i, kvbase + t * (NSW * 4096)); if (PRE0 && 8 * t + i + 1 == PRE0) { SB(); __builtin_amdgcn_s_barrier(); SB(); } if (DPRE && 8 * t + i + 1 >= DPRE) { SB(); asm volatile("s_waitcnt vmcnt(%0)" :: "i"(DPRE - 1) : "memory"); SB(); } if (i == 7 && t < 3) STAMPC(w == 0, 28 + t); }
    SB();
    STAMPC(w == 0, 11);
    {
      unsigned spin = 0;
      while (*(volatile LAS unsigned*)(LAS unsigned char*)(lds + L_FLAG + 4) == 0u && ++spin < (SPIN_CAP << 4)) __builtin_amdgcn_s_sleep(1);
      asm volatile("" ::: "memory");
    }
    HOLD_UNTIL(3, 40);
    const v4u q = *(const v4u*)(lds + L_Q + c * 16);
    STAMPC(w == 0, 1);
    float m = -INFINITY, l = 0.f, acc[8];
#pragma unroll
    for (int e = 0; e < 8; ++e) acc[e] = 0.f;

    int thr = (half && ffn_of_rank(rank_of(bid)) >= 0) ? LAGF : LAGN;
#define THROTTLE() asm volatile("s_cmp_lt_u32 %0, 1\n\ts_cbranch_scc1 1f\n\ts_waitcnt vmcnt(6)\n\ts_cmp_lt_u32 %0, 2\n\ts_cbranch_scc1 1f\n\ts_waitcnt vmcnt(5)\n1:" :: "s"(thr) : "scc", "memory")
#define REFILL(i, DO_CUR, CUR, DO_NXT, NXT) if (DO_NXT) { if (DO_CUR) THROTTLE(); B[0][i] = LD(i, NXT); SB(); }
#define QK(T, j, DO_CUR, CUR, DO_NXT, NXT) { \
      float d = dot2(B[T][j][0], q[0], 0.f); d = dot2(B[T][j][1], q[1], d); d = dot2(B[T][j][2], q[2], d); d = dot2(B[T][j][3], q[3], d); \
      asm volatile("" : "+v"(d)); sx[j] = d; SB(); REFILL(j, DO_CUR, CUR, DO_NXT, NXT) }
#define PV(T, j, DO_CUR, CUR, DO_NXT, NXT) { const unsigned pl = dppu<BCAST + 4 * (j)>(plo), ph = dppu<BCAST + 4 * (j)>(phi); \
      _Pragma("unroll") for (int d = 0; d < 4; ++d) { acc[2 * d] = dot2(B[T][4 + j][d], pl, acc[2 * d]); acc[2 * d + 1] = dot2(B[T][4 + j][d], ph, acc[2 * d + 1]); } \
      asm volatile("" : "+v"(acc[0]), "+v"(acc[1]), "+v"(acc[2]), "+v"(acc[3]), "+v"(acc[4]), "+v"(acc[5]), "+v"(acc[6]), "+v"(acc[7])); SB(); \
      REFILL(4 + j, DO_CUR, CUR, DO_NXT, NXT) }
#define TILE_BODY(T, DO_CUR, CUR, DO_NXT, NXT) { \
    float sx[4]; \
    QK(T, 0, DO_CUR, CUR, DO_NXT, NXT) QK(T, 1, DO_CUR, CUR, DO_NXT, NXT) QK(T, 2, DO_CUR, CUR, DO_NXT, NXT) QK(T, 3, DO_CUR, CUR, DO_NXT, NXT) \
    const float a0 = sx[0] + dppf<ROR8>(sx[0]), a2 = sx[2] + dppf<ROR8>(sx[2]), a1 = sx[1] + dppf<ROR8>(sx[1]), a3 = sx[3] + dppf<ROR8>(sx[3]); \
    const float b0 = (c & 8) ? a2 : a0, b1 = (c & 8) ? a3 : a1; \
    const float c0 = b0 + dppf<XOR7>(b0), c1 = b1 + dppf<XOR7>(b1); \
    float s = (c & 4) ? c1 : c0; \
    s += dppf<XOR2>(s); s += dppf<XOR1>(s); \
    s = rbf(rbf(s) * 0.08838834764831845f) * LOG2E; \
    float pm = fmaxf(s, dppf<ROR4>(s)); pm = fmaxf(pm, dppf<ROR8>(pm)); \
    const float mn = fmaxf(m, pm); \
    const float alpha = __builtin_amdgcn_exp2f(m - mn), p = __builtin_amdgcn_exp2f(s - mn); \
    l = fmaf(l, alpha, p); m = mn; \
    _Pragma("unroll") for (int e = 0; e < 8; ++e) acc[e] *= alpha; \
    const unsigned plo = pk_bf16(p, 0.f), phi = pk_bf16(0.f, p); \
    PV(T, 0, DO_CUR, CUR, DO_NXT, NXT) PV(T, 1, DO_CUR, CUR, DO_NXT, NXT) PV(T, 2, DO_CUR, CUR, DO_NXT, NXT) PV(T, 3, DO_CUR, CUR, DO_NXT, NXT) }

    int so = kvbase + NPRE * (NSW * 4096);
    TILE_BODY(0, false, 0, true, so)
    TILE_BODY(1, false, 0, false, 0)
    TILE_BODY(2, false, 0, false, 0)
    TILE_BODY(3, false, 0, false, 0)
    for (int it = NPRE; it < nr; ++it) {
      const bool more = it + 1 < nr;
      so = more ? so + NSW * 4096 : 0x40000000;
      thr = more ? thr : 0;
      TILE_BODY(0, true, 0, true, so)
      STAMPC(w == 0 && (it & 7) == 7, 20 + (it >> 3));
    }
    STAMPC(true, 16 + w);
    {
      const float Mw = xrow_max(m);
      const float f = __builtin_amdgcn_exp2f(m - Mw);
      float lr = l + dppf<ROR4>(l); lr += dppf<ROR8>(lr);
      float t[4];
#pragma unroll
      for (int j = 0; j < 4; ++j) t[j] = rs32(acc[j] * f, acc[j + 4] * f);
      *(unsigned*)(lds + L_OP + w * OP_S + (4 * c + r) * 4) = pk_bf16(rs16(t[0], t[2]), rs16(t[1], t[3]));
      const float Lw = xrow_sum(lr * f);
      if (lane == 0) *(f32x2*)(lds + L_ML + w * 8) = f32x2{Mw, Lw};
    }
    BAR();
    return;
  }

  const int w = wv;
  const int ri = half * 128 + rank_of(bid);
  __builtin_amdgcn_s_setprio(2);
  const rsrc_t rws = mk_rsrc(ws, WS_END);
  unsigned tag;
  {
    FRESH_IDS();
    const rsrc_t rx = mk_rsrc(x, ABL & 1 ? 0 : NB * DM * 2), rwq = mk_rsrc(wq, ABL & 2 ? 0 : DM * NH * DH * 2);
    v4u xs5[5];
#pragma unroll
    for (int n = 0; n < 5; ++n) xs5[n] = __builtin_amdgcn_raw_buffer_load_b128(rx, (c >> 2) * (DM * 2) + (4 * r + (c & 3)) * 16, n * 256, AUX_X);
    const v4u xa = __builtin_amdgcn_raw_buffer_load_b128(rx, (c >> 2) * (DM * 2) + r * 16, (160 * b + 32 * w) * 2, AUX_X);
    v2u Wq[8];
#pragma unroll
    for (int e = 0; e < 8; ++e) Wq[e] = __builtin_amdgcn_raw_buffer_load_b64(rwq, (8 * r) * 8192 + c * 8, (160 * b + 32 * w + e) * 8192 + head * 256 + half * 128, AUX_W);
    const unsigned epoch_v = __builtin_amdgcn_raw_buffer_load_b32(rws, ABL & 8 ? 0x40000000 : 0, WS_EPOCH, AUX_EP);
    const unsigned xres = __builtin_amdgcn_raw_buffer_load_b32(rx, (lane & 7) * 4, ri * 32, AUX_X);
    if (htid < 2) *(unsigned*)(lds + L_FLAG + htid * 4) = 0u;
    SB();
    BAR();
    SB();
    STAMPC(w == 0, 27);
    const float piv = dppf<0x00>(xrow_max(r == 0 ? lo_f(xs5[0][0]) : -INFINITY));
    float s1 = 0.f, s2 = 0.f;
#pragma unroll
    for (int n = 0; n < 5; ++n)
#pragma unroll
      for (int d = 0; d < 4; ++d) { const float d0 = lo_f(xs5[n][d]) - piv, d1 = hi_f(xs5[n][d]) - piv; s1 += d0 + d1; s2 = fmaf(d0, d0, s2); s2 = fmaf(d1, d1, s2); }
    s1 += dppf<XOR1>(s1); s2 += dppf<XOR1>(s2); s1 += dppf<XOR2>(s1); s2 += dppf<XOR2>(s2);
    s1 = xrow_sum(s1) * (1.0f / DM); s2 = xrow_sum(s2) * (1.0f / DM);
    const float var = fmaxf(fmaf(-s1, s1, s2), 0.f);
    const float mb = rbf(piv + s1), rden = __builtin_amdgcn_rcpf(rbf(__builtin_amdgcn_sqrtf(rbf(rbf(var) + 1e-5f))));
    const v4u af = {ln_pair(xa[0], mb, rden), ln_pair(xa[1], mb, rden), ln_pair(xa[2], mb, rden), ln_pair(xa[3], mb, rden)};
    STAMPC(w == 0, 8);
    const f32x4 zero = {0.f, 0.f, 0.f, 0.f};
    f32x4 dq[4];
#pragma unroll
    for (int f = 0; f < 4; ++f) {
      v4u bf;
#pragma unroll
      for (int d = 0; d < 4; ++d) bf[d] = (f & 1) ? perm_hi(Wq[2 * d][f >> 1], Wq[2 * d + 1][f >> 1]) : perm_lo(Wq[2 * d][f >> 1], Wq[2 * d + 1][f >> 1]);
      dq[f] = mfma(af, bf, zero);
    }
    *(f32x4*)(lds + L_GP + w * 1024 + lane * 16) = f32x4{dq[0][0], dq[1][0], dq[2][0], dq[3][0]};
    if (htid < 8) *(unsigned*)(lds + L_XR + htid * 4) = xres;
    tag = __builtin_amdgcn_readfirstlane(epoch_v) + 1u;
    asm volatile("s_waitcnt lgkmcnt(0)" ::: "memory");
    if (lane == 0) __hip_atomic_fetch_add((LAS unsigned*)(LAS unsigned char*)(lds + L_FLAG), 1u, __ATOMIC_RELAXED, __HIP_MEMORY_SCOPE_WORKGROUP);
    STAMPC(w == 0, 24);
    if (w < 2 && !(ABL & 4)) {
      unsigned spin = 0;
      while (*(volatile LAS unsigned*)(LAS unsigned char*)(lds + L_FLAG) < (unsigned)NHW && ++spin < (SPIN_CAP << 4)) { }
      asm volatile("" ::: "memory");
      f32x2 s = {0.f, 0.f};
#pragma unroll
      for (int ww = 0; ww < NHW; ++ww) s += *(const f32x2*)(lds + L_GP + ww * 1024 + htid * 8);
      st128_sc1(v4u{as_u(s.x), tag, as_u(s.y), tag}, rws, (htid >> 5) * (NH * 4096) + (htid & 31) * 16, WS_Q + head * 4096 + b * 1024 + half * 512);
      STAMPC(w == 0, 25);
    }
    if (w == 2) {
      v4u g[4];
      if (QDELAY) { unsigned spin = 0; while (*(volatile LAS unsigned*)(LAS unsigned char*)(lds + L_FLAG) < (unsigned)NHW && ++spin < (SPIN_CAP << 4)) { }
        __builtin_amdgcn_s_sleep(QDELAY); }
      if (ABL & 4) { for (int k = 0; k < 4; ++k) g[k] = v4u{0, 0, 0, 0}; __builtin_amdgcn_s_sleep(100); } else
      for (unsigned spin = 0;; ++spin) {
#pragma unroll
        for (int k = 0; k < 4; ++k) g[k] = __builtin_amdgcn_raw_buffer_load_b128(rws, lane * 16, WS_Q + (b * NH + head) * 4096 + k * 1024, AUX_QP);
        SB();
        const unsigned bad = TAGBAD(g[0]) | TAGBAD(g[1]) | TAGBAD(g[2]) | TAGBAD(g[3]);
        if (__all(bad == 0) || spin > SPIN_CAP) break;
        asm volatile("" ::: "memory");
      }
      *(unsigned*)(lds + L_Q + lane * 4) = pk_bf16((as_f(g[0][0]) + as_f(g[1][0])) + (as_f(g[2][0]) + as_f(g[3][0])), (as_f(g[0][2]) + as_f(g[1][2])) + (as_f(g[2][2]) + as_f(g[3][2])));
      asm volatile("s_waitcnt lgkmcnt(0)" ::: "memory");
      *(volatile LAS unsigned*)(LAS unsigned char*)(lds + L_FLAG + 4) = 1u;
      STAMPC(true, 26);
    } else {
      unsigned spin = 0;
      while (*(volatile LAS unsigned*)(LAS unsigned char*)(lds + L_FLAG + 4) == 0u && ++spin < (SPIN_CAP << 4)) __builtin_amdgcn_s_sleep(2);
      asm volatile("" ::: "memory");
    }
  }

  const bool is_red = ri < NRED;
  const int rb = ri / 40, cb = ri % 40;
  const int fi = half ? ffn_of_rank(rank_of(bid)) : -1;
  const bool is_ffn = fi >= 0;

#define STAGE_GU() if (is_ffn) { \
    const int ln = fresh_lane(); \
    const GAS unsigned char* gp = (const GAS unsigned char*)((ln & 4) ? wu : wg) + (size_t)((128 * w + (ln >> 3)) * (FF * 2) + fi * 64 + (ln & 3) * 16); \
    _Pragma("nounroll") for (int s = 0; s < 16 / PG; ++s) { \
      v4u GU[PG]; \
      _Pragma("unroll") for (int n = 0; n < PG; ++n) GU[n] = AUX_W ? __builtin_nontemporal_load((const GAS v4u*)(gp + 8 * n * (FF * 2))) : *(const GAS v4u*)(gp + 8 * n * (FF * 2)); \
      gp += 8 * PG * (FF * 2); \
      _Pragma("unroll") for (int n = 0; n < PG; ++n) *(v4u*)(lds + L_BG + (w * 16 + s * PG + n) * 1024 + ln * 16) = GU[n]; \
    } }
  {
    v4u F[32];
    {
      FRESH_IDS();
      const rsrc_t rwo = mk_rsrc(wo, DM * NH * DH * 2);
      const int vo = (8 * r) * (DM * 2) + c * 16, so = head * DH * (DM * 2) + w * 256;
#pragma unroll
      for (int s = 0; s < 4; ++s) {
        v4u T[8];
#pragma unroll
        for (int e = 0; e < 8; ++e) { T[e] = __builtin_amdgcn_raw_buffer_load_b128(rwo, vo, so + (32 * s + e) * (DM * 2), AUX_W); if (e % PW == PW - 1) VM0(); }
#pragma unroll
        for (int f = 0; f < 8; ++f)
#pragma unroll
          for (int d = 0; d < 4; ++d) F[8 * s + f][d] = (f & 1) ? perm_hi(T[2 * d][f >> 1], T[2 * d + 1][f >> 1]) : perm_lo(T[2 * d][f >> 1], T[2 * d + 1][f >> 1]);
#pragma unroll
        for (int f = 0; f < 8; ++f) asm volatile("" : "+v"(F[8 * s + f]));
        SB();
      }
    }
    STAMPC(w == 0, 9);
    STAGE_GU()
    STAMPC(w == 0, 10);
    HOLD_UNTIL(2, 80);
    BAR();
    STAMPC(w == 0, 2);
    FRESH_IDS();
    v4u a[4];
#pragma unroll
    for (int s = 0; s < 4; ++s) a[s] = *(const v4u*)(lds + L_OP + (c >> 2) * OP_S + s * 64 + r * 16);
    const f32x4 m01 = *(const f32x4*)(lds + L_ML), m23 = *(const f32x4*)(lds + L_ML + 16);
    const float mown = *(const float*)(lds + L_ML + r * 8);
    const f32x4 zero = {0.f, 0.f, 0.f, 0.f};
    f32x4 d[8];
#pragma unroll
    for (int f = 0; f < 8; ++f) d[f] = mfma(a[0], F[f], zero);
#pragma unroll
    for (int s = 1; s < 4; ++s)
#pragma unroll
      for (int f = 0; f < 8; ++f) d[f] = mfma(a[s], F[8 * s + f], d[f]);
    const float M = fmaxf(fmaxf(m01[0], m01[2]), fmaxf(m23[0], m23[2]));
    const float fo = __builtin_amdgcn_exp2f(mown - M);
    const int p = head * 2 + half;
    if (w == 0) {
      const float L = fmaf(__builtin_amdgcn_exp2f(m01[0] - M), m01[1], __builtin_amdgcn_exp2f(m01[2] - M) * m01[3]) + fmaf(__builtin_amdgcn_exp2f(m23[0] - M), m23[1], __builtin_amdgcn_exp2f(m23[2] - M) * m23[3]);
      st128_sc1(v4u{as_u(M), tag, as_u(L), tag}, rws, lane == 0 ? 0 : 0x40000000, WS_ML + (b * 64 + p) * 16);
    }
    float t[4];
#pragma unroll
    for (int j = 0; j < 4; ++j) t[j] = rs32(d[j][0] * fo, d[j + 4][0] * fo);
    const float y0 = rs16(t[0], t[2]), y1 = rs16(t[1], t[3]);
    st128_sc1(v4u{as_u(y0), tag, as_u(y1), tag}, rws, (c >> 1) * 8192 + ((c & 1) * 4 + r) * 16, WS_Y + ((b * 40 + 8 * w) * 64 + p) * 128);
  }
  STAMPC(w == 0, 3);

  float h0 = 0.f, h1 = 0.f;
  if (is_red) {
    FRESH_IDS();
    if (w < 4) {
      const int hp = htid >> 3, cp = htid & 7;
      const int yo = (2 * hp) * 128 + cp * 16, ys = WS_Y + (rb * 40 + cb) * 8192, mo = 2 * hp * 16, ms = WS_ML + rb * 1024;
      v4u y0, y1, m0, m1;
      for (unsigned spin = 0;; ++spin) {
        y0 = __builtin_amdgcn_raw_buffer_load_b128(rws, yo, ys, AUX_SC1);
        y1 = __builtin_amdgcn_raw_buffer_load_b128(rws, yo + 128, ys, AUX_SC1);
        m0 = __builtin_amdgcn_raw_buffer_load_b128(rws, mo, ms, AUX_SC1);
        m1 = __builtin_amdgcn_raw_buffer_load_b128(rws, mo + 16, ms, AUX_SC1);
        SB();
        const unsigned bad = TAGBAD(y0) | TAGBAD(y1) | TAGBAD(m0) | TAGBAD(m1);
        if (__all(bad == 0) || spin > SPIN_CAP) break;
        asm volatile("" ::: "memory");
      }
      STAMPC(w == 0, 12);
      const float M = fmaxf(as_f(m0[0]), as_f(m1[0]));
      const float w0 = __builtin_amdgcn_exp2f(as_f(m0[0]) - M), w1 = __builtin_amdgcn_exp2f(as_f(m1[0]) - M);
      const float inv = __builtin_amdgcn_rcpf(fmaf(w0, as_f(m0[2]), w1 * as_f(m1[2])));
      float v0 = fmaf(w0, as_f(y0[0]), w1 * as_f(y1[0])) * inv, v1 = fmaf(w0, as_f(y0[2]), w1 * as_f(y1[2])) * inv;
      v0 += dppf<ROR8>(v0); v1 += dppf<ROR8>(v1);
      float vv = rs16(v0, v1);
      vv = rs32(vv, vv);
      if (lane < 32 && c < 8) *(float*)(lds + L_RED + w * 64 + c * 8 + r * 4) = vv;
    }
    BAR();
    if (htid < 8) {
      f32x2 s = *(const f32x2*)(lds + L_RED + htid * 8);
#pragma unroll
      for (int ww = 1; ww < 4; ++ww) s += *(const f32x2*)(lds + L_RED + ww * 64 + htid * 8);
      const unsigned xres = *(const unsigned*)(lds + L_XR + htid * 4);
      const unsigned hp2 = pk_bf16(lo_f(xres) + rbf(s.x), hi_f(xres) + rbf(s.y));
      h0 = lo_f(hp2); h1 = hi_f(hp2);
      __builtin_amdgcn_raw_buffer_store_b64(v2u{hp2, tag}, rws, htid * 8, WS_H + (rb * 320 + cb * 8) * 8, AUX_SC1);
    }
  }
  STAMPC(w == 0, 4);

  if (is_ffn) {
    FRESH_IDS();
    v4u BG[16], BD[8];
    {
      const rsrc_t rd = mk_rsrc(wd, DM * FF * 2);
      v4u D[8];
#pragma unroll
      for (int n = 0; n < 8; ++n) D[n] = __builtin_amdgcn_raw_buffer_load_b128(rd, r * (DM * 2) + c * 16, (fi * 32 + 4 * n) * (DM * 2) + w * 256, AUX_W);
      SB();
#pragma unroll
      for (int s = 0; s < 4; ++s) {
        v2u t[8];
#pragma unroll
        for (int e = 0; e < 8; ++e) t[e] = *(const v2u*)(lds + L_BG + (128 * w + 32 * s + 8 * r + e) * 128 + c * 8);
#pragma unroll
        for (int d = 0; d < 4; ++d) {
          BG[4 * s + 0][d] = perm_lo(t[2 * d][0], t[2 * d + 1][0]); BG[4 * s + 1][d] = perm_hi(t[2 * d][0], t[2 * d + 1][0]);
          BG[4 * s + 2][d] = perm_lo(t[2 * d][1], t[2 * d + 1][1]); BG[4 * s + 3][d] = perm_hi(t[2 * d][1], t[2 * d + 1][1]);
        }
      }
#pragma unroll
      for (int n = 0; n < 16; ++n) asm volatile("" : "+v"(BG[n]));
      SB();
#pragma unroll
      for (int n = 0; n < 8; ++n) *(v4u*)(lds + L_WD + (w * 8 + n) * 1024 + lane * 16) = D[n];
      asm volatile("" ::: "memory");
#pragma unroll
      for (int s = 0; s < 4; ++s) {
        unsigned t[8];
#pragma unroll
        for (int e = 0; e < 8; ++e) t[e] = *(const unsigned*)(lds + L_WD + w * 8192 + (8 * r + e) * 256 + s * 64 + c * 4);
#pragma unroll
        for (int d = 0; d < 4; ++d) { BD[2 * s][d] = perm_lo(t[2 * d], t[2 * d + 1]); BD[2 * s + 1][d] = perm_hi(t[2 * d], t[2 * d + 1]); }
      }
#pragma unroll
      for (int n = 0; n < 8; ++n) asm volatile("" : "+v"(BD[n]));
    }
    STAMPC(w == 0, 15);
    if (w < 4) {
      v4u g0, g1, g2;
      const int hs = WS_H + w * 2560;
      for (unsigned spin = 0;; ++spin) {
        g0 = __builtin_amdgcn_raw_buffer_load_b128(rws, lane * 16, hs, AUX_SC1);
        g1 = __builtin_amdgcn_raw_buffer_load_b128(rws, lane * 16 + 1024, hs, AUX_SC1);
        g2 = __builtin_amdgcn_raw_buffer_load_b128(rws, (lane & 31) * 16 + 2048, hs, AUX_SC1);
        SB();
        const unsigned bad = TAGBAD(g0) | TAGBAD(g1) | TAGBAD(g2);
        if (__all(bad == 0) || spin > SPIN_CAP) break;
        asm volatile("" ::: "memory");
      }
      const unsigned hv[5] = {g0[0], g0[2], g1[0], g1[2], lane < 32 ? g2[0] : g2[2]};
      const float piv = as_f(__builtin_amdgcn_readfirstlane(hv[0] << 16));
      float s1 = 0.f, s2 = 0.f;
#pragma unroll
      for (int j = 0; j < 5; ++j) { const float d0 = lo_f(hv[j]) - piv, d1 = hi_f(hv[j]) - piv; s1 += d0 + d1; s2 = fmaf(d0, d0, s2); s2 = fmaf(d1, d1, s2); }
      float ss = rs32(s1, s2);
      ss = rs16(ss, ss);
      ss += dppf<ROR8>(ss); ss += dppf<ROR4>(ss); ss += dppf<ROR2>(ss); ss += dppf<ROR1>(ss);
      const float e1 = as_f(__builtin_amdgcn_readlane(as_u(ss), 0)) * (1.0f / DM), e2 = as_f(__builtin_amdgcn_readlane(as_u(ss), 32)) * (1.0f / DM);
      const float var = fmaxf(fmaf(-e1, e1, e2), 0.f);
      const float mb = rbf(piv + e1), rden = __builtin_amdgcn_rcpf(rbf(__builtin_amdgcn_sqrtf(rbf(rbf(var) + 1e-5f))));
      unsigned char* hrow = lds + L_HN + w * HN_S;
      *(v2u*)(hrow + lane * 8) = v2u{ln_pair(hv[0], mb, rden), ln_pair(hv[1], mb, rden)};
      *(v2u*)(hrow + 512 + lane * 8) = v2u{ln_pair(hv[2], mb, rden), ln_pair(hv[3], mb, rden)};
      *(unsigned*)(hrow + 1024 + (lane & 31) * 8 + (lane >> 5) * 4) = ln_pair(hv[4], mb, rden);
    }
    BAR();
    STAMPC(w == 0, 5);
    const f32x4 zero = {0.f, 0.f, 0.f, 0.f};
    f32x4 dd[4] = {zero, zero, zero, zero};
    v4u a[4];
#pragma unroll
    for (int s = 0; s < 4; ++s) a[s] = *(const v4u*)(lds + L_HN + (c >> 2) * HN_S + (128 * w + 32 * s + 8 * r) * 2);
    SB();
#pragma unroll
    for (int s = 0; s < 4; ++s)
#pragma unroll
      for (int f = 0; f < 4; ++f) dd[f] = mfma(a[s], BG[4 * s + f], dd[f]);
    {
      unsigned char* gp = lds + L_GP + (w * 4 + r) * GP_S + (c & 7) * 32 + (c >> 3) * 8;
      *(f32x2*)gp = f32x2{dd[0][0], dd[1][0]};
      *(f32x2*)(gp + 16) = f32x2{dd[2][0], dd[3][0]};
    }
    BAR();
    f32x4 gu[NHW];
#pragma unroll
    for (int ww = 0; ww < NHW; ++ww) gu[ww] = *(const f32x4*)(lds + L_GP + (ww * 4 + (c >> 2)) * GP_S + (4 * r + (c & 3)) * 16);
    SB();
    const f32x4 GU = ((gu[0] + gu[1]) + (gu[2] + gu[3])) + gu[4];
    float av[2];
#pragma unroll
    for (int j = 0; j < 2; ++j) {
      const float g = rbf(GU[j]), u = rbf(GU[2 + j]);
      const float u2 = 2.0f * 0.7978845608028654f * LOG2E * (g + 0.044715f * g * g * g);
      av[j] = rbf(g * __builtin_amdgcn_rcpf(1.0f + __builtin_amdgcn_exp2f(-u2))) * u;
    }
    const unsigned ap = pk_bf16(av[0], av[1]);
    const v4u af = {dppu<0x00>(ap), dppu<0x55>(ap), dppu<0xAA>(ap), dppu<0xFF>(ap)};
    f32x4 z[8];
#pragma unroll
    for (int f = 0; f < 8; ++f) z[f] = mfma(af, BD[f], zero);
#pragma unroll
    for (int s = 0; s < 4; ++s)
      st128_sc1(v4u{as_u(z[2 * s][0]), tag, as_u(z[2 * s + 1][0]), tag}, rws, (r * 40 + (c >> 3)) * (NFFN * 128) + (c & 7) * 16, WS_Z + ((8 * w + 2 * s) * NFFN + fi) * 128);
  }
  STAMPC(w == 0, 6);

  if (is_red) {
    FRESH_IDS();
    v4u z0, z1;
    const int zs = WS_Z + (rb * 40 + cb) * (NFFN * 128);
    const bool has2 = htid + 320 < NFFN * 8;
    for (unsigned spin = 0;; ++spin) {
      z0 = __builtin_amdgcn_raw_buffer_load_b128(rws, htid * 16, zs, AUX_SC1);
      z1 = __builtin_amdgcn_raw_buffer_load_b128(rws, has2 ? htid * 16 + 5120 : htid * 16, zs, AUX_SC1);
      SB();
      const unsigned bad = TAGBAD(z0) | TAGBAD(z1);
      if (__all(bad == 0) || spin > SPIN_CAP) break;
      asm volatile("" ::: "memory");
    }
    STAMPC(w == 0, 14);
    float v0 = as_f(z0[0]) + (has2 ? as_f(z1[0]) : 0.f), v1 = as_f(z0[2]) + (has2 ? as_f(z1[2]) : 0.f);
    v0 += dppf<ROR8>(v0); v1 += dppf<ROR8>(v1);
    float vv = rs16(v0, v1);
    vv = rs32(vv, vv);
    if (lane < 32 && c < 8) *(float*)(lds + L_RED2 + w * 64 + c * 8 + r * 4) = vv;
    BAR();
    if (htid < 8) {
      f32x2 s = *(const f32x2*)(lds + L_RED2 + htid * 8);
#pragma unroll
      for (int ww = 1; ww < NHW; ++ww) s += *(const f32x2*)(lds + L_RED2 + ww * 64 + htid * 8);
      *(unsigned*)(out + ri * 16 + htid * 2) = pk_bf16(h0 + rbf(s.x), h1 + rbf(s.y));
      if (bid == 0 && htid == 0) __builtin_amdgcn_raw_buffer_store_b32(tag, rws, 0, WS_EPOCH, AUX_SC1);
    }
  }
  STAMPC(w == 0, 7);
  HOLD_UNTIL(1, 100);
  if (HOLD == 4 || HOLD == 5) {
    const int src = HOLD == 4 ? 0 : 255;
    if (bid == src && w == 0) __builtin_amdgcn_raw_buffer_store_b64(v2u{(unsigned)t_begin, tag}, rws, 0, 1024, AUX_SC1);
    v2u g; unsigned spin = 0;
    do { g = __builtin_amdgcn_raw_buffer_load_b64(rws, 0, 1024, AUX_SC1); __builtin_amdgcn_s_sleep(8); } while (__builtin_amdgcn_readfirstlane(g[1]) != tag && ++spin < SPIN_CAP);
    const unsigned t0 = __builtin_amdgcn_readfirstlane(g[0]);
    while ((unsigned)__builtin_amdgcn_s_memrealtime() - t0 < 10000u) __builtin_amdgcn_s_sleep(1);
  }
  if (HOLD == 6 && bid == 0) while (__builtin_amdgcn_s_memrealtime() - t_begin < 10000ull) __builtin_amdgcn_s_sleep(1);
}

extern "C" void kernel_launch(void* const* d_in, const int* in_sizes, int n_in, void* d_out, int out_size, void* d_ws, size_t ws_size, hipStream_t stream) {
  fused_block<<<NWG, NT, 0, stream>>>((const u16*)d_in[0], (const u16*)d_in[1], (const u16*)d_in[2], (const u16*)d_in[3], (const u16*)d_in[4],
                                      (const u16*)d_in[6], (const u16*)d_in[7], (const u16*)d_in[8], (u16*)d_out, d_ws);
}
```

```cpp
#define NAUX 4
#define LAGN 1
#define LAGPQ 1
#define LAGP 0
#define LAGQ 0
#include <hip/hip_runtime.h>
#include <hip/hip_bf16.h>
#include <stdint.h>

typedef unsigned short u16;
typedef unsigned v4u __attribute__((ext_vector_type(4)));
typedef unsigned v2u __attribute__((ext_vector_type(2)));
typedef float f32x2 __attribute__((ext_vector_type(2)));
typedef float f32x4 __attribute__((ext_vector_type(4)));
typedef __bf16 bf16x2 __attribute__((ext_vector_type(2)));
typedef __bf16 bf16x8 __attribute__((ext_vector_type(8)));
typedef __amdgpu_buffer_rsrc_t rsrc_t;
#define GAS __attribute__((address_space(1)))
#define LAS __attribute__((address_space(3)))

#define LAGF 0
#define SKEW 0
#define PW 4
#define PG 2
#define GU_FIRST 0
#define AUX_X 0
#define DPRE 8
#define QDELAY 0
#define AUX_QP 16
#define AUX_EP 16
#define ABL 0
#define PRE0 4
#define AUX_K 2
#define AUX_V 2
#define AUX_W 0
#define AUX_OUT 0
#define PWN 2
#define AUX_WQ 2
#define SHF 4
#define SHQ 4
#define WAITQ 0
constexpr int NPRE = 4;

constexpr int NB = 4, DM = 640, NH = 32, SK = 4096, DH = 128, FF = 1728;
constexpr int NSW = 4, NHW = 5, NT = 64 * (NSW + NHW), NWG = 256;
constexpr int NRED = 160, NFFN = 54;
constexpr int WS_EPOCH = 0;
constexpr int WS_ML = 32768;
constexpr int WS_H = 8192;
constexpr int WS_Q = 65536;
constexpr int WS_Y = WS_Q + NB * NH * 512;
constexpr int WS_Z = WS_Y + NB * 40 * 64 * 128;
constexpr int WS_END = WS_Z + NB * 40 * NFFN * 128;
constexpr int WS_STAMP = WS_END;
constexpr int AUX_NT = 2, AUX_SC1 = 16;
constexpr unsigned SPIN_CAP = 1u << 18;
constexpr float LOG2E = 1.4426950408889634f;

#define STAMPC(cond, i) do { } while (0)

__device__ __forceinline__ rsrc_t mk_rsrc(const void* p, int bytes) { return __builtin_amdgcn_make_buffer_rsrc((void*)p, 0, bytes, 0x00020000); }
__device__ __forceinline__ float as_f(unsigned u) { return __builtin_bit_cast(float, u); }
__device__ __forceinline__ unsigned as_u(float f) { return __builtin_bit_cast(unsigned, f); }
__device__ __forceinline__ unsigned pk_bf16(float lo, float hi) { f32x2 v = {lo, hi}; return __builtin_bit_cast(unsigned, __builtin_convertvector(v, bf16x2)); }
__device__ __forceinline__ float rbf(float x) { return as_f(pk_bf16(0.f, x)); }
__device__ __forceinline__ float lo_f(unsigned u) { return as_f(u << 16); }
__device__ __forceinline__ float hi_f(unsigned u) { return as_f(u & 0xffff0000u); }
__device__ __forceinline__ float dot2(unsigned a, unsigned b, float c) { return __builtin_amdgcn_fdot2_f32_bf16(__builtin_bit_cast(bf16x2, a), __builtin_bit_cast(bf16x2, b), c, false); }
__device__ __forceinline__ f32x4 mfma(v4u a, v4u b, f32x4 c) { return __builtin_amdgcn_mfma_f32_16x16x32_bf16(__builtin_bit_cast(bf16x8, a), __builtin_bit_cast(bf16x8, b), c, 0, 0, 0); }
template <int CTRL> __device__ __forceinline__ unsigned dppu(unsigned x) { return __builtin_amdgcn_mov_dpp((int)x, CTRL, 0xf, 0xf, true); }
template <int CTRL> __device__ __forceinline__ float dppf(float x) { return as_f(dppu<CTRL>(as_u(x))); }
constexpr int ROR1 = 0x121, ROR2 = 0x122, ROR4 = 0x124, ROR8 = 0x128, XOR1 = 0xB1, XOR2 = 0x4E, XOR7 = 0x141, BCAST = 0x150;
__device__ __forceinline__ unsigned perm_lo(unsigned a, unsigned b) { return __builtin_amdgcn_perm(b, a, 0x05040100u); }
__device__ __forceinline__ unsigned perm_hi(unsigned a, unsigned b) { return __builtin_amdgcn_perm(b, a, 0x07060302u); }
__device__ __forceinline__ float rs16(float a, float b) { auto s = __builtin_amdgcn_permlane16_swap(as_u(a), as_u(b), false, false); return as_f(s[0]) + as_f(s[1]); }
__device__ __forceinline__ float rs32(float a, float b) { auto s = __builtin_amdgcn_permlane32_swap(as_u(a), as_u(b), false, false); return as_f(s[0]) + as_f(s[1]); }
__device__ __forceinline__ float xrow_sum(float x) { x = rs16(x, x); return rs32(x, x); }
__device__ __forceinline__ float xrow_max(float x) {
  auto s = __builtin_amdgcn_permlane16_swap(as_u(x), as_u(x), false, false); x = fmaxf(as_f(s[0]), as_f(s[1]));
  auto t = __builtin_amdgcn_permlane32_swap(as_u(x), as_u(x), false, false); return fmaxf(as_f(t[0]), as_f(t[1]));
}
__device__ __forceinline__ void st128_sc1(v4u v, rsrc_t rs, int voff, int soff) {
  __builtin_amdgcn_raw_buffer_store_b128(v, rs, voff, soff, AUX_SC1);
  asm volatile("s_nop 1" :: "v"(v) : "memory");
}
__device__ __forceinline__ int fresh_lane() { int l; asm volatile("v_mbcnt_lo_u32_b32 %0, -1, 0\n\tv_mbcnt_hi_u32_b32 %0, -1, %0" : "=v"(l)); return l; }
__device__ __forceinline__ unsigned ln_pair(unsigned v, float mb, float rden) { return pk_bf16(rbf(lo_f(v) - mb) * rden, rbf(hi_f(v) - mb) * rden); }
__device__ __forceinline__ f32x2 ln_stats(const unsigned (&hv)[5]) {
  const float piv = as_f(__builtin_amdgcn_readfirstlane(hv[0] << 16));
  float s1 = 0.f, s2 = 0.f;
#pragma unroll
  for (int j = 0; j < 5; ++j) { const float d0 = lo_f(hv[j]) - piv, d1 = hi_f(hv[j]) - piv; s1 += d0 + d1; s2 = fmaf(d0, d0, s2); s2 = fmaf(d1, d1, s2); }
  float ss = rs32(s1, s2);
  ss = rs16(ss, ss);
  ss += dppf<ROR8>(ss); ss += dppf<ROR4>(ss); ss += dppf<ROR2>(ss); ss += dppf<ROR1>(ss);
  const float e1 = as_f(__builtin_amdgcn_readlane(as_u(ss), 0)) * (1.0f / DM), e2 = as_f(__builtin_amdgcn_readlane(as_u(ss), 32)) * (1.0f / DM);
  const float var = fmaxf(fmaf(-e1, e1, e2), 0.f);
  return f32x2{rbf(piv + e1), __builtin_amdgcn_rcpf(rbf(__builtin_amdgcn_sqrtf(rbf(rbf(var) + 1e-5f))))};
}
#define SB() __builtin_amdgcn_sched_barrier(0)
#define VM0() do { SB(); asm volatile("s_waitcnt vmcnt(0)" ::: "memory"); SB(); } while (0)
#define BAR() do { asm volatile("s_waitcnt lgkmcnt(0)" ::: "memory"); __builtin_amdgcn_s_barrier(); asm volatile("" ::: "memory"); } while (0)
#define TAGBAD(g) (((g)[1] ^ tag) | ((g)[3] ^ tag))
#define FRESH_IDS() const int lane = fresh_lane(); const int r = lane >> 4, c = lane & 15, htid = w * 64 + lane; (void)r; (void)c; (void)htid

constexpr int L_FLAG = 0;
constexpr int L_ML = 64;
constexpr int L_Q = 128;
constexpr int OP_S = 320;
constexpr int L_ST = 384;
constexpr int L_OP = 448;
constexpr int GP_S = 384;
constexpr int L_GP = L_OP + 4 * OP_S;
constexpr int HN_S = 1344;
constexpr int L_HN = L_GP + 5 * 4 * GP_S;
constexpr int L_BG = L_HN + 4 * HN_S;
constexpr int L_F4 = L_BG + 640 * 128;
constexpr int L_SC = L_F4 + 32 * 1024;
constexpr int SC_S = 2176;
constexpr int L_END = L_SC + 4 * SC_S;
static_assert(L_BG % 16 == 0 && L_HN % 16 == 0 && L_OP % 16 == 0 && L_GP % 16 == 0, "align");
#define LFLAG(i) (*(volatile LAS unsigned*)(LAS unsigned char*)(lds + L_FLAG + 4 * (i)))
#define LWAIT(i, n, SLP) do { unsigned spin_ = 0; while (LFLAG(i) < (unsigned)(n) && ++spin_ < (SPIN_CAP << 4)) { if (SLP) __builtin_amdgcn_s_sleep(SLP); } asm volatile("" ::: "memory"); } while (0)
#define LADD(i) do { asm volatile("s_waitcnt lgkmcnt(0)" ::: "memory"); if (fresh_lane() == 0) __hip_atomic_fetch_add((LAS unsigned*)(LAS unsigned char*)(lds + L_FLAG + 4 * (i)), 1u, __ATOMIC_RELAXED, __HIP_MEMORY_SCOPE_WORKGROUP); asm volatile("" ::: "memory"); } while (0)

__device__ __forceinline__ int rank_of(int bid) { return (bid & 7) + 8 * (bid >> 4); }
__device__ __forceinline__ int ffn_of_rank(int j) { const int s = j - 64; if (s < 0) return -1; const int k = (s & 7) + 8 * (s >> 4); return k < 27 ? 2 * k + ((s >> 3) & 1) : -1; }

__global__ void __launch_bounds__(NT, 3) fused_block(const u16* __restrict__ x, const u16* __restrict__ kc, const u16* __restrict__ vc,
                                                     const u16* __restrict__ wq, const u16* __restrict__ wo, const u16* __restrict__ wg,
                                                     const u16* __restrict__ wu, const u16* __restrict__ wd, u16* __restrict__ out, void* ws) {
  __shared__ __attribute__((aligned(16))) unsigned char lds[L_END + 256];
  const int wv = __builtin_amdgcn_readfirstlane(threadIdx.x >> 6);
  const int bid = blockIdx.x, xs = bid & 7, rr = bid >> 3;
  const int head = xs + 8 * (rr >> 3), b = (rr >> 1) & 3, half = rr & 1;
  STAMPC(wv == 0, 0);
#define HOLD_UNTIL(k, us) do { } while (0)

  if (threadIdx.x < 16) *(unsigned*)(lds + L_FLAG + threadIdx.x * 4) = 0u;
  BAR();

  constexpr int STEP = 2 * NSW * 4096;
  const int rank = rank_of(bid);
  const bool pair_ffn = ffn_of_rank(rank) >= 0, is_q = !half && rank < 64;
  const int sw = wv >= NHW ? wv - NHW : wv;
  const int mv = pair_ffn ? (sw < SHF ? 1 : 0) : (rank < 64 && sw < SHQ ? -1 : 0);
  const int ns = half ? 16 - mv : 16 + mv;
  const int kvbase = (b * NH + head) * (SK * DH * 2) + (half ? (16 + mv) * STEP : 0) + sw * 4096;
  const int naux = (is_q || (half && pair_ffn)) ? 0 : NAUX;
#define LD(RS, AUX, i, so) __builtin_amdgcn_raw_buffer_load_b128(RS, voff, (so) + ((i) >> 2) * (NSW * 4096) + ((i) & 3) * 1024, AUX)
#define PREFETCH(N, BASE) \
    _Pragma("unroll") for (int t = 0; t < N; ++t) \
      _Pragma("unroll") for (int i = 0; i < 8; ++i) { \
        B[t][i] = LD(rk, AUX_K, i, (BASE) + t * STEP); \
        if (8 * t + i + 1 >= DPRE && 8 * t + i + 1 < 8 * N) { SB(); asm volatile("s_waitcnt vmcnt(%0)" :: "i"(DPRE - 1) : "memory"); SB(); } } \
    SB();
#define REFILL(i, MODE, RS, AUX, NXT) if (MODE) { if (MODE == 2) THROTTLE(); B[0][i] = LD(RS, AUX, i, NXT); SB(); }
#define KSTEP(T, MODE, RS, AUX, NXT) { \
    float sx[8]; \
    _Pragma("unroll") for (int j = 0; j < 8; ++j) { \
      float d = dot2(B[T][j][0], q[0], 0.f); d = dot2(B[T][j][1], q[1], d); d = dot2(B[T][j][2], q[2], d); d = dot2(B[T][j][3], q[3], d); \
      asm volatile("" : "+v"(d)); sx[j] = d; SB(); REFILL(j, MODE, RS, AUX, NXT) } \
    float t4[4], t2[2]; \
    _Pragma("unroll") for (int j = 0; j < 4; ++j) { const float lo = sx[j] + dppf<ROR8>(sx[j]), hi = sx[j + 4] + dppf<ROR8>(sx[j + 4]); t4[j] = (c & 8) ? hi : lo; } \
    _Pragma("unroll") for (int j = 0; j < 2; ++j) { const float lo = t4[j] + dppf<XOR7>(t4[j]), hi = t4[j + 2] + dppf<XOR7>(t4[j + 2]); t2[j] = (c & 4) ? hi : lo; } \
    const float lo = t2[0] + dppf<XOR2>(t2[0]), hi = t2[1] + dppf<XOR2>(t2[1]); \
    float s = (c & 2) ? hi : lo; \
    s += dppf<XOR1>(s); \
    *(float*)sc = rbf(rbf(s) * 0.08838834764831845f); sc += 128; SB(); }

  if (wv >= NHW) {
    const int w = wv - NHW;
    const int lane = threadIdx.x & 63, r = lane >> 4, c = lane & 15;
    const rsrc_t rk = mk_rsrc(kc, NB * NH * SK * DH * 2), rv = mk_rsrc(vc, NB * NH * SK * DH * 2);
    const int voff = lane * 16;
    *(float*)(lds + L_SC + w * SC_S + 15 * 128 + lane * 4) = -INFINITY;
    if (is_q) LWAIT(9, NHW, 1);
    v4u B[NPRE][8];
    PREFETCH(NPRE, kvbase)
    LFLAG(10 + w) = 1u;
    STAMPC(w == 0, 11);
    LWAIT(1, 1, 1);
    const unsigned tag = __builtin_amdgcn_readfirstlane(LFLAG(5));
    const v4u q = *(const v4u*)(lds + L_Q + c * 16);
    STAMPC(w == 0, 1);

    const int thr = __builtin_amdgcn_readfirstlane((half && pair_ffn) ? LAGF : is_q ? LAGQ : ns > 16 ? (half ? LAGPQ : LAGP) : LAGN);
#define THROTTLE() asm volatile("s_cmp_lt_u32 %0, 1\n\ts_cbranch_scc1 1f\n\ts_waitcnt vmcnt(6)\n\ts_cmp_lt_u32 %0, 2\n\ts_cbranch_scc1 1f\n\ts_waitcnt vmcnt(5)\n1:" :: "s"(thr) : "scc", "memory")
    unsigned char* sc = lds + L_SC + w * SC_S + r * 32 + (c >> 1) * 4;
    int so = kvbase + (NPRE + naux) * STEP;
    KSTEP(0, 1, rk, AUX_K, so)
    KSTEP(1, 0, rk, AUX_K, 0)
    KSTEP(2, 0, rk, AUX_K, 0)
    KSTEP(3, 0, rk, AUX_K, 0)
    sc += naux * 128;
    for (int it = NPRE + naux; it < ns - 1; ++it) { so += STEP; KSTEP(0, 2, rk, AUX_K, so) STAMPC(w == 0 && it == 7, 20); }
    so = kvbase;
    KSTEP(0, 2, rv, AUX_V, so)
    STAMPC(w == 0, 21);
    float mw;
    {
      if (NAUX && naux) LWAIT(10 + w, 2, 1);
      asm volatile("" ::: "memory");
      const f32x4 s0 = *(const f32x4*)(lds + L_SC + w * SC_S + lane * 32), s1 = *(const f32x4*)(lds + L_SC + w * SC_S + lane * 32 + 16);
      const f32x4 s2 = *(const f32x4*)(lds + L_SC + w * SC_S + 2048 + (lane & 3) * 32), s3 = *(const f32x4*)(lds + L_SC + w * SC_S + 2048 + (lane & 3) * 32 + 16);
      float mx = -INFINITY;
#pragma unroll
      for (int j = 0; j < 4; ++j) mx = fmaxf(fmaxf(mx, fmaxf(s0[j], s1[j])), fmaxf(s2[j], s3[j]));
      mx = fmaxf(mx, dppf<ROR8>(mx)); mx = fmaxf(mx, dppf<ROR4>(mx)); mx = fmaxf(mx, dppf<ROR2>(mx)); mx = fmaxf(mx, dppf<ROR1>(mx));
      mw = xrow_max(mx) * LOG2E;
      float p[8], e[8];
#pragma unroll
      for (int j = 0; j < 4; ++j) { p[j] = __builtin_amdgcn_exp2f(fmaf(s0[j], LOG2E, -mw)); p[4 + j] = __builtin_amdgcn_exp2f(fmaf(s1[j], LOG2E, -mw));
                                    e[j] = __builtin_amdgcn_exp2f(fmaf(s2[j], LOG2E, -mw)); e[4 + j] = __builtin_amdgcn_exp2f(fmaf(s3[j], LOG2E, -mw)); }
      asm volatile("" ::: "memory");
      *(v4u*)(lds + L_SC + w * SC_S + lane * 16) = v4u{pk_bf16(p[0], p[1]), pk_bf16(p[2], p[3]), pk_bf16(p[4], p[5]), pk_bf16(p[6], p[7])};
      *(v4u*)(lds + L_SC + w * SC_S + 1024 + (lane & 3) * 16) = v4u{pk_bf16(e[0], e[1]), pk_bf16(e[2], e[3]), pk_bf16(e[4], e[5]), pk_bf16(e[6], e[7])};
      const float le = ((e[0] + e[1]) + (e[2] + e[3])) + ((e[4] + e[5]) + (e[6] + e[7]));
      float l = ((p[0] + p[1]) + (p[2] + p[3])) + ((p[4] + p[5]) + (p[6] + p[7])) + (lane < 4 ? le : 0.f);
      l += dppf<ROR8>(l); l += dppf<ROR4>(l); l += dppf<ROR2>(l); l += dppf<ROR1>(l);
      l = xrow_sum(l);
      if (lane == 0) *(f32x2*)(lds + L_ML + w * 8) = f32x2{mw, l};
      LADD(6);
      SB();
    }
    float acc[8];
#pragma unroll
    for (int e = 0; e < 8; ++e) acc[e] = 0.f;
    const unsigned char* pp = lds + L_SC + w * SC_S + r * 16;
#define VSTEP(MODE, NXT) { \
    const v4u pw = *(const v4u*)pp; pp += 64; \
    _Pragma("unroll") for (int j = 0; j < 8; ++j) { \
      const unsigned u = pw[j >> 1], pl = (j & 1) ? u >> 16 : u & 0xffffu, ph = (j & 1) ? u & 0xffff0000u : u << 16; \
      _Pragma("unroll") for (int d = 0; d < 4; ++d) { acc[2 * d] = dot2(B[0][j][d], pl, acc[2 * d]); acc[2 * d + 1] = dot2(B[0][j][d], ph, acc[2 * d + 1]); } \
      asm volatile("" : "+v"(acc[0]), "+v"(acc[1]), "+v"(acc[2]), "+v"(acc[3]), "+v"(acc[4]), "+v"(acc[5]), "+v"(acc[6]), "+v"(acc[7])); SB(); \
      REFILL(j, MODE, rv, AUX_V, NXT) } }
    for (int it = 0; it < ns - 1; ++it) { so += STEP; VSTEP(2, so) STAMPC(w == 0 && it == 7, 22); }
    if (w == 0) LFLAG(4) = 1u;
    LWAIT(3, 1, 1);
    LWAIT(7, 1, 1);
#pragma unroll
    for (int n = 0; n < 8; ++n) B[1][n] = *(const v4u*)(lds + L_F4 + (w * 8 + n) * 1024 + lane * 16);
    const f32x2 gl = *(const f32x2*)(lds + L_ML + 32);
    float f = __builtin_amdgcn_exp2f(mw - gl.x) * gl.y;
    asm volatile("" : "+v"(f));
    SB();
    VSTEP(0, 0)
    STAMPC(true, 16 + w);
    {
      float t[4];
#pragma unroll
      for (int j = 0; j < 4; ++j) t[j] = rs32(acc[j], acc[j + 4]);
      *(unsigned*)(lds + L_OP + w * OP_S + (4 * c + r) * 4) = pk_bf16(rs16(t[0], t[2]) * f, rs16(t[1], t[3]) * f);
    }
    LADD(2);
    LWAIT(2, NSW, 0);
    {
      const rsrc_t rws = mk_rsrc(ws, WS_END);
      v4u a[4];
#pragma unroll
      for (int s = 0; s < 4; ++s) a[s] = *(const v4u*)(lds + L_OP + (c >> 2) * OP_S + s * 64 + r * 16);
      f32x4 d0 = {0.f, 0.f, 0.f, 0.f}, d1 = {0.f, 0.f, 0.f, 0.f};
#pragma unroll
      for (int s = 0; s < 4; ++s) { d0 = mfma(a[s], B[1][2 * s], d0); d1 = mfma(a[s], B[1][2 * s + 1], d1); }
      const float y0 = xrow_sum(d0[0]), y1 = xrow_sum(d1[0]);
      st128_sc1(v4u{as_u(y0), tag, as_u(y1), tag}, rws, lane < 16 ? (c >> 3) * 8192 + (c & 7) * 16 : 0x40000000, WS_Y + ((b * 40 + 32 + 2 * w) * 64 + head * 2 + half) * 128);
    }
    return;
  }

  const int w = wv;
  const int ri = half * 128 + rank_of(bid);
  __builtin_amdgcn_s_setprio(2);
  const rsrc_t rws = mk_rsrc(ws, WS_END);
  unsigned tag, xres;
  {
    FRESH_IDS();
    const rsrc_t rx = mk_rsrc(x, NB * DM * 2);
    if (ri < 64) {
      const rsrc_t rwq = mk_rsrc(wq, DM * NH * DH * 2);
      const v4u x8 = __builtin_amdgcn_raw_buffer_load_b128(rx, lane * 16, w * (DM * 2), AUX_X);
      const unsigned x2 = __builtin_amdgcn_raw_buffer_load_b32(rx, lane * 4 + 1024, w * (DM * 2), AUX_X);
      v4u xa[4];
#pragma unroll
      for (int s = 0; s < 4; ++s) xa[s] = __builtin_amdgcn_raw_buffer_load_b128(rx, (c >> 2) * (DM * 2) + r * 16, (128 * w + 32 * s) * 2, AUX_X);
      const unsigned epoch_v = __builtin_amdgcn_raw_buffer_load_b32(rws, 0, WS_EPOCH, AUX_SC1);
      xres = __builtin_amdgcn_raw_buffer_load_b32(rx, (lane & 7) * 4, ri * 32, AUX_X);
      SB();
      v2u Wq[32];
#define WQLD(n) Wq[n] = __builtin_amdgcn_raw_buffer_load_b64(rwq, (8 * r) * 8192 + c * 8, (128 * w + 32 * ((n) >> 3) + ((n) & 7)) * 8192 + ri * 128, AUX_WQ)
#pragma unroll
      for (int n = 0; n < 12; ++n) WQLD(n);
      SB();
      if (w < 4) {
        const unsigned hv[5] = {x8[0], x8[1], x8[2], x8[3], x2};
        *(f32x2*)(lds + L_ST + w * 8) = ln_stats(hv);
        LADD(8);
      }
      SB();
#pragma unroll
      for (int n = 12; n < 20; ++n) WQLD(n);
      SB();
      STAMPC(w == 0, 8);
      LWAIT(8, 4, 0);
      const f32x2 st = *(const f32x2*)(lds + L_ST + (c >> 2) * 8);
#pragma unroll
      for (int s = 0; s < 4; ++s) {
        xa[s] = v4u{ln_pair(xa[s][0], st.x, st.y), ln_pair(xa[s][1], st.x, st.y), ln_pair(xa[s][2], st.x, st.y), ln_pair(xa[s][3], st.x, st.y)}; asm volatile("" : "+v"(xa[s]));
        SB();
#pragma unroll
        for (int n = 20 + 3 * s; n < 23 + 3 * s; ++n) WQLD(n);
        SB();
      }
      LADD(9);
      STAMPC(w == 0, 27);
      const f32x4 zero = {0.f, 0.f, 0.f, 0.f};
      f32x4 dq[4] = {zero, zero, zero, zero};
#pragma unroll
      for (int s = 0; s < 4; ++s) {
#pragma unroll
        for (int f = 0; f < 4; ++f) {
          v4u bf;
#pragma unroll
          for (int d = 0; d < 4; ++d) bf[d] = (f & 1) ? perm_hi(Wq[8 * s + 2 * d][f >> 1], Wq[8 * s + 2 * d + 1][f >> 1]) : perm_lo(Wq[8 * s + 2 * d][f >> 1], Wq[8 * s + 2 * d + 1][f >> 1]);
          dq[f] = mfma(xa[s], bf, dq[f]);
        }
        SB();
      }
      *(f32x4*)(lds + L_GP + w * 1024 + lane * 16) = f32x4{dq[0][0], dq[1][0], dq[2][0], dq[3][0]};
      tag = __builtin_amdgcn_readfirstlane(epoch_v) + 1u;
      LADD(0);
      STAMPC(w == 0, 24);
      if (w == 1) {
        LWAIT(0, NHW, 0);
        f32x4 pq[NHW];
#pragma unroll
        for (int ww = 0; ww < NHW; ++ww) pq[ww] = *(const f32x4*)(lds + L_GP + ww * 1024 + lane * 16);
        const f32x4 sq = ((pq[0] + pq[1]) + (pq[2] + pq[3])) + pq[4];
        st128_sc1(v4u{pk_bf16(sq[0], sq[1]), tag, pk_bf16(sq[2], sq[3]), tag}, rws, r * (NH * 512) + c * 16, WS_Q + ri * 256);
        STAMPC(true, 25);
      }
    } else {
      const unsigned epoch_v = __builtin_amdgcn_raw_buffer_load_b32(rws, 0, WS_EPOCH, AUX_SC1);
      xres = __builtin_amdgcn_raw_buffer_load_b32(rx, (lane & 7) * 4, ri * 32, AUX_X);
      tag = __builtin_amdgcn_readfirstlane(epoch_v) + 1u;
    }
    if (w == 4) {
      v4u g;
      for (unsigned spin = 0;; ++spin) {
        g = __builtin_amdgcn_raw_buffer_load_b128(rws, (lane & 31) * 16, WS_Q + (b * NH + head) * 512, AUX_SC1);
        if (__all(TAGBAD(g) == 0) || spin > SPIN_CAP) break;
        asm volatile("" ::: "memory");
      }
      *(v2u*)(lds + L_Q + (lane & 31) * 8) = v2u{g[0], g[2]};
      LFLAG(5) = tag;
      asm volatile("s_waitcnt lgkmcnt(0)" ::: "memory");
      LFLAG(1) = 1u;
      STAMPC(true, 26);
    } else if (NAUX && naux) {
      const rsrc_t rk = mk_rsrc(kc, NB * NH * SK * DH * 2);
      const int voff = lane * 16;
      const int thr = 0;
      v4u B[NAUX ? NAUX : 1][8];
      LWAIT(10 + w, 1, 1);
      PREFETCH(NAUX, kvbase + NPRE * STEP)
      LWAIT(1, 1, 1);
      const v4u q = *(const v4u*)(lds + L_Q + c * 16);
      unsigned char* sc = lds + L_SC + w * SC_S + NPRE * 128 + r * 32 + (c >> 1) * 4;
#pragma unroll
      for (int t = 0; t < NAUX; ++t) KSTEP(t, 0, rk, AUX_K, 0)
      asm volatile("s_waitcnt lgkmcnt(0)" ::: "memory");
      LFLAG(10 + w) = 2u;
      STAMPC(w == 0, 28);
    }
  }

  const bool is_red = ri < NRED;
  const int rb = ri / 40, cb = ri % 40;
  const int fi = half ? ffn_of_rank(rank_of(bid)) : -1;
  const bool is_ffn = fi >= 0;

#define STAGE_GU(HOOK) if (is_ffn) { \
    const int ln = fresh_lane(); \
    const GAS unsigned char* gp = (const GAS unsigned char*)((ln & 4) ? wu : wg) + (size_t)((128 * w + (ln >> 3)) * (FF * 2) + fi * 64 + (ln & 3) * 16); \
    _Pragma("nounroll") for (int s = 0; s < 16 / PG; ++s) { \
      HOOK \
      v4u GU[PG]; \
      _Pragma("unroll") for (int n = 0; n < PG; ++n) GU[n] = AUX_W ? __builtin_nontemporal_load((const GAS v4u*)(gp + 8 * n * (FF * 2))) : *(const GAS v4u*)(gp + 8 * n * (FF * 2)); \
      gp += 8 * PG * (FF * 2); \
      _Pragma("unroll") for (int n = 0; n < PG; ++n) *(v4u*)(lds + L_BG + (w * 16 + s * PG + n) * 1024 + ln * 16) = GU[n]; \
    } }
#define WO_STEP(s, OUT) { \
    v4u T[8]; \
    _Pragma("unroll") for (int e = 0; e < 8; ++e) { T[e] = __builtin_amdgcn_raw_buffer_load_b128(rwo, vo, so + (32 * (s) + 4 * e) * (DM * 2), AUX_W); if (e % 4 == 3) VM0(); else if (e % PWN == PWN - 1) { SB(); asm volatile("s_cmp_lt_i32 %0, 0\n\ts_cbranch_scc0 1f\n\ts_waitcnt vmcnt(0)\n1:" :: "s"(fi) : "scc", "memory"); SB(); } } \
    _Pragma("unroll") for (int e = 0; e < 8; ++e) *(v4u*)(scr + e * 1024 + lane * 16) = T[e]; \
    asm volatile("" ::: "memory"); \
    _Pragma("unroll") for (int t = 0; t < 4; ++t) { \
      unsigned u[8]; \
      _Pragma("unroll") for (int e = 0; e < 8; ++e) u[e] = *(const unsigned*)(scr + (8 * r + e) * 256 + t * 64 + c * 4); \
      v4u f0, f1; \
      _Pragma("unroll") for (int d = 0; d < 4; ++d) { f0[d] = perm_lo(u[2 * d], u[2 * d + 1]); f1[d] = perm_hi(u[2 * d], u[2 * d + 1]); } \
      OUT(s, t, f0, f1) } \
    asm volatile("" ::: "memory"); SB(); }
  if (w < 4) {
    v4u F[32];
    {
      FRESH_IDS();
      const rsrc_t rwo = mk_rsrc(wo, DM * NH * DH * 2);
      const int vo = r * (DM * 2) + c * 16, so = head * DH * (DM * 2) + w * 256;
      unsigned char* scr = lds + L_BG + w * 16384;
#define OUT_REG(s, t, f0, f1) F[8 * (s) + 2 * (t)] = f0; F[8 * (s) + 2 * (t) + 1] = f1; asm volatile("" : "+v"(F[8 * (s) + 2 * (t)]), "+v"(F[8 * (s) + 2 * (t) + 1]));
      WO_STEP(0, OUT_REG) WO_STEP(1, OUT_REG) WO_STEP(2, OUT_REG) WO_STEP(3, OUT_REG)
    }
    STAMPC(w == 0, 9);
    STAGE_GU()
    STAMPC(w == 0, 10);
    HOLD_UNTIL(2, 80);
    LWAIT(4, 1, 4);
    LWAIT(2, NSW, 0);
    STAMPC(w == 0, 2);
    FRESH_IDS();
    v4u a[4];
#pragma unroll
    for (int s = 0; s < 4; ++s) a[s] = *(const v4u*)(lds + L_OP + (c >> 2) * OP_S + s * 64 + r * 16);
    const f32x4 zero = {0.f, 0.f, 0.f, 0.f};
    f32x4 d[8];
#pragma unroll
    for (int f = 0; f < 8; ++f) d[f] = mfma(a[0], F[f], zero);
#pragma unroll
    for (int s = 1; s < 4; ++s)
#pragma unroll
      for (int f = 0; f < 8; ++f) d[f] = mfma(a[s], F[8 * s + f], d[f]);
    float t[4];
#pragma unroll
    for (int j = 0; j < 4; ++j) t[j] = rs32(d[j][0], d[j + 4][0]);
    const float y0 = rs16(t[0], t[2]), y1 = rs16(t[1], t[3]);
    st128_sc1(v4u{as_u(y0), tag, as_u(y1), tag}, rws, (lane >> 3) * 8192 + (lane & 7) * 16, WS_Y + ((b * 40 + 8 * w) * 64 + head * 2 + half) * 128);
    STAMPC(w == 0, 3);
    if (!is_ffn) return;
  } else {
    {
      FRESH_IDS();
      const rsrc_t rwo = mk_rsrc(wo, DM * NH * DH * 2);
      const int vo = r * (DM * 2) + c * 16, so = head * DH * (DM * 2) + w * 256;
      unsigned char* scr = lds + L_BG + w * 16384;
#define OUT_LDS(s, t, f0, f1) *(v4u*)(lds + L_F4 + ((t) * 8 + 2 * (s)) * 1024 + lane * 16) = f0; *(v4u*)(lds + L_F4 + ((t) * 8 + 2 * (s) + 1) * 1024 + lane * 16) = f1;
#pragma nounroll
      for (int s = 0; s < 4; ++s) WO_STEP(s, OUT_LDS)
      asm volatile("s_waitcnt lgkmcnt(0)" ::: "memory");
      LFLAG(3) = 1u;
    }
    float Mo = 0.f, Lo = 0.f;
    bool sent = false;
    const int mls = WS_ML + (b * NH + head) * 256;
#define SEND_ML() { \
      const f32x4 m01 = *(const f32x4*)(lds + L_ML), m23 = *(const f32x4*)(lds + L_ML + 16); \
      Mo = fmaxf(fmaxf(m01[0], m01[2]), fmaxf(m23[0], m23[2])); \
      Lo = fmaf(__builtin_amdgcn_exp2f(m01[0] - Mo), m01[1], __builtin_amdgcn_exp2f(m01[2] - Mo) * m01[3]) + fmaf(__builtin_amdgcn_exp2f(m23[0] - Mo), m23[1], __builtin_amdgcn_exp2f(m23[2] - Mo) * m23[3]); \
      st128_sc1(v4u{as_u(Mo), tag, as_u(Lo), tag}, rws, fresh_lane() == 0 ? 0 : 0x40000000, mls + half * 128); sent = true; }
    STAGE_GU(if (!sent && __builtin_amdgcn_readfirstlane(LFLAG(6)) >= (unsigned)NSW) { asm volatile("" ::: "memory"); SEND_ML() })
    if (!sent) { LWAIT(6, NSW, 8); SEND_ML() }
    {
      v4u g;
      for (unsigned spin = 0;; ++spin) {
        g = __builtin_amdgcn_raw_buffer_load_b128(rws, 0, mls + (half ^ 1) * 128, AUX_SC1);
        if (__all(TAGBAD(g) == 0) || spin > SPIN_CAP) break;
        __builtin_amdgcn_s_sleep(16);
        asm volatile("" ::: "memory");
      }
      const float Mg = fmaxf(Mo, as_f(g[0]));
      const float Lg = __builtin_amdgcn_exp2f(Mo - Mg) * Lo + __builtin_amdgcn_exp2f(as_f(g[0]) - Mg) * as_f(g[2]);
      *(f32x2*)(lds + L_ML + 32) = f32x2{Mg, 1.0f / Lg};
      asm volatile("s_waitcnt lgkmcnt(0)" ::: "memory");
      LFLAG(7) = 1u;
      STAMPC(true, 13);
    }
    LWAIT(4, 1, 4);
    if (is_red) {
      FRESH_IDS();
      const int ys = WS_Y + (rb * 40 + cb) * 8192;
      v4u y[8];
      for (unsigned spin = 0;; ++spin) {
#pragma unroll
        for (int k = 0; k < 8; ++k) y[k] = __builtin_amdgcn_raw_buffer_load_b128(rws, lane * 16, ys + k * 1024, AUX_SC1);
        SB();
        unsigned bad = 0;
#pragma unroll
        for (int k = 0; k < 8; ++k) bad |= TAGBAD(y[k]);
        if (__all(bad == 0) || spin > SPIN_CAP) break;
        asm volatile("" ::: "memory");
      }
      STAMPC(true, 12);
      float s0 = ((as_f(y[0][0]) + as_f(y[1][0])) + (as_f(y[2][0]) + as_f(y[3][0]))) + ((as_f(y[4][0]) + as_f(y[5][0])) + (as_f(y[6][0]) + as_f(y[7][0])));
      float s1 = ((as_f(y[0][2]) + as_f(y[1][2])) + (as_f(y[2][2]) + as_f(y[3][2]))) + ((as_f(y[4][2]) + as_f(y[5][2])) + (as_f(y[6][2]) + as_f(y[7][2])));
      s0 += dppf<ROR8>(s0); s1 += dppf<ROR8>(s1);
      s0 = xrow_sum(s0); s1 = xrow_sum(s1);
      const unsigned hp2 = pk_bf16(lo_f(xres) + rbf(s0), hi_f(xres) + rbf(s1));
      __builtin_amdgcn_raw_buffer_store_b64(v2u{hp2, tag}, rws, lane < 8 ? lane * 8 : 0x40000000, WS_H + (rb * 320 + cb * 8) * 8, AUX_SC1);
      STAMPC(true, 4);
      v4u z[7];
      const int zs = WS_Z + (rb * 40 + cb) * (NFFN * 128);
      const bool has7 = lane < NFFN * 8 - 6 * 64;
      for (unsigned spin = 0;; ++spin) {
#pragma unroll
        for (int k = 0; k < 6; ++k) z[k] = __builtin_amdgcn_raw_buffer_load_b128(rws, lane * 16, zs + k * 1024, AUX_SC1);
        z[6] = __builtin_amdgcn_raw_buffer_load_b128(rws, has7 ? lane * 16 + 6144 : lane * 16, zs, AUX_SC1);
        SB();
        unsigned bad = 0;
#pragma unroll
        for (int k = 0; k < 7; ++k) bad |= TAGBAD(z[k]);
        if (__all(bad == 0) || spin > SPIN_CAP) break;
        asm volatile("" ::: "memory");
      }
      STAMPC(true, 14);
      s0 = ((as_f(z[0][0]) + as_f(z[1][0])) + (as_f(z[2][0]) + as_f(z[3][0]))) + ((as_f(z[4][0]) + as_f(z[5][0])) + (has7 ? as_f(z[6][0]) : 0.f));
      s1 = ((as_f(z[0][2]) + as_f(z[1][2])) + (as_f(z[2][2]) + as_f(z[3][2]))) + ((as_f(z[4][2]) + as_f(z[5][2])) + (has7 ? as_f(z[6][2]) : 0.f));
      s0 += dppf<ROR8>(s0); s1 += dppf<ROR8>(s1);
      s0 = xrow_sum(s0); s1 = xrow_sum(s1);
      const rsrc_t rout = mk_rsrc(out, NB * DM * 2);
      __builtin_amdgcn_raw_buffer_store_b32(pk_bf16(lo_f(hp2) + rbf(s0), hi_f(hp2) + rbf(s1)), rout, lane < 8 ? lane * 4 : 0x40000000, ri * 32, AUX_OUT);
      if (bid == 0) __builtin_amdgcn_raw_buffer_store_b32(tag, rws, lane == 0 ? 0 : 0x40000000, WS_EPOCH, AUX_SC1);
      STAMPC(true, 7);
    } else if (is_ffn) {
      LWAIT(2, NSW, 1);
    }
  }

  if (is_ffn) {
    FRESH_IDS();
    v4u BG[16], BD[8];
    {
      const rsrc_t rd = mk_rsrc(wd, DM * FF * 2);
      v4u D[8];
#pragma unroll
      for (int n = 0; n < 8; ++n) D[n] = __builtin_amdgcn_raw_buffer_load_b128(rd, r * (DM * 2) + c * 16, (fi * 32 + 4 * n) * (DM * 2) + w * 256, AUX_W);
      SB();
#pragma unroll
      for (int s = 0; s < 4; ++s) {
        v2u t[8];
#pragma unroll
        for (int e = 0; e < 8; ++e) t[e] = *(const v2u*)(lds + L_BG + (128 * w + 32 * s + 8 * r + e) * 128 + c * 8);
#pragma unroll
        for (int d = 0; d < 4; ++d) {
          BG[4 * s + 0][d] = perm_lo(t[2 * d][0], t[2 * d + 1][0]); BG[4 * s + 1][d] = perm_hi(t[2 * d][0], t[2 * d + 1][0]);
          BG[4 * s + 2][d] = perm_lo(t[2 * d][1], t[2 * d + 1][1]); BG[4 * s + 3][d] = perm_hi(t[2 * d][1], t[2 * d + 1][1]);
        }
      }
#pragma unroll
      for (int n = 0; n < 16; ++n) asm volatile("" : "+v"(BG[n]));
      SB();
#pragma unroll
      for (int n = 0; n < 8; ++n) *(v4u*)(lds + L_BG + w * 16384 + n * 1024 + lane * 16) = D[n];
      asm volatile("" ::: "memory");
#pragma unroll
      for (int s = 0; s < 4; ++s) {
        unsigned t[8];
#pragma unroll
        for (int e = 0; e < 8; ++e) t[e] = *(const unsigned*)(lds + L_BG + w * 16384 + (8 * r + e) * 256 + s * 64 + c * 4);
#pragma unroll
        for (int d = 0; d < 4; ++d) { BD[2 * s][d] = perm_lo(t[2 * d], t[2 * d + 1]); BD[2 * s + 1][d] = perm_hi(t[2 * d], t[2 * d + 1]); }
      }
#pragma unroll
      for (int n = 0; n < 8; ++n) asm volatile("" : "+v"(BD[n]));
    }
    STAMPC(w == 0, 15);
    if (w < 4) {
      v4u g0, g1, g2;
      const int hs = WS_H + w * 2560;
      for (unsigned spin = 0;; ++spin) {
        g0 = __builtin_amdgcn_raw_buffer_load_b128(rws, lane * 16, hs, AUX_SC1);
        g1 = __builtin_amdgcn_raw_buffer_load_b128(rws, lane * 16 + 1024, hs, AUX_SC1);
        g2 = __builtin_amdgcn_raw_buffer_load_b128(rws, (lane & 31) * 16 + 2048, hs, AUX_SC1);
        SB();
        const unsigned bad = TAGBAD(g0) | TAGBAD(g1) | TAGBAD(g2);
        if (__all(bad == 0) || spin > SPIN_CAP) break;
        asm volatile("" ::: "memory");
      }
      const unsigned hv[5] = {g0[0], g0[2], g1[0], g1[2], lane < 32 ? g2[0] : g2[2]};
      const f32x2 st = ln_stats(hv);
      const float mb = st.x, rden = st.y;
      unsigned char* hrow = lds + L_HN + w * HN_S;
      *(v2u*)(hrow + lane * 8) = v2u{ln_pair(hv[0], mb, rden), ln_pair(hv[1], mb, rden)};
      *(v2u*)(hrow + 512 + lane * 8) = v2u{ln_pair(hv[2], mb, rden), ln_pair(hv[3], mb, rden)};
      *(unsigned*)(hrow + 1024 + (lane & 31) * 8 + (lane >> 5) * 4) = ln_pair(hv[4], mb, rden);
    }
    BAR();
    STAMPC(w == 0, 5);
    const f32x4 zero = {0.f, 0.f, 0.f, 0.f};
    f32x4 dd[4] = {zero, zero, zero, zero};
    v4u a[4];
#pragma unroll
    for (int s = 0; s < 4; ++s) a[s] = *(const v4u*)(lds + L_HN + (c >> 2) * HN_S + (128 * w + 32 * s + 8 * r) * 2);
    SB();
#pragma unroll
    for (int s = 0; s < 4; ++s)
#pragma unroll
      for (int f = 0; f < 4; ++f) dd[f] = mfma(a[s], BG[4 * s + f], dd[f]);
    {
      unsigned char* gp = lds + L_GP + (w * 4 + r) * GP_S + (c & 7) * 32 + (c >> 3) * 8;
      *(f32x2*)gp = f32x2{dd[0][0], dd[1][0]};
      *(f32x2*)(gp + 16) = f32x2{dd[2][0], dd[3][0]};
    }
    BAR();
    f32x4 gu[NHW];
#pragma unroll
    for (int ww = 0; ww < NHW; ++ww) gu[ww] = *(const f32x4*)(lds + L_GP + (ww * 4 + (c >> 2)) * GP_S + (4 * r + (c & 3)) * 16);
    SB();
    const f32x4 GU = ((gu[0] + gu[1]) + (gu[2] + gu[3])) + gu[4];
    float av[2];
#pragma unroll
    for (int j = 0; j < 2; ++j) {
      const float g = rbf(GU[j]), u = rbf(GU[2 + j]);
      const float u2 = 2.0f * 0.7978845608028654f * LOG2E * (g + 0.044715f * g * g * g);
      av[j] = rbf(g * __builtin_amdgcn_rcpf(1.0f + __builtin_amdgcn_exp2f(-u2))) * u;
    }
    const unsigned ap = pk_bf16(av[0], av[1]);
    const v4u af = {dppu<0x00>(ap), dppu<0x55>(ap), dppu<0xAA>(ap), dppu<0xFF>(ap)};
    f32x4 z[8];
#pragma unroll
    for (int f = 0; f < 8; ++f) z[f] = mfma(af, BD[f], zero);
#pragma unroll
    for (int s = 0; s < 4; ++s)
      st128_sc1(v4u{as_u(z[2 * s][0]), tag, as_u(z[2 * s + 1][0]), tag}, rws, (r * 40 + (c >> 3)) * (NFFN * 128) + (c & 7) * 16, WS_Z + ((8 * w + 2 * s) * NFFN + fi) * 128);
  }
  STAMPC(w == 0, 6);

  HOLD_UNTIL(1, 100);
}

extern "C" void kernel_launch(void* const* d_in, const int* in_sizes, int n_in, void* d_out, int out_size, void* d_ws, size_t ws_size, hipStream_t stream) {
  fused_block<<<NWG, NT, 0, stream>>>((const u16*)d_in[0], (const u16*)d_in[1], (const u16*)d_in[2], (const u16*)d_in[3], (const u16*)d_in[4],
                                      (const u16*)d_in[6], (const u16*)d_in[7], (const u16*)d_in[8], (u16*)d_out, d_ws);
}
```

```cpp
#include <hip/hip_runtime.h>
#include <hip/hip_bf16.h>
#include <stdint.h>

typedef unsigned short u16;
typedef unsigned v4u __attribute__((ext_vector_type(4)));
typedef unsigned v2u __attribute__((ext_vector_type(2)));
typedef float f32x2 __attribute__((ext_vector_type(2)));
typedef float f32x4 __attribute__((ext_vector_type(4)));
typedef __bf16 bf16x2 __attribute__((ext_vector_type(2)));
typedef __bf16 bf16x8 __attribute__((ext_vector_type(8)));
typedef __amdgpu_buffer_rsrc_t rsrc_t;
#define GAS __attribute__((address_space(1)))
#define LAS __attribute__((address_space(3)))

#define LAGN 2
#define LAGF 0
#define SKEW 0
#define PW 4
#define PG 2
#define GU_FIRST 0
#define AUX_X 0
#define DPRE 8
#define QDELAY 0
#define AUX_QP 16
#define AUX_EP 16
#define ABL 0
#define PRE0 4
#define AUX_K 2
#define AUX_V 2
#define AUX_W 0
#define AUX_OUT 17
#define PWN 2
#define AUX_WQ 2
#define SHF 4
#define SHQ 4
#define LAGQ 1
#define LAGP 1
#define WAITQ 0
#define NAUX 4
#define LAGPQ 2
#define BOOST 4
constexpr int NPRE = 4;

constexpr int NB = 4, DM = 640, NH = 32, SK = 4096, DH = 128, FF = 1728;
constexpr int NSW = 4, NHW = 5, NT = 64 * (NSW + NHW), NWG = 256;
constexpr int NRED = 160, NFFN = 54;
constexpr int WS_EPOCH = 0;
constexpr int WS_ML = 32768;
constexpr int WS_H = 8192;
constexpr int WS_Q = 65536;
constexpr int WS_Y = WS_Q + NB * NH * 512;
constexpr int WS_Z = WS_Y + NB * 40 * 64 * 128;
constexpr int WS_END = WS_Z + NB * 40 * NFFN * 128;
constexpr int WS_STAMP = WS_END;
constexpr int AUX_NT = 2, AUX_SC1 = 16;
constexpr unsigned SPIN_CAP = 1u << 18;
constexpr float LOG2E = 1.4426950408889634f;

#define STAMPC(cond, i) do { } while (0)

__device__ __forceinline__ rsrc_t mk_rsrc(const void* p, int bytes) { return __builtin_amdgcn_make_buffer_rsrc((void*)p, 0, bytes, 0x00020000); }
__device__ __forceinline__ float as_f(unsigned u) { return __builtin_bit_cast(float, u); }
__device__ __forceinline__ unsigned as_u(float f) { return __builtin_bit_cast(unsigned, f); }
__device__ __forceinline__ unsigned pk_bf16(float lo, float hi) { f32x2 v = {lo, hi}; return __builtin_bit_cast(unsigned, __builtin_convertvector(v, bf16x2)); }
__device__ __forceinline__ float rbf(float x) { return as_f(pk_bf16(0.f, x)); }
__device__ __forceinline__ float lo_f(unsigned u) { return as_f(u << 16); }
__device__ __forceinline__ float hi_f(unsigned u) { return as_f(u & 0xffff0000u); }
__device__ __forceinline__ float dot2(unsigned a, unsigned b, float c) { return __builtin_amdgcn_fdot2_f32_bf16(__builtin_bit_cast(bf16x2, a), __builtin_bit_cast(bf16x2, b), c, false); }
__device__ __forceinline__ f32x4 mfma(v4u a, v4u b, f32x4 c) { return __builtin_amdgcn_mfma_f32_16x16x32_bf16(__builtin_bit_cast(bf16x8, a), __builtin_bit_cast(bf16x8, b), c, 0, 0, 0); }
template <int CTRL> __device__ __forceinline__ unsigned dppu(unsigned x) { return __builtin_amdgcn_mov_dpp((int)x, CTRL, 0xf, 0xf, true); }
template <int CTRL> __device__ __forceinline__ float dppf(float x) { return as_f(dppu<CTRL>(as_u(x))); }
constexpr int ROR1 = 0x121, ROR2 = 0x122, ROR4 = 0x124, ROR8 = 0x128, XOR1 = 0xB1, XOR2 = 0x4E, XOR7 = 0x141, BCAST = 0x150;
__device__ __forceinline__ unsigned perm_lo(unsigned a, unsigned b) { return __builtin_amdgcn_perm(b, a, 0x05040100u); }
__device__ __forceinline__ unsigned perm_hi(unsigned a, unsigned b) { return __builtin_amdgcn_perm(b, a, 0x07060302u); }
__device__ __forceinline__ float rs16(float a, float b) { auto s = __builtin_amdgcn_permlane16_swap(as_u(a), as_u(b), false, false); return as_f(s[0]) + as_f(s[1]); }
__device__ __forceinline__ float rs32(float a, float b) { auto s = __builtin_amdgcn_permlane32_swap(as_u(a), as_u(b), false, false); return as_f(s[0]) + as_f(s[1]); }
__device__ __forceinline__ float xrow_sum(float x) { x = rs16(x, x); return rs32(x, x); }
__device__ __forceinline__ float xrow_max(float x) {
  auto s = __builtin_amdgcn_permlane16_swap(as_u(x), as_u(x), false, false); x = fmaxf(as_f(s[0]), as_f(s[1]));
  auto t = __builtin_amdgcn_permlane32_swap(as_u(x), as_u(x), false, false); return fmaxf(as_f(t[0]), as_f(t[1]));
}
__device__ __forceinline__ void st128_sc1(v4u v, rsrc_t rs, int voff, int soff) {
  __builtin_amdgcn_raw_buffer_store_b128(v, rs, voff, soff, AUX_SC1);
  asm volatile("s_nop 1" :: "v"(v) : "memory");
}
__device__ __forceinline__ int fresh_lane() { int l; asm volatile("v_mbcnt_lo_u32_b32 %0, -1, 0\n\tv_mbcnt_hi_u32_b32 %0, -1, %0" : "=v"(l)); return l; }
__device__ __forceinline__ unsigned ln_pair(unsigned v, float mb, float rden) { return pk_bf16(rbf(lo_f(v) - mb) * rden, rbf(hi_f(v) - mb) * rden); }
__device__ __forceinline__ f32x2 ln_stats(const unsigned (&hv)[5]) {
  const float piv = as_f(__builtin_amdgcn_readfirstlane(hv[0] << 16));
  float s1 = 0.f, s2 = 0.f;
#pragma unroll
  for (int j = 0; j < 5; ++j) { const float d0 = lo_f(hv[j]) - piv, d1 = hi_f(hv[j]) - piv; s1 += d0 + d1; s2 = fmaf(d0, d0, s2); s2 = fmaf(d1, d1, s2); }
  float ss = rs32(s1, s2);
  ss = rs16(ss, ss);
  ss += dppf<ROR8>(ss); ss += dppf<ROR4>(ss); ss += dppf<ROR2>(ss); ss += dppf<ROR1>(ss);
  const float e1 = as_f(__builtin_amdgcn_readlane(as_u(ss), 0)) * (1.0f / DM), e2 = as_f(__builtin_amdgcn_readlane(as_u(ss), 32)) * (1.0f / DM);
  const float var = fmaxf(fmaf(-e1, e1, e2), 0.f);
  return f32x2{rbf(piv + e1), __builtin_amdgcn_rcpf(rbf(__builtin_amdgcn_sqrtf(rbf(rbf(var) + 1e-5f))))};
}
#define SB() __builtin_amdgcn_sched_barrier(0)
#define VM0() do { SB(); asm volatile("s_waitcnt vmcnt(0)" ::: "memory"); SB(); } while (0)
#define BAR() do { asm volatile("s_waitcnt lgkmcnt(0)" ::: "memory"); __builtin_amdgcn_s_barrier(); asm volatile("" ::: "memory"); } while (0)
#define TAGBAD(g) (((g)[1] ^ tag) | ((g)[3] ^ tag))
#define FRESH_IDS() const int lane = fresh_lane(); const int r = lane >> 4, c = lane & 15, htid = w * 64 + lane; (void)r; (void)c; (void)htid

constexpr int L_FLAG = 0;
constexpr int L_ML = 64;
constexpr int L_Q = 128;
constexpr int OP_S = 320;
constexpr int L_ST = 384;
constexpr int L_OP = 512;
constexpr int GP_S = 384;
constexpr int L_GP = L_OP + 4 * OP_S;
constexpr int L_BG = L_GP + 5 * 4 * GP_S;
constexpr int L_F4 = L_BG + 640 * 128;
constexpr int L_SC = L_F4 + 32 * 1024;
constexpr int SC_S = 2176;
constexpr int L_END = L_SC + 4 * SC_S;
static_assert(L_BG % 16 == 0 && L_OP % 16 == 0 && L_GP % 16 == 0, "align");
#define LFLAG(i) (*(volatile LAS unsigned*)(LAS unsigned char*)(lds + L_FLAG + 4 * (i)))
#define LWAIT(i, n, SLP) do { unsigned spin_ = 0; _Pragma("nounroll") while (LFLAG(i) < (unsigned)(n) && ++spin_ < (SPIN_CAP << 4)) { if (SLP) __builtin_amdgcn_s_sleep(SLP); } asm volatile("" ::: "memory"); } while (0)
#define LADD(i) do { asm volatile("s_waitcnt lgkmcnt(0)" ::: "memory"); if (fresh_lane() == 0) __hip_atomic_fetch_add((LAS unsigned*)(LAS unsigned char*)(lds + L_FLAG + 4 * (i)), 1u, __ATOMIC_RELAXED, __HIP_MEMORY_SCOPE_WORKGROUP); asm volatile("" ::: "memory"); } while (0)

__device__ __forceinline__ int rank_of(int bid) { return (bid & 7) + 8 * (bid >> 4); }
__device__ __forceinline__ int ffn_of_rank(int j) { const int s = j - 64; if (s < 0) return -1; const int k = (s & 7) + 8 * (s >> 4); return k < 27 ? 2 * k + ((s >> 3) & 1) : -1; }

__global__ void __launch_bounds__(NT, 3) fused_block(const u16* __restrict__ kc, const u16* __restrict__ vc, void* ws, const u16* __restrict__ x,
                                                     const u16* __restrict__ wq, const u16* __restrict__ wo, const u16* __restrict__ wg,
                                                     const u16* __restrict__ wu, const u16* __restrict__ wd, u16* __restrict__ out) {
  __shared__ __attribute__((aligned(16))) unsigned char lds[L_END + 256];
  const int wv = __builtin_amdgcn_readfirstlane(threadIdx.x >> 6);
  const int bid = blockIdx.x, xs = bid & 7, rr = bid >> 3;
  const int head = xs + 8 * (rr >> 3), b = (rr >> 1) & 3, half = rr & 1;
  STAMPC(wv == 0, 0);
#define HOLD_UNTIL(k, us) do { } while (0)

  constexpr int STEP = 2 * NSW * 4096;
  const int rank = rank_of(bid);
  const bool pair_ffn = ffn_of_rank(rank) >= 0, is_q = !half && rank < 64;
  const int sw = wv >= NHW ? wv - NHW : wv;
  const int mv = pair_ffn ? (sw < SHF ? 1 : 0) : (rank < 64 && sw < SHQ ? -1 : 0);
  const int ns = half ? 16 - mv : 16 + mv;
  const int kvbase = (b * NH + head) * (SK * DH * 2) + (half ? (16 + mv) * STEP : 0) + sw * 4096;
  const int naux = (is_q || (half && pair_ffn)) ? 0 : NAUX;
#define LD(RS, AUX, i, so) __builtin_amdgcn_raw_buffer_load_b128(RS, voff, (so) + ((i) >> 2) * (NSW * 4096) + ((i) & 3) * 1024, AUX)
#define PREFETCH(N, BASE, AT8) \
    _Pragma("unroll") for (int t = 0; t < N; ++t) \
      _Pragma("unroll") for (int i = 0; i < 8; ++i) { \
        B[t][i] = LD(rk, AUX_K, i, (BASE) + t * STEP); \
        if (t == 0 && i == 7) { SB(); AT8 SB(); } \
        if (8 * t + i + 1 >= DPRE && 8 * t + i + 1 < 8 * N) { SB(); asm volatile("s_waitcnt vmcnt(%0)" :: "i"(DPRE - 1) : "memory"); SB(); } } \
    SB();
#define REFILL(i, MODE, RS, AUX, NXT) if (MODE) { if (MODE == 2) THROTTLE(); B[0][i] = LD(RS, AUX, i, NXT); SB(); }
#define KSTEP(T, MODE, RS, AUX, NXT) { \
    float sx[8]; \
    _Pragma("unroll") for (int j = 0; j < 8; ++j) { \
      float d = dot2(B[T][j][0], q[0], 0.f); d = dot2(B[T][j][1], q[1], d); d = dot2(B[T][j][2], q[2], d); d = dot2(B[T][j][3], q[3], d); \
      asm volatile("" : "+v"(d)); sx[j] = d; SB(); REFILL(j, MODE, RS, AUX, NXT) } \
    float t4[4], t2[2]; \
    _Pragma("unroll") for (int j = 0; j < 4; ++j) { const float lo = sx[j] + dppf<ROR8>(sx[j]), hi = sx[j + 4] + dppf<ROR8>(sx[j + 4]); t4[j] = (c & 8) ? hi : lo; } \
    _Pragma("unroll") for (int j = 0; j < 2; ++j) { const float lo = t4[j] + dppf<XOR7>(t4[j]), hi = t4[j + 2] + dppf<XOR7>(t4[j + 2]); t2[j] = (c & 4) ? hi : lo; } \
    const float lo = t2[0] + dppf<XOR2>(t2[0]), hi = t2[1] + dppf<XOR2>(t2[1]); \
    float s = (c & 2) ? hi : lo; \
    s += dppf<XOR1>(s); \
    *(float*)sc = rbf(rbf(s) * 0.08838834764831845f); sc += 128; SB(); }

#define THROTTLE() asm volatile("s_cmp_lt_u32 %0, 1\n\ts_cbranch_scc1 1f\n\ts_waitcnt vmcnt(6)\n\ts_cmp_lt_u32 %0, 2\n\ts_cbranch_scc1 1f\n\ts_waitcnt vmcnt(5)\n1:" :: "s"(thr) : "scc", "memory")
  if (wv < NHW) {
  const int w = wv;
  const int ri = half * 128 + rank_of(bid);
  __builtin_amdgcn_s_setprio(2);
  if (threadIdx.x < 16) *(unsigned*)(lds + L_FLAG + threadIdx.x * 4) = 0u;
  BAR();
  const rsrc_t rws = mk_rsrc(ws, WS_END);
  unsigned tag, xres;
  {
    FRESH_IDS();
    const rsrc_t rx = mk_rsrc(x, NB * DM * 2);
    if (ri < 64) {
      const rsrc_t rwq = mk_rsrc(wq, ABL & 4 ? 0 : DM * NH * DH * 2);
      const v4u x8 = __builtin_amdgcn_raw_buffer_load_b128(rx, lane * 16, w * (DM * 2), AUX_X);
      const unsigned x2 = __builtin_amdgcn_raw_buffer_load_b32(rx, lane * 4 + 1024, w * (DM * 2), AUX_X);
      v4u xa[4];
#pragma unroll
      for (int s = 0; s < 4; ++s) xa[s] = __builtin_amdgcn_raw_buffer_load_b128(rx, (c >> 2) * (DM * 2) + r * 16, (128 * w + 32 * s) * 2, AUX_X);
      const unsigned epoch_v = __builtin_amdgcn_raw_buffer_load_b32(rws, 0, WS_EPOCH, AUX_SC1);
      xres = __builtin_amdgcn_raw_buffer_load_b32(rx, (lane & 7) * 4, ri * 32, AUX_X);
      SB();
      v2u Wq[32];
#define WQLD(n) Wq[n] = __builtin_amdgcn_raw_buffer_load_b64(rwq, (8 * r) * 8192 + c * 8, (128 * w + 32 * ((n) >> 3) + ((n) & 7)) * 8192 + ri * 128, AUX_WQ)
#pragma unroll
      for (int n = 0; n < 12; ++n) WQLD(n);
      SB();
      if (w < 4) {
        const unsigned hv[5] = {x8[0], x8[1], x8[2], x8[3], x2};
        *(f32x2*)(lds + L_ST + w * 8) = ln_stats(hv);
        LADD(8);
      }
      SB();
#pragma unroll
      for (int n = 12; n < 20; ++n) WQLD(n);
      SB();
      STAMPC(w == 0, 8);
      LWAIT(8, 4, 0);
      const f32x2 st = *(const f32x2*)(lds + L_ST + (c >> 2) * 8);
#pragma unroll
      for (int s = 0; s < 4; ++s) {
        xa[s] = v4u{ln_pair(xa[s][0], st.x, st.y), ln_pair(xa[s][1], st.x, st.y), ln_pair(xa[s][2], st.x, st.y), ln_pair(xa[s][3], st.x, st.y)}; asm volatile("" : "+v"(xa[s]));
        SB();
#pragma unroll
        for (int n = 20 + 3 * s; n < 23 + 3 * s; ++n) WQLD(n);
        SB();
      }
      LADD(9);
      STAMPC(w == 0, 27);
      const f32x4 zero = {0.f, 0.f, 0.f, 0.f};
      f32x4 dq[4] = {zero, zero, zero, zero};
#pragma unroll
      for (int s = 0; s < 4; ++s) {
#pragma unroll
        for (int f = 0; f < 4; ++f) {
          v4u bf;
#pragma unroll
          for (int d = 0; d < 4; ++d) bf[d] = (f & 1) ? perm_hi(Wq[8 * s + 2 * d][f >> 1], Wq[8 * s + 2 * d + 1][f >> 1]) : perm_lo(Wq[8 * s + 2 * d][f >> 1], Wq[8 * s + 2 * d + 1][f >> 1]);
          dq[f] = mfma(xa[s], bf, dq[f]);
        }
        SB();
      }
      *(f32x4*)(lds + L_GP + w * 1024 + lane * 16) = f32x4{dq[0][0], dq[1][0], dq[2][0], dq[3][0]};
      tag = __builtin_amdgcn_readfirstlane(epoch_v) + 1u;
      LADD(0);
      STAMPC(w == 0, 24);
      if (w == 1) {
        LWAIT(0, NHW, 0);
        f32x4 pq[NHW];
#pragma unroll
        for (int ww = 0; ww < NHW; ++ww) pq[ww] = *(const f32x4*)(lds + L_GP + ww * 1024 + lane * 16);
        const f32x4 sq = ((pq[0] + pq[1]) + (pq[2] + pq[3])) + pq[4];
        st128_sc1(v4u{pk_bf16(sq[0], sq[1]), tag, pk_bf16(sq[2], sq[3]), tag}, rws, r * (NH * 512) + c * 16, WS_Q + ri * 256);
        STAMPC(true, 25);
      }
    } else {
      const unsigned epoch_v = __builtin_amdgcn_raw_buffer_load_b32(rws, 0, WS_EPOCH, AUX_SC1);
      xres = __builtin_amdgcn_raw_buffer_load_b32(rx, (lane & 7) * 4, ri * 32, AUX_X);
      tag = __builtin_amdgcn_readfirstlane(epoch_v) + 1u;
    }
    if (w == 4) {
      v4u g;
      for (unsigned spin = 0;; ++spin) {
        g = __builtin_amdgcn_raw_buffer_load_b128(rws, (lane & 31) * 16, WS_Q + (b * NH + head) * 512, AUX_SC1);
        if (__all(TAGBAD(g) == 0) || spin > SPIN_CAP) break;
        asm volatile("" ::: "memory");
      }
      *(v2u*)(lds + L_Q + (lane & 31) * 8) = v2u{g[0], g[2]};
      LFLAG(5) = tag;
      asm volatile("s_waitcnt lgkmcnt(0)" ::: "memory");
      LFLAG(1) = 1u;
      STAMPC(true, 26);
    } else if (NAUX && naux) {
      const rsrc_t rk = mk_rsrc(kc, NB * NH * SK * DH * 2);
      const int voff = lane * 16;
      const int thr = 0;
      v4u B[NAUX ? NAUX : 1][8];
      LWAIT(10 + w, 1, 1);
      PREFETCH(NAUX, kvbase + NPRE * STEP, )
      LWAIT(1, 1, 1);
      const v4u q = *(const v4u*)(lds + L_Q + c * 16);
      unsigned char* sc = lds + L_SC + w * SC_S + NPRE * 128 + r * 32 + (c >> 1) * 4;
#pragma unroll
      for (int t = 0; t < NAUX; ++t) KSTEP(t, 0, rk, AUX_K, 0)
      asm volatile("s_waitcnt lgkmcnt(0)" ::: "memory");
      LFLAG(10 + w) = 2u;
    }
  }

  const bool is_red = ri < NRED;
  const int rb = ri / 40, cb = ri % 40;
  const int fi = half ? ffn_of_rank(rank_of(bid)) : -1;
  const bool is_ffn = fi >= 0;

#define STAGE_GU(HOOK) if (is_ffn && !(ABL & 2)) { \
    const int ln = fresh_lane(); \
    const GAS unsigned char* gp = (const GAS unsigned char*)((ln & 4) ? wu : wg) + (size_t)((128 * w + (ln >> 3)) * (FF * 2) + fi * 64 + (ln & 3) * 16); \
    _Pragma("nounroll") for (int s = 0; s < 16 / PG; ++s) { \
      HOOK \
      v4u GU[PG]; \
      _Pragma("unroll") for (int n = 0; n < PG; ++n) GU[n] = AUX_W ? __builtin_nontemporal_load((const GAS v4u*)(gp + 8 * n * (FF * 2))) : *(const GAS v4u*)(gp + 8 * n * (FF * 2)); \
      gp += 8 * PG * (FF * 2); \
      _Pragma("unroll") for (int n = 0; n < PG; ++n) *(v4u*)(lds + L_BG + (w * 16 + s * PG + n) * 1024 + ln * 16) = GU[n]; \
    } }
#define WO_STEP(s, OUT) { \
    v4u T[8]; \
    _Pragma("unroll") for (int e = 0; e < 8; ++e) { T[e] = __builtin_amdgcn_raw_buffer_load_b128(rwo, vo, so + (32 * (s) + 4 * e) * (DM * 2), AUX_W); if (e % 4 == 3) VM0(); else if (e % PWN == PWN - 1) { SB(); asm volatile("s_cmp_lt_i32 %0, 0\n\ts_cbranch_scc0 1f\n\ts_waitcnt vmcnt(0)\n1:" :: "s"(fi) : "scc", "memory"); SB(); } } \
    _Pragma("unroll") for (int e = 0; e < 8; ++e) *(v4u*)(scr + e * 1024 + lane * 16) = T[e]; \
    asm volatile("" ::: "memory"); \
    _Pragma("unroll") for (int t = 0; t < 4; ++t) { \
      unsigned u[8]; \
      _Pragma("unroll") for (int e = 0; e < 8; ++e) u[e] = *(const unsigned*)(scr + (8 * r + e) * 256 + t * 64 + c * 4); \
      v4u f0, f1; \
      _Pragma("unroll") for (int d = 0; d < 4; ++d) { f0[d] = perm_lo(u[2 * d], u[2 * d + 1]); f1[d] = perm_hi(u[2 * d], u[2 * d + 1]); } \
      OUT(s, t, f0, f1) } \
    asm volatile("" ::: "memory"); SB(); }
  if (w < 4) {
    v4u F[32];
    {
      FRESH_IDS();
      const rsrc_t rwo = mk_rsrc(wo, ABL & 1 ? 0 : DM * NH * DH * 2);
      const int vo = r * (DM * 2) + c * 16, so = head * DH * (DM * 2) + w * 256;
      unsigned char* scr = lds + L_BG + w * 16384;
#define OUT_REG(s, t, f0, f1) F[8 * (s) + 2 * (t)] = f0; F[8 * (s) + 2 * (t) + 1] = f1; asm volatile("" : "+v"(F[8 * (s) + 2 * (t)]), "+v"(F[8 * (s) + 2 * (t) + 1]));
      WO_STEP(0, OUT_REG) WO_STEP(1, OUT_REG) WO_STEP(2, OUT_REG) WO_STEP(3, OUT_REG)
    }
    STAMPC(w == 0, 9);
    STAGE_GU()
    STAMPC(w == 0, 10);
    HOLD_UNTIL(2, 80);
    LWAIT(4, 1, 4);
    LWAIT(2, NSW, 0);
    STAMPC(w == 0, 2);
    FRESH_IDS();
    v4u a[4];
#pragma unroll
    for (int s = 0; s < 4; ++s) a[s] = *(const v4u*)(lds + L_OP + (c >> 2) * OP_S + s * 64 + r * 16);
    const f32x4 zero = {0.f, 0.f, 0.f, 0.f};
    f32x4 d[8];
#pragma unroll
    for (int f = 0; f < 8; ++f) d[f] = mfma(a[0], F[f], zero);
#pragma unroll
    for (int s = 1; s < 4; ++s)
#pragma unroll
      for (int f = 0; f < 8; ++f) d[f] = mfma(a[s], F[8 * s + f], d[f]);
    float t[4];
#pragma unroll
    for (int j = 0; j < 4; ++j) t[j] = rs32(d[j][0], d[j + 4][0]);
    const float y0 = rs16(t[0], t[2]), y1 = rs16(t[1], t[3]);
    st128_sc1(v4u{as_u(y0), tag, as_u(y1), tag}, rws, (lane >> 3) * 8192 + (lane & 7) * 16, WS_Y + ((b * 40 + 8 * w) * 64 + head * 2 + half) * 128);
    STAMPC(w == 0, 3);
    if (!is_ffn) return;
  } else {
    {
      FRESH_IDS();
      const rsrc_t rwo = mk_rsrc(wo, ABL & 1 ? 0 : DM * NH * DH * 2);
      const int vo = r * (DM * 2) + c * 16, so = head * DH * (DM * 2) + w * 256;
      unsigned char* scr = lds + L_BG + w * 16384;
#define OUT_LDS(s, t, f0, f1) *(v4u*)(lds + L_F4 + ((t) * 8 + 2 * (s)) * 1024 + lane * 16) = f0; *(v4u*)(lds + L_F4 + ((t) * 8 + 2 * (s) + 1) * 1024 + lane * 16) = f1;
#pragma nounroll
      for (int s = 0; s < 4; ++s) WO_STEP(s, OUT_LDS)
      asm volatile("s_waitcnt lgkmcnt(0)" ::: "memory");
      LFLAG(3) = 1u;
    }
    float Mo = 0.f, Lo = 0.f;
    bool sent = false;
    const int mls = WS_ML + (b * NH + head) * 256;
#define SEND_ML() { \
      const f32x4 m01 = *(const f32x4*)(lds + L_ML), m23 = *(const f32x4*)(lds + L_ML + 16); \
      Mo = fmaxf(fmaxf(m01[0], m01[2]), fmaxf(m23[0], m23[2])); \
      Lo = fmaf(__builtin_amdgcn_exp2f(m01[0] - Mo), m01[1], __builtin_amdgcn_exp2f(m01[2] - Mo) * m01[3]) + fmaf(__builtin_amdgcn_exp2f(m23[0] - Mo), m23[1], __builtin_amdgcn_exp2f(m23[2] - Mo) * m23[3]); \
      st128_sc1(v4u{as_u(Mo), tag, as_u(Lo), tag}, rws, fresh_lane() == 0 ? 0 : 0x40000000, mls + half * 128); sent = true; }
    STAGE_GU(if (!sent && __builtin_amdgcn_readfirstlane(LFLAG(6)) >= (unsigned)NSW) { asm volatile("" ::: "memory"); SEND_ML() })
    if (!sent) { LWAIT(6, NSW, 8); SEND_ML() }
    {
      v4u g;
      for (unsigned spin = 0;; ++spin) {
        g = __builtin_amdgcn_raw_buffer_load_b128(rws, 0, mls + (half ^ 1) * 128, AUX_SC1);
        if (__all(TAGBAD(g) == 0) || spin > SPIN_CAP) break;
        __builtin_amdgcn_s_sleep(16);
        asm volatile("" ::: "memory");
      }
      const float Mg = fmaxf(Mo, as_f(g[0]));
      const float Lg = __builtin_amdgcn_exp2f(Mo - Mg) * Lo + __builtin_amdgcn_exp2f(as_f(g[0]) - Mg) * as_f(g[2]);
      *(f32x2*)(lds + L_ML + 32) = f32x2{Mg, 1.0f / Lg};
      asm volatile("s_waitcnt lgkmcnt(0)" ::: "memory");
      LFLAG(7) = 1u;
      STAMPC(true, 13);
    }
    LWAIT(4, 1, 4);
    if (is_red) {
      FRESH_IDS();
      const int ys = WS_Y + (rb * 40 + cb) * 8192;
      v4u y[8];
      for (unsigned spin = 0;; ++spin) {
#pragma unroll
        for (int k = 0; k < 8; ++k) y[k] = __builtin_amdgcn_raw_buffer_load_b128(rws, lane * 16, ys + k * 1024, AUX_SC1);
        SB();
        unsigned bad = 0;
#pragma unroll
        for (int k = 0; k < 8; ++k) bad |= TAGBAD(y[k]);
        if (__all(bad == 0) || spin > SPIN_CAP) break;
        asm volatile("" ::: "memory");
      }
      STAMPC(true, 12);
      float s0 = ((as_f(y[0][0]) + as_f(y[1][0])) + (as_f(y[2][0]) + as_f(y[3][0]))) + ((as_f(y[4][0]) + as_f(y[5][0])) + (as_f(y[6][0]) + as_f(y[7][0])));
      float s1 = ((as_f(y[0][2]) + as_f(y[1][2])) + (as_f(y[2][2]) + as_f(y[3][2]))) + ((as_f(y[4][2]) + as_f(y[5][2])) + (as_f(y[6][2]) + as_f(y[7][2])));
      s0 += dppf<ROR8>(s0); s1 += dppf<ROR8>(s1);
      s0 = xrow_sum(s0); s1 = xrow_sum(s1);
      const unsigned hp2 = pk_bf16(lo_f(xres) + rbf(s0), hi_f(xres) + rbf(s1));
      __builtin_amdgcn_raw_buffer_store_b64(v2u{hp2, tag}, rws, lane < 8 ? lane * 8 : 0x40000000, WS_H + (rb * 320 + cb * 8) * 8, AUX_SC1);
      STAMPC(true, 4);
      v4u z[7];
      const int zs = WS_Z + (rb * 40 + cb) * (NFFN * 128);
      const bool has7 = lane < NFFN * 8 - 6 * 64;
      for (unsigned spin = 0;; ++spin) {
#pragma unroll
        for (int k = 0; k < 6; ++k) z[k] = __builtin_amdgcn_raw_buffer_load_b128(rws, lane * 16, zs + k * 1024, AUX_SC1);
        z[6] = __builtin_amdgcn_raw_buffer_load_b128(rws, has7 ? lane * 16 + 6144 : lane * 16, zs, AUX_SC1);
        SB();
        unsigned bad = 0;
#pragma unroll
        for (int k = 0; k < 7; ++k) bad |= TAGBAD(z[k]);
        if (__all(bad == 0) || spin > SPIN_CAP) break;
        asm volatile("" ::: "memory");
      }
      STAMPC(true, 14);
      s0 = ((as_f(z[0][0]) + as_f(z[1][0])) + (as_f(z[2][0]) + as_f(z[3][0]))) + ((as_f(z[4][0]) + as_f(z[5][0])) + (has7 ? as_f(z[6][0]) : 0.f));
      s1 = ((as_f(z[0][2]) + as_f(z[1][2])) + (as_f(z[2][2]) + as_f(z[3][2]))) + ((as_f(z[4][2]) + as_f(z[5][2])) + (has7 ? as_f(z[6][2]) : 0.f));
      s0 += dppf<ROR8>(s0); s1 += dppf<ROR8>(s1);
      s0 = xrow_sum(s0); s1 = xrow_sum(s1);
      const rsrc_t rout = mk_rsrc(out, NB * DM * 2);
      __builtin_amdgcn_raw_buffer_store_b32(pk_bf16(lo_f(hp2) + rbf(s0), hi_f(hp2) + rbf(s1)), rout, lane < 8 ? lane * 4 : 0x40000000, ri * 32, AUX_OUT);
      if (bid == 0) __builtin_amdgcn_raw_buffer_store_b32(tag, rws, lane == 0 ? 0 : 0x40000000, WS_EPOCH, AUX_SC1);
      STAMPC(true, 7);
    } else if (is_ffn) {
      LWAIT(2, NSW, 1);
    }
  }

  if (is_ffn) {
    FRESH_IDS();
    {
      const rsrc_t rd = mk_rsrc(wd, DM * FF * 2);
      v4u D[8];
#pragma unroll
      for (int n = 0; n < 8; ++n) D[n] = __builtin_amdgcn_raw_buffer_load_b128(rd, lane * 16, fi * 32 * (DM * 2) + (w * 8 + n) * 1024, AUX_W);
      SB();
      BAR();
#pragma unroll
      for (int n = 0; n < 8; ++n) *(v4u*)(lds + L_F4 + (w * 8 + n) * 1024 + lane * 16) = D[n];
    }
    v4u BG[20], BD[10];
    const f32x4 zero = {0.f, 0.f, 0.f, 0.f};
    unsigned char* gpw = lds + L_GP + (w * 4 + r) * GP_S + (c & 7) * 32 + (c >> 3) * 8;
    if (w < 4) {
#pragma unroll
      for (int s = 0; s < 5; ++s) {
        v2u t[8];
#pragma unroll
        for (int e = 0; e < 8; ++e) t[e] = *(const v2u*)(lds + L_BG + (160 * w + (s < 4 ? 32 * r + 4 * s + (e & 3) + 16 * (e >> 2) : 128 + 8 * r + e)) * 128 + c * 8);
#pragma unroll
        for (int d = 0; d < 4; ++d) {
          BG[4 * s + 0][d] = perm_lo(t[2 * d][0], t[2 * d + 1][0]); BG[4 * s + 1][d] = perm_hi(t[2 * d][0], t[2 * d + 1][0]);
          BG[4 * s + 2][d] = perm_lo(t[2 * d][1], t[2 * d + 1][1]); BG[4 * s + 3][d] = perm_hi(t[2 * d][1], t[2 * d + 1][1]);
        }
      }
      const v4u ones = {0x3f803f80u, 0x3f803f80u, 0x3f803f80u, 0x3f803f80u};
      f32x4 cs[4] = {zero, zero, zero, zero};
#pragma unroll
      for (int s = 0; s < 5; ++s)
#pragma unroll
        for (int f = 0; f < 4; ++f) cs[f] = mfma(ones, BG[4 * s + f], cs[f]);
      *(f32x2*)gpw = f32x2{cs[0][0], cs[1][0]};
      *(f32x2*)(gpw + 16) = f32x2{cs[2][0], cs[3][0]};
    }
    BAR();
    if (w == 4) { BAR();
      return;
    } else {
    const unsigned char* gpr = lds + L_GP + (c >> 2) * GP_S + (4 * r + (c & 3)) * 16;
    f32x4 CS = (*(const f32x4*)gpr + *(const f32x4*)(gpr + 4 * GP_S)) + (*(const f32x4*)(gpr + 8 * GP_S) + *(const f32x4*)(gpr + 12 * GP_S));
#pragma unroll
    for (int s = 0; s < 5; ++s) {
      unsigned t[8];
#pragma unroll
      for (int e = 0; e < 8; ++e) t[e] = *(const unsigned*)(lds + L_F4 + (8 * r + e) * (DM * 2) + w * 320 + s * 64 + c * 4);
#pragma unroll
      for (int d = 0; d < 4; ++d) { BD[2 * s][d] = perm_lo(t[2 * d], t[2 * d + 1]); BD[2 * s + 1][d] = perm_hi(t[2 * d], t[2 * d + 1]); }
    }
#pragma unroll
    for (int n = 0; n < 20; ++n) asm volatile("" : "+v"(BG[n]));
#pragma unroll
    for (int n = 0; n < 10; ++n) asm volatile("" : "+v"(BD[n]));
    asm volatile("" : "+v"(CS));
    BAR();
    STAMPC(w == 0, 15);
    v4u g0, g1, g2;
    {
      const int o0 = (c >> 2) * 2560 + r * 128 + (c & 3) * 16, o1 = (c >> 2) * 2560 + 512 + r * 32 + (c & 1) * 16, hs = WS_H + w * 640;
      for (unsigned spin = 0;; ++spin) {
        g0 = __builtin_amdgcn_raw_buffer_load_b128(rws, o0, hs, AUX_SC1);
        g1 = __builtin_amdgcn_raw_buffer_load_b128(rws, o0 + 64, hs, AUX_SC1);
        g2 = __builtin_amdgcn_raw_buffer_load_b128(rws, o1, hs, AUX_SC1);
        SB();
        const unsigned bad = TAGBAD(g0) | TAGBAD(g1) | TAGBAD(g2);
        if (__all(bad == 0) || spin > SPIN_CAP) break;
        asm volatile("" ::: "memory");
      }
    }
    STAMPC(true, 28 + w);
    const v4u a0 = {g0[0], g0[2], g1[0], g1[2]}, a1 = {dppu<0x00>(g2[0]), dppu<0x00>(g2[2]), dppu<0x55>(g2[0]), dppu<0x55>(g2[2])};
    f32x4 dd[4][4];
#pragma unroll
    for (int s = 0; s < 4; ++s)
#pragma unroll
      for (int f = 0; f < 4; ++f) dd[s][f] = mfma(a0, BG[4 * s + f], zero);
#pragma unroll
    for (int f = 0; f < 4; ++f) dd[0][f] = mfma(a1, BG[16 + f], dd[0][f]);
    {
      const unsigned ax = (c & 2) ? g2[2] : g2[0];
      float s1 = dot2(ax, 0x3f803f80u, 0.f), s2 = dot2(ax, ax, 0.f);
#pragma unroll
      for (int d = 0; d < 4; ++d) { s1 = dot2(a0[d], 0x3f803f80u, s1); s2 = dot2(a0[d], a0[d], s2); }
      float ss = rs32(s1, s2);
      ss = rs16(ss, ss);
      ss += dppf<XOR1>(ss); ss += dppf<XOR2>(ss);
      *(float*)(lds + L_ST + (c >> 2) * 32 + (lane >> 5) * 16 + w * 4) = ss;
    }
    float pv[4];
#pragma unroll
    for (int f = 0; f < 4; ++f) pv[f] = (dd[0][f][0] + dd[1][f][1]) + (dd[2][f][2] + dd[3][f][3]);
    *(f32x2*)gpw = f32x2{pv[0], pv[1]};
    *(f32x2*)(gpw + 16) = f32x2{pv[2], pv[3]};
    BAR();
    STAMPC(w == 0, 5);
    const f32x4 t1 = *(const f32x4*)(lds + L_ST + (c >> 2) * 32), t2 = *(const f32x4*)(lds + L_ST + (c >> 2) * 32 + 16);
    f32x4 gu[4];
#pragma unroll
    for (int ww = 0; ww < 4; ++ww) gu[ww] = *(const f32x4*)(gpr + ww * 4 * GP_S);
    SB();
    const float e1 = ((t1[0] + t1[1]) + (t1[2] + t1[3])) * (1.0f / DM), e2 = ((t2[0] + t2[1]) + (t2[2] + t2[3])) * (1.0f / DM);
    const float var = fmaxf(fmaf(-e1, e1, e2), 0.f);
    const float mb = rbf(e1), rden = __builtin_amdgcn_rcpf(rbf(__builtin_amdgcn_sqrtf(rbf(rbf(var) + 1e-5f))));
    const f32x4 GU = (((gu[0] + gu[1]) + (gu[2] + gu[3])) - mb * CS) * rden;
    float av[2];
#pragma unroll
    for (int j = 0; j < 2; ++j) {
      const float g = rbf(GU[j]), u = rbf(GU[2 + j]);
      const float u2 = 2.0f * 0.7978845608028654f * LOG2E * (g + 0.044715f * g * g * g);
      av[j] = rbf(g * __builtin_amdgcn_rcpf(1.0f + __builtin_amdgcn_exp2f(-u2))) * u;
    }
    const unsigned ap = pk_bf16(av[0], av[1]);
    const v4u af = {dppu<0x00>(ap), dppu<0x55>(ap), dppu<0xAA>(ap), dppu<0xFF>(ap)};
    f32x4 z[10];
#pragma unroll
    for (int f = 0; f < 10; ++f) z[f] = mfma(af, BD[f], zero);
#pragma unroll
    for (int s = 0; s < 5; ++s)
      st128_sc1(v4u{as_u(z[2 * s][0]), tag, as_u(z[2 * s + 1][0]), tag}, rws, (r * 40 + (c >> 3)) * (NFFN * 128) + (c & 7) * 16, WS_Z + ((10 * w + 2 * s) * NFFN + fi) * 128);
    }
  }
  STAMPC(w == 0, 6);

  HOLD_UNTIL(1, 100);
  return;
  }
  {
    const int w = wv - NHW;
    const int lane = threadIdx.x & 63, r = lane >> 4, c = lane & 15;
    const rsrc_t rk = mk_rsrc(kc, NB * NH * SK * DH * 2), rv = mk_rsrc(vc, NB * NH * SK * DH * 2);
    const int voff = lane * 16;
    if (is_q) { __builtin_amdgcn_s_barrier(); LWAIT(9, NHW, 1); }
    v4u B[NPRE][8];
    PREFETCH(NPRE, kvbase, if (!is_q) __builtin_amdgcn_s_barrier();)
    *(float*)(lds + L_SC + w * SC_S + 15 * 128 + lane * 4) = -INFINITY;
    LFLAG(10 + w) = 1u;
    STAMPC(w == 0, 11);
    LWAIT(1, 1, 1);
    const unsigned tag = __builtin_amdgcn_readfirstlane(LFLAG(5));
    const v4u q = *(const v4u*)(lds + L_Q + c * 16);
    STAMPC(w == 0, 1);

    int thr = __builtin_amdgcn_readfirstlane((half && pair_ffn) ? LAGF : is_q ? LAGQ : ns > 16 ? (half ? LAGPQ : LAGP) : LAGN);
    unsigned char* sc = lds + L_SC + w * SC_S + r * 32 + (c >> 1) * 4;
    int so = kvbase + (NPRE + naux) * STEP;
    KSTEP(0, 1, rk, AUX_K, so)
    KSTEP(1, 0, rk, AUX_K, 0)
    KSTEP(2, 0, rk, AUX_K, 0)
    KSTEP(3, 0, rk, AUX_K, 0)
    sc += naux * 128;
    for (int it = NPRE + naux; it < ns - 1; ++it) { so += STEP; KSTEP(0, 2, rk, AUX_K, so) STAMPC(w == 0 && it == 7, 20); }
    so = kvbase;
    KSTEP(0, 2, rv, AUX_V, so)
    STAMPC(w == 0, 21);
    float mw;
    {
      if (NAUX && naux) LWAIT(10 + w, 2, 1);
      asm volatile("" ::: "memory");
      const f32x4 s0 = *(const f32x4*)(lds + L_SC + w * SC_S + lane * 32), s1 = *(const f32x4*)(lds + L_SC + w * SC_S + lane * 32 + 16);
      const f32x4 s2 = *(const f32x4*)(lds + L_SC + w * SC_S + 2048 + (lane & 3) * 32), s3 = *(const f32x4*)(lds + L_SC + w * SC_S + 2048 + (lane & 3) * 32 + 16);
      float mx = -INFINITY;
#pragma unroll
      for (int j = 0; j < 4; ++j) mx = fmaxf(fmaxf(mx, fmaxf(s0[j], s1[j])), fmaxf(s2[j], s3[j]));
      mx = fmaxf(mx, dppf<ROR8>(mx)); mx = fmaxf(mx, dppf<ROR4>(mx)); mx = fmaxf(mx, dppf<ROR2>(mx)); mx = fmaxf(mx, dppf<ROR1>(mx));
      mw = xrow_max(mx) * LOG2E;
      float p[8], e[8];
#pragma unroll
      for (int j = 0; j < 4; ++j) { p[j] = __builtin_amdgcn_exp2f(fmaf(s0[j], LOG2E, -mw)); p[4 + j] = __builtin_amdgcn_exp2f(fmaf(s1[j], LOG2E, -mw));
                                    e[j] = __builtin_amdgcn_exp2f(fmaf(s2[j], LOG2E, -mw)); e[4 + j] = __builtin_amdgcn_exp2f(fmaf(s3[j], LOG2E, -mw)); }
      asm volatile("" ::: "memory");
      *(v4u*)(lds + L_SC + w * SC_S + lane * 16) = v4u{pk_bf16(p[0], p[1]), pk_bf16(p[2], p[3]), pk_bf16(p[4], p[5]), pk_bf16(p[6], p[7])};
      *(v4u*)(lds + L_SC + w * SC_S + 1024 + (lane & 3) * 16) = v4u{pk_bf16(e[0], e[1]), pk_bf16(e[2], e[3]), pk_bf16(e[4], e[5]), pk_bf16(e[6], e[7])};
      const float le = ((e[0] + e[1]) + (e[2] + e[3])) + ((e[4] + e[5]) + (e[6] + e[7]));
      float l = ((p[0] + p[1]) + (p[2] + p[3])) + ((p[4] + p[5]) + (p[6] + p[7])) + (lane < 4 ? le : 0.f);
      l += dppf<ROR8>(l); l += dppf<ROR4>(l); l += dppf<ROR2>(l); l += dppf<ROR1>(l);
      l = xrow_sum(l);
      if (lane == 0) *(f32x2*)(lds + L_ML + w * 8) = f32x2{mw, l};
      LADD(6);
      SB();
    }
    float acc[8];
#pragma unroll
    for (int e = 0; e < 8; ++e) acc[e] = 0.f;
    const unsigned char* pp = lds + L_SC + w * SC_S + r * 16;
#define VSTEP(MODE, NXT) { \
    const v4u pw = *(const v4u*)pp; pp += 64; \
    _Pragma("unroll") for (int j = 0; j < 8; ++j) { \
      const unsigned u = pw[j >> 1], pl = (j & 1) ? u >> 16 : u & 0xffffu, ph = (j & 1) ? u & 0xffff0000u : u << 16; \
      _Pragma("unroll") for (int d = 0; d < 4; ++d) { acc[2 * d] = dot2(B[0][j][d], pl, acc[2 * d]); acc[2 * d + 1] = dot2(B[0][j][d], ph, acc[2 * d + 1]); } \
      asm volatile("" : "+v"(acc[0]), "+v"(acc[1]), "+v"(acc[2]), "+v"(acc[3]), "+v"(acc[4]), "+v"(acc[5]), "+v"(acc[6]), "+v"(acc[7])); SB(); \
      REFILL(j, MODE, rv, AUX_V, NXT) } }
    for (int it = 0; it < ns - 1; ++it) { so += STEP; thr = it + BOOST >= ns ? 0 : thr; VSTEP(2, so) STAMPC(w == 0 && it == 7, 22); }
    if (w == 0) LFLAG(4) = 1u;
    LWAIT(3, 1, 1);
    LWAIT(7, 1, 1);
#pragma unroll
    for (int n = 0; n < 8; ++n) B[1][n] = *(const v4u*)(lds + L_F4 + (w * 8 + n) * 1024 + lane * 16);
    const f32x2 gl = *(const f32x2*)(lds + L_ML + 32);
    float f = __builtin_amdgcn_exp2f(mw - gl.x) * gl.y;
    asm volatile("" : "+v"(f));
    SB();
    VSTEP(0, 0)
    STAMPC(true, 16 + w);
    {
      float t[4];
#pragma unroll
      for (int j = 0; j < 4; ++j) t[j] = rs32(acc[j], acc[j + 4]);
      *(unsigned*)(lds + L_OP + w * OP_S + (4 * c + r) * 4) = pk_bf16(rs16(t[0], t[2]) * f, rs16(t[1], t[3]) * f);
    }
    LADD(2);
    LWAIT(2, NSW, 0);
    {
      const rsrc_t rws = mk_rsrc(ws, WS_END);
      v4u a[4];
#pragma unroll
      for (int s = 0; s < 4; ++s) a[s] = *(const v4u*)(lds + L_OP + (c >> 2) * OP_S + s * 64 + r * 16);
      f32x4 d0 = {0.f, 0.f, 0.f, 0.f}, d1 = {0.f, 0.f, 0.f, 0.f};
#pragma unroll
      for (int s = 0; s < 4; ++s) { d0 = mfma(a[s], B[1][2 * s], d0); d1 = mfma(a[s], B[1][2 * s + 1], d1); }
      const float y0 = xrow_sum(d0[0]), y1 = xrow_sum(d1[0]);
      st128_sc1(v4u{as_u(y0), tag, as_u(y1), tag}, rws, lane < 16 ? (c >> 3) * 8192 + (c & 7) * 16 : 0x40000000, WS_Y + ((b * 40 + 32 + 2 * w) * 64 + head * 2 + half) * 128);
    }
  }
}

extern "C" void kernel_launch(void* const* d_in, const int* in_sizes, int n_in, void* d_out, int out_size, void* d_ws, size_t ws_size, hipStream_t stream) {
  fused_block<<<NWG, NT, 0, stream>>>((const u16*)d_in[1], (const u16*)d_in[2], d_ws, (const u16*)d_in[0], (const u16*)d_in[3], (const u16*)d_in[4],
                                      (const u16*)d_in[6], (const u16*)d_in[7], (const u16*)d_in[8], (u16*)d_out);
}
```

```cpp
#define SWZ 0
#define NAUX 2
#define LAGQ 2
#include <hip/hip_runtime.h>
#include <hip/hip_bf16.h>
#include <stdint.h>

typedef unsigned short u16;
typedef unsigned v4u __attribute__((ext_vector_type(4)));
typedef unsigned v2u __attribute__((ext_vector_type(2)));
typedef float f32x2 __attribute__((ext_vector_type(2)));
typedef float f32x4 __attribute__((ext_vector_type(4)));
typedef __bf16 bf16x2 __attribute__((ext_vector_type(2)));
typedef __bf16 bf16x8 __attribute__((ext_vector_type(8)));
typedef __amdgpu_buffer_rsrc_t rsrc_t;
#define GAS __attribute__((address_space(1)))
#define LAS __attribute__((address_space(3)))

#define LAGN 2
#define LAGF 0
#define SKEW 0
#define PW 4
#define PG 2
#define GU_FIRST 0
#define AUX_X 0
#define DPRE 8
#define QDELAY 0
#define AUX_QP 16
#define AUX_EP 16
#define ABL 0
#define PRE0 4
#define AUX_K 2
#define AUX_V 2
#define AUX_W 0
#define AUX_OUT 17
#define PWN 2
#define AUX_WQ 2
#define SHF 4
#define SHQ 4
#define LAGP 2
#define WAITQ 0
#define LAGPQ 2
#define BOOST 4
#define HREP 8
#define ROTA 4
#define ROTB 8
#define MVF 1
#define MVQ 1
#define GULATE 0
#define QAUX 1
constexpr int NPRE = 4;

constexpr int NB = 4, DM = 640, NH = 32, SK = 4096, DH = 128, FF = 1728;
constexpr int NSW = 4, NHW = 5, NT = 64 * (NSW + NHW), NWG = 256;
constexpr int NRED = 160, NFFN = 54;
constexpr int WS_EPOCH = 0;
constexpr int WS_ML = 32768;
constexpr int WS_H_OLD = 8192;
constexpr int WS_Q = 65536;
constexpr int WS_Y = WS_Q + NB * NH * 2048;
constexpr int WS_Z = WS_Y + NB * 40 * 64 * 128;
constexpr int WS_H = WS_Z + NB * 40 * NFFN * 128;
constexpr int WS_END = WS_H + 8 * 10240;
constexpr int WS_STAMP = WS_END;
constexpr int AUX_NT = 2, AUX_SC1 = 16;
constexpr unsigned SPIN_CAP = 1u << 18;
constexpr float LOG2E = 1.4426950408889634f;

#define STAMPC(cond, i) do { } while (0)

__device__ __forceinline__ rsrc_t mk_rsrc(const void* p, int bytes) { return __builtin_amdgcn_make_buffer_rsrc((void*)p, 0, bytes, 0x00020000); }
__device__ __forceinline__ float as_f(unsigned u) { return __builtin_bit_cast(float, u); }
__device__ __forceinline__ unsigned as_u(float f) { return __builtin_bit_cast(unsigned, f); }
__device__ __forceinline__ unsigned pk_bf16(float lo, float hi) { f32x2 v = {lo, hi}; return __builtin_bit_cast(unsigned, __builtin_convertvector(v, bf16x2)); }
__device__ __forceinline__ float rbf(float x) { return as_f(pk_bf16(0.f, x)); }
__device__ __forceinline__ float lo_f(unsigned u) { return as_f(u << 16); }
__device__ __forceinline__ float hi_f(unsigned u) { return as_f(u & 0xffff0000u); }
__device__ __forceinline__ float dot2(unsigned a, unsigned b, float c) { return __builtin_amdgcn_fdot2_f32_bf16(__builtin_bit_cast(bf16x2, a), __builtin_bit_cast(bf16x2, b), c, false); }
__device__ __forceinline__ f32x4 mfma(v4u a, v4u b, f32x4 c) { return __builtin_amdgcn_mfma_f32_16x16x32_bf16(__builtin_bit_cast(bf16x8, a), __builtin_bit_cast(bf16x8, b), c, 0, 0, 0); }
template <int CTRL> __device__ __forceinline__ unsigned dppu(unsigned x) { return __builtin_amdgcn_mov_dpp((int)x, CTRL, 0xf, 0xf, true); }
template <int CTRL> __device__ __forceinline__ float dppf(float x) { return as_f(dppu<CTRL>(as_u(x))); }
constexpr int ROR1 = 0x121, ROR2 = 0x122, ROR4 = 0x124, ROR8 = 0x128, XOR1 = 0xB1, XOR2 = 0x4E, XOR7 = 0x141, BCAST = 0x150;
__device__ __forceinline__ unsigned perm_lo(unsigned a, unsigned b) { return __builtin_amdgcn_perm(b, a, 0x05040100u); }
__device__ __forceinline__ unsigned perm_hi(unsigned a, unsigned b) { return __builtin_amdgcn_perm(b, a, 0x07060302u); }
__device__ __forceinline__ float rs16(float a, float b) { auto s = __builtin_amdgcn_permlane16_swap(as_u(a), as_u(b), false, false); return as_f(s[0]) + as_f(s[1]); }
__device__ __forceinline__ float rs32(float a, float b) { auto s = __builtin_amdgcn_permlane32_swap(as_u(a), as_u(b), false, false); return as_f(s[0]) + as_f(s[1]); }
__device__ __forceinline__ float xrow_sum(float x) { x = rs16(x, x); return rs32(x, x); }
__device__ __forceinline__ float xrow_max(float x) {
  auto s = __builtin_amdgcn_permlane16_swap(as_u(x), as_u(x), false, false); x = fmaxf(as_f(s[0]), as_f(s[1]));
  auto t = __builtin_amdgcn_permlane32_swap(as_u(x), as_u(x), false, false); return fmaxf(as_f(t[0]), as_f(t[1]));
}
__device__ __forceinline__ void st128_sc1(v4u v, rsrc_t rs, int voff, int soff) {
  __builtin_amdgcn_raw_buffer_store_b128(v, rs, voff, soff, AUX_SC1);
  asm volatile("s_nop 1" :: "v"(v) : "memory");
}
__device__ __forceinline__ int fresh_lane() { int l; asm volatile("v_mbcnt_lo_u32_b32 %0, -1, 0\n\tv_mbcnt_hi_u32_b32 %0, -1, %0" : "=v"(l)); return l; }
__device__ __forceinline__ unsigned ln_pair(unsigned v, float mb, float rden) { return pk_bf16(rbf(lo_f(v) - mb) * rden, rbf(hi_f(v) - mb) * rden); }
__device__ __forceinline__ f32x2 ln_stats(const unsigned (&hv)[5]) {
  const float piv = as_f(__builtin_amdgcn_readfirstlane(hv[0] << 16));
  float s1 = 0.f, s2 = 0.f;
#pragma unroll
  for (int j = 0; j < 5; ++j) { const float d0 = lo_f(hv[j]) - piv, d1 = hi_f(hv[j]) - piv; s1 += d0 + d1; s2 = fmaf(d0, d0, s2); s2 = fmaf(d1, d1, s2); }
  float ss = rs32(s1, s2);
  ss = rs16(ss, ss);
  ss += dppf<ROR8>(ss); ss += dppf<ROR4>(ss); ss += dppf<ROR2>(ss); ss += dppf<ROR1>(ss);
  const float e1 = as_f(__builtin_amdgcn_readlane(as_u(ss), 0)) * (1.0f / DM), e2 = as_f(__builtin_amdgcn_readlane(as_u(ss), 32)) * (1.0f / DM);
  const float var = fmaxf(fmaf(-e1, e1, e2), 0.f);
  return f32x2{rbf(piv + e1), __builtin_amdgcn_rcpf(rbf(__builtin_amdgcn_sqrtf(rbf(rbf(var) + 1e-5f))))};
}
#define SB() __builtin_amdgcn_sched_barrier(0)
#define VM0() do { SB(); asm volatile("s_waitcnt vmcnt(0)" ::: "memory"); SB(); } while (0)
#define BAR() do { asm volatile("s_waitcnt lgkmcnt(0)" ::: "memory"); __builtin_amdgcn_s_barrier(); asm volatile("" ::: "memory"); } while (0)
#define TAGBAD(g) (((g)[1] ^ tag) | ((g)[3] ^ tag))
#define FRESH_IDS() const int lane = fresh_lane(); const int r = lane >> 4, c = lane & 15, htid = w * 64 + lane; (void)r; (void)c; (void)htid

constexpr int L_FLAG = 0;
constexpr int L_ML = 64;
constexpr int L_Q = 128;
constexpr int OP_S = 320;
constexpr int L_ST = 384;
constexpr int L_OP = 512;
constexpr int GP_S = 384;
constexpr int L_GP = L_OP + 4 * OP_S;
constexpr int L_BG = L_GP + 5 * 4 * GP_S;
constexpr int L_F4 = L_BG + 640 * 128;
constexpr int L_SC = L_F4 + 32 * 1024;
constexpr int SC_S = 2304;
constexpr int L_END = L_SC + 4 * SC_S;
static_assert(L_BG % 16 == 0 && L_OP % 16 == 0 && L_GP % 16 == 0, "align");
#define LFLAG(i) (*(volatile LAS unsigned*)(LAS unsigned char*)(lds + L_FLAG + 4 * (i)))
#define LWAIT(i, n, SLP) do { unsigned spin_ = 0; _Pragma("nounroll") while (LFLAG(i) < (unsigned)(n) && ++spin_ < (SPIN_CAP << 4)) { if (SLP) __builtin_amdgcn_s_sleep(SLP); } asm volatile("" ::: "memory"); } while (0)
#define LADD(i) do { asm volatile("s_waitcnt lgkmcnt(0)" ::: "memory"); if (fresh_lane() == 0) __hip_atomic_fetch_add((LAS unsigned*)(LAS unsigned char*)(lds + L_FLAG + 4 * (i)), 1u, __ATOMIC_RELAXED, __HIP_MEMORY_SCOPE_WORKGROUP); asm volatile("" ::: "memory"); } while (0)

__device__ __forceinline__ int rank_of(int bid) { return (bid & 7) + 8 * (bid >> 4); }
__device__ __forceinline__ int ffn_of_rank(int j) { const int s = j - 64; if (s < 0) return -1; const int k = (s & 7) + 8 * (s >> 4); return k < 27 ? 2 * k + ((s >> 3) & 1) : -1; }

__global__ void __launch_bounds__(NT, 3) fused_block(const u16* __restrict__ kc, const u16* __restrict__ vc, void* ws, const u16* __restrict__ x,
                                                     const u16* __restrict__ wq, const u16* __restrict__ wo, const u16* __restrict__ wg,
                                                     const u16* __restrict__ wu, const u16* __restrict__ wd, u16* __restrict__ out) {
  __shared__ __attribute__((aligned(16))) unsigned char lds[L_END + 256];
  const int wv = __builtin_amdgcn_readfirstlane(threadIdx.x >> 6);
  const int bid = blockIdx.x, xs = bid & 7, rr = bid >> 3;
  const int head = xs + 8 * (rr >> 3), b = (rr >> 1) & 3, half = rr & 1;
  STAMPC(wv == 0, 0);
#define HOLD_UNTIL(k, us) do { } while (0)

  constexpr int STEP = 2 * NSW * 4096;
  const int rank = rank_of(bid);
  const bool pair_ffn = ffn_of_rank(rank) >= 0, is_q = rank < 64;
  const int sw = wv >= NHW ? wv - NHW : wv;
  const int mv = pair_ffn ? MVF : 0;
  const int ns = half ? 16 - mv : 16 + mv;
  const int kvbase = (b * NH + head) * (SK * DH * 2) + (half ? (16 + mv) * STEP : 0) + sw * 4096;
  const int naux = ((is_q && !QAUX) || (half && pair_ffn)) ? 0 : NAUX;
  const int kvend = kvbase + ns * STEP, kv0 = kvbase + (ROTA * (head & 1) + ROTB * ((head >> 2) & 1)) * STEP;
#define NEXT(so) ((so) + STEP == kvend ? kvbase : (so) + STEP)
  const int t2off = NSW * 4096 + (SWZ ? ((sw ^ SWZ) - sw) * 4096 : 0);
#define LD(RS, AUX, i, so) __builtin_amdgcn_raw_buffer_load_b128(RS, voff, (so) + ((i) >> 2) * t2off + ((i) & 3) * 1024, AUX)
#define PREFETCH(N, AT8) \
    _Pragma("unroll") for (int t = 0; t < N; ++t, so = NEXT(so)) \
      _Pragma("unroll") for (int i = 0; i < 8; ++i) { \
        B[t][i] = LD(rk, AUX_K, i, so); \
        if (t == 0 && i == 7) { SB(); AT8 SB(); } \
        if (8 * t + i + 1 >= DPRE && 8 * t + i + 1 < 8 * N) { SB(); asm volatile("s_waitcnt vmcnt(%0)" :: "i"(DPRE - 1) : "memory"); SB(); } } \
    SB();
#define REFILL(i, MODE, RS, AUX, NXT) if (MODE) { if (MODE == 2) THROTTLE(); B[0][i] = LD(RS, AUX, i, NXT); SB(); }
#define KSTEP(T, MODE, RS, AUX, NXT) { \
    float sx[8]; \
    _Pragma("unroll") for (int j = 0; j < 8; ++j) { \
      float d = dot2(B[T][j][0], q[0], 0.f); d = dot2(B[T][j][1], q[1], d); d = dot2(B[T][j][2], q[2], d); d = dot2(B[T][j][3], q[3], d); \
      asm volatile("" : "+v"(d)); sx[j] = d; SB(); REFILL(j, MODE, RS, AUX, NXT) } \
    float t4[4], t2[2]; \
    _Pragma("unroll") for (int j = 0; j < 4; ++j) { const float lo = sx[j] + dppf<ROR8>(sx[j]), hi = sx[j + 4] + dppf<ROR8>(sx[j + 4]); t4[j] = (c & 8) ? hi : lo; } \
    _Pragma("unroll") for (int j = 0; j < 2; ++j) { const float lo = t4[j] + dppf<XOR7>(t4[j]), hi = t4[j + 2] + dppf<XOR7>(t4[j + 2]); t2[j] = (c & 4) ? hi : lo; } \
    const float lo = t2[0] + dppf<XOR2>(t2[0]), hi = t2[1] + dppf<XOR2>(t2[1]); \
    float s = (c & 2) ? hi : lo; \
    s += dppf<XOR1>(s); \
    *(float*)sc = rbf(rbf(s) * 0.08838834764831845f); sc += 128; SB(); }

#define THROTTLE() asm volatile("s_cmp_lt_u32 %0, 1\n\ts_cbranch_scc1 1f\n\ts_waitcnt vmcnt(6)\n\ts_cmp_lt_u32 %0, 2\n\ts_cbranch_scc1 1f\n\ts_waitcnt vmcnt(5)\n1:" :: "s"(thr) : "scc", "memory")
  if (wv < NHW) {
  const int w = wv;
  const int ri = half * 128 + rank_of(bid);
  __builtin_amdgcn_s_setprio(2);
  if (threadIdx.x < 16) *(unsigned*)(lds + L_FLAG + threadIdx.x * 4) = 0u;
  const rsrc_t rws = mk_rsrc(ws, WS_END);
  unsigned tag, xres;
  {
    FRESH_IDS();
    const rsrc_t rx = mk_rsrc(x, NB * DM * 2);
    if (is_q) {
      const rsrc_t rwq = mk_rsrc(wq, DM * NH * DH * 2);
      const v4u x8 = __builtin_amdgcn_raw_buffer_load_b128(rx, lane * 16, w * (DM * 2), AUX_X);
      const unsigned x2 = __builtin_amdgcn_raw_buffer_load_b32(rx, lane * 4 + 1024, w * (DM * 2), AUX_X);
      v4u xa[2];
#pragma unroll
      for (int s = 0; s < 2; ++s) xa[s] = __builtin_amdgcn_raw_buffer_load_b128(rx, (c >> 2) * (DM * 2) + r * 16, (320 * half + 64 * w + 32 * s) * 2, AUX_X);
      SB();
      v2u Wq[16];
#pragma unroll
      for (int n = 0; n < 16; ++n) Wq[n] = __builtin_amdgcn_raw_buffer_load_b64(rwq, (8 * r) * 8192 + c * 8, (320 * half + 64 * w + 32 * (n >> 3) + (n & 7)) * 8192 + rank * 128, AUX_WQ);
      const unsigned epoch_v = __builtin_amdgcn_raw_buffer_load_b32(rws, 0, WS_EPOCH, AUX_SC1);
      xres = __builtin_amdgcn_raw_buffer_load_b32(rx, (lane & 7) * 4, ri * 32, AUX_X);
      SB();
      BAR();
      if (w < 4) {
        const unsigned hv[5] = {x8[0], x8[1], x8[2], x8[3], x2};
        *(f32x2*)(lds + L_ST + w * 8) = ln_stats(hv);
        LADD(8);
      }
      STAMPC(w == 0, 8);
      LWAIT(8, 4, 0);
      const f32x2 st = *(const f32x2*)(lds + L_ST + (c >> 2) * 8);
#pragma unroll
      for (int s = 0; s < 2; ++s) { xa[s] = v4u{ln_pair(xa[s][0], st.x, st.y), ln_pair(xa[s][1], st.x, st.y), ln_pair(xa[s][2], st.x, st.y), ln_pair(xa[s][3], st.x, st.y)}; asm volatile("" : "+v"(xa[s])); }
      SB();
      STAMPC(w == 0, 27);
      const f32x4 zero = {0.f, 0.f, 0.f, 0.f};
      f32x4 dq[4] = {zero, zero, zero, zero};
#pragma unroll
      for (int s = 0; s < 2; ++s) {
#pragma unroll
        for (int f = 0; f < 4; ++f) {
          v4u bf;
#pragma unroll
          for (int d = 0; d < 4; ++d) bf[d] = (f & 1) ? perm_hi(Wq[8 * s + 2 * d][f >> 1], Wq[8 * s + 2 * d + 1][f >> 1]) : perm_lo(Wq[8 * s + 2 * d][f >> 1], Wq[8 * s + 2 * d + 1][f >> 1]);
          dq[f] = mfma(xa[s], bf, dq[f]);
        }
        SB();
      }
      *(f32x4*)(lds + L_GP + w * 1024 + lane * 16) = f32x4{dq[0][0], dq[1][0], dq[2][0], dq[3][0]};
      tag = __builtin_amdgcn_readfirstlane(epoch_v) + 1u;
      LADD(0);
      STAMPC(w == 0, 24);
      if (w == 1 || w == 2) {
        LWAIT(0, NHW, 0);
        const int u = htid - 64;
        f32x2 pq[NHW];
#pragma unroll
        for (int ww = 0; ww < NHW; ++ww) pq[ww] = *(const f32x2*)(lds + L_GP + ww * 1024 + u * 8);
        const f32x2 sq = ((pq[0] + pq[1]) + (pq[2] + pq[3])) + pq[4];
        st128_sc1(v4u{as_u(sq.x), tag, as_u(sq.y), tag}, rws, (u >> 5) * (NH * 2048) + (u & 31) * 16, WS_Q + (rank >> 1) * 2048 + half * 1024 + (rank & 1) * 512);
        STAMPC(w == 1, 25);
      }
    } else {
      const unsigned epoch_v = __builtin_amdgcn_raw_buffer_load_b32(rws, 0, WS_EPOCH, AUX_SC1);
      xres = __builtin_amdgcn_raw_buffer_load_b32(rx, (lane & 7) * 4, ri * 32, AUX_X);
      SB();
      BAR();
      tag = __builtin_amdgcn_readfirstlane(epoch_v) + 1u;
    }
    if (w == 4) {
      v4u g0, g1;
      for (unsigned spin = 0;; ++spin) {
        g0 = __builtin_amdgcn_raw_buffer_load_b128(rws, lane * 16, WS_Q + (b * NH + head) * 2048, AUX_SC1);
        g1 = __builtin_amdgcn_raw_buffer_load_b128(rws, lane * 16 + 1024, WS_Q + (b * NH + head) * 2048, AUX_SC1);
        SB();
        if (__all((TAGBAD(g0) | TAGBAD(g1)) == 0) || spin > SPIN_CAP) break;
        asm volatile("" ::: "memory");
      }
      *(unsigned*)(lds + L_Q + lane * 4) = pk_bf16(as_f(g0[0]) + as_f(g1[0]), as_f(g0[2]) + as_f(g1[2]));
      LFLAG(5) = tag;
      asm volatile("s_waitcnt lgkmcnt(0)" ::: "memory");
      LFLAG(1) = 1u;
      STAMPC(true, 26);
    } else if (NAUX && naux) {
      const rsrc_t rk = mk_rsrc(kc, NB * NH * SK * DH * 2);
      const int voff = lane * 16;
      const int thr = 0;
      v4u B[NAUX ? NAUX : 1][8];
      LWAIT(10 + w, 1, 1);
      int so = kv0;
#pragma unroll
      for (int t = 0; t < NPRE; ++t) so = NEXT(so);
      PREFETCH(NAUX, )
      LWAIT(1, 1, 1);
      const v4u q = *(const v4u*)(lds + L_Q + c * 16);
      unsigned char* sc = lds + L_SC + w * SC_S + NPRE * 128 + r * 32 + (c >> 1) * 4;
#pragma unroll
      for (int t = 0; t < NAUX; ++t) KSTEP(t, 0, rk, AUX_K, 0)
      asm volatile("s_waitcnt lgkmcnt(0)" ::: "memory");
      LFLAG(10 + w) = 2u;
    }
  }

  const bool is_red = ri < NRED;
  const int rb = ri / 40, cb = ri % 40;
  const int fi = half ? ffn_of_rank(rank_of(bid)) : -1;
  const bool is_ffn = fi >= 0;

#define STAGE_GU(S0, S1, HOOK) if (is_ffn) { \
    const int ln = fresh_lane(); \
    const GAS unsigned char* gp = (const GAS unsigned char*)((ln & 4) ? wu : wg) + (size_t)((128 * w + (ln >> 3)) * (FF * 2) + fi * 64 + (ln & 3) * 16) + (size_t)(S0) * 8 * PG * (FF * 2); \
    _Pragma("nounroll") for (int s = (S0); s < (S1); ++s) { \
      HOOK \
      v4u GU[PG]; \
      _Pragma("unroll") for (int n = 0; n < PG; ++n) GU[n] = AUX_W ? __builtin_nontemporal_load((const GAS v4u*)(gp + 8 * n * (FF * 2))) : *(const GAS v4u*)(gp + 8 * n * (FF * 2)); \
      gp += 8 * PG * (FF * 2); \
      _Pragma("unroll") for (int n = 0; n < PG; ++n) *(v4u*)(lds + L_BG + (w * 16 + s * PG + n) * 1024 + ln * 16) = GU[n]; \
    } }
#define WO_STEP(s, OUT) { \
    v4u T[8]; \
    _Pragma("unroll") for (int e = 0; e < 8; ++e) { T[e] = __builtin_amdgcn_raw_buffer_load_b128(rwo, vo, so + (32 * (s) + 4 * e) * (DM * 2), AUX_W); if (e % 4 == 3) VM0(); else if (e % PWN == PWN - 1) { SB(); asm volatile("s_cmp_lt_i32 %0, 0\n\ts_cbranch_scc0 1f\n\ts_waitcnt vmcnt(0)\n1:" :: "s"(fi) : "scc", "memory"); SB(); } } \
    _Pragma("unroll") for (int e = 0; e < 8; ++e) *(v4u*)(scr + e * 1024 + lane * 16) = T[e]; \
    asm volatile("" ::: "memory"); \
    _Pragma("unroll") for (int t = 0; t < 4; ++t) { \
      unsigned u[8]; \
      _Pragma("unroll") for (int e = 0; e < 8; ++e) u[e] = *(const unsigned*)(scr + (8 * r + e) * 256 + t * 64 + c * 4); \
      v4u f0, f1; \
      _Pragma("unroll") for (int d = 0; d < 4; ++d) { f0[d] = perm_lo(u[2 * d], u[2 * d + 1]); f1[d] = perm_hi(u[2 * d], u[2 * d + 1]); } \
      OUT(s, t, f0, f1) } \
    asm volatile("" ::: "memory"); SB(); }
  if (w < 4) {
    v4u F[32];
    {
      FRESH_IDS();
      const rsrc_t rwo = mk_rsrc(wo, ABL & 1 ? 0 : DM * NH * DH * 2);
      const int vo = r * (DM * 2) + c * 16, so = head * DH * (DM * 2) + w * 256;
      unsigned char* scr = lds + L_BG + w * 16384;
#define OUT_REG(s, t, f0, f1) F[8 * (s) + 2 * (t)] = f0; F[8 * (s) + 2 * (t) + 1] = f1; asm volatile("" : "+v"(F[8 * (s) + 2 * (t)]), "+v"(F[8 * (s) + 2 * (t) + 1]));
      WO_STEP(0, OUT_REG) WO_STEP(1, OUT_REG) WO_STEP(2, OUT_REG) WO_STEP(3, OUT_REG)
    }
    STAMPC(w == 0, 9);
    STAGE_GU(0, 16 / PG - GULATE, )
    if (!GULATE && is_ffn) LADD(9);
    STAMPC(w == 0, 10);
    HOLD_UNTIL(2, 80);
    LWAIT(4, 1, 4);
    LWAIT(2, NSW, 0);
    STAMPC(w == 0, 2);
    FRESH_IDS();
    v4u a[4];
#pragma unroll
    for (int s = 0; s < 4; ++s) a[s] = *(const v4u*)(lds + L_OP + (c >> 2) * OP_S + s * 64 + r * 16);
    const f32x4 zero = {0.f, 0.f, 0.f, 0.f};
    f32x4 d[8];
#pragma unroll
    for (int f = 0; f < 8; ++f) d[f] = mfma(a[0], F[f], zero);
#pragma unroll
    for (int s = 1; s < 4; ++s)
#pragma unroll
      for (int f = 0; f < 8; ++f) d[f] = mfma(a[s], F[8 * s + f], d[f]);
    float t[4];
#pragma unroll
    for (int j = 0; j < 4; ++j) t[j] = rs32(d[j][0], d[j + 4][0]);
    const float y0 = rs16(t[0], t[2]), y1 = rs16(t[1], t[3]);
    st128_sc1(v4u{as_u(y0), tag, as_u(y1), tag}, rws, (lane >> 3) * 8192 + (lane & 7) * 16, WS_Y + ((b * 40 + 8 * w) * 64 + head * 2 + half) * 128);
    STAMPC(w == 0, 3);
    if (!is_ffn) return;
  } else {
    {
      FRESH_IDS();
      const rsrc_t rwo = mk_rsrc(wo, ABL & 1 ? 0 : DM * NH * DH * 2);
      const int vo = r * (DM * 2) + c * 16, so = head * DH * (DM * 2) + w * 256;
      unsigned char* scr = lds + L_BG + w * 16384;
#define OUT_LDS(s, t, f0, f1) *(v4u*)(lds + L_F4 + ((t) * 8 + 2 * (s)) * 1024 + lane * 16) = f0; *(v4u*)(lds + L_F4 + ((t) * 8 + 2 * (s) + 1) * 1024 + lane * 16) = f1;
#pragma nounroll
      for (int s = 0; s < 4; ++s) WO_STEP(s, OUT_LDS)
      asm volatile("s_waitcnt lgkmcnt(0)" ::: "memory");
      LFLAG(3) = 1u;
    }
    float Mo = 0.f, Lo = 0.f;
    bool sent = false;
    const int mls = WS_ML + (b * NH + head) * 256;
#define SEND_ML() { \
      const f32x4 m01 = *(const f32x4*)(lds + L_ML), m23 = *(const f32x4*)(lds + L_ML + 16); \
      Mo = fmaxf(fmaxf(m01[0], m01[2]), fmaxf(m23[0], m23[2])); \
      Lo = fmaf(__builtin_amdgcn_exp2f(m01[0] - Mo), m01[1], __builtin_amdgcn_exp2f(m01[2] - Mo) * m01[3]) + fmaf(__builtin_amdgcn_exp2f(m23[0] - Mo), m23[1], __builtin_amdgcn_exp2f(m23[2] - Mo) * m23[3]); \
      st128_sc1(v4u{as_u(Mo), tag, as_u(Lo), tag}, rws, fresh_lane() == 0 ? 0 : 0x40000000, mls + half * 128); sent = true; }
    STAGE_GU(0, 16 / PG - GULATE, if (!sent && __builtin_amdgcn_readfirstlane(LFLAG(6)) >= (unsigned)NSW) { asm volatile("" ::: "memory"); SEND_ML() })
    if (!GULATE && is_ffn) LADD(9);
    if (!sent) { LWAIT(6, NSW, 8); SEND_ML() }
    {
      v4u g;
      for (unsigned spin = 0;; ++spin) {
        g = __builtin_amdgcn_raw_buffer_load_b128(rws, 0, mls + (half ^ 1) * 128, AUX_SC1);
        if (__all(TAGBAD(g) == 0) || spin > SPIN_CAP) break;
        __builtin_amdgcn_s_sleep(16);
        asm volatile("" ::: "memory");
      }
      const float Mg = fmaxf(Mo, as_f(g[0]));
      const float Lg = __builtin_amdgcn_exp2f(Mo - Mg) * Lo + __builtin_amdgcn_exp2f(as_f(g[0]) - Mg) * as_f(g[2]);
      *(f32x2*)(lds + L_ML + 32) = f32x2{Mg, 1.0f / Lg};
      asm volatile("s_waitcnt lgkmcnt(0)" ::: "memory");
      LFLAG(7) = 1u;
      STAMPC(true, 13);
    }
    LWAIT(4, 1, 4);
    if (is_red) {
      FRESH_IDS();
      const int ys = WS_Y + (rb * 40 + cb) * 8192;
      v4u y[8];
      for (unsigned spin = 0;; ++spin) {
#pragma unroll
        for (int k = 0; k < 8; ++k) y[k] = __builtin_amdgcn_raw_buffer_load_b128(rws, lane * 16, ys + k * 1024, AUX_SC1);
        SB();
        unsigned bad = 0;
#pragma unroll
        for (int k = 0; k < 8; ++k) bad |= TAGBAD(y[k]);
        if (__all(bad == 0) || spin > SPIN_CAP) break;
        asm volatile("" ::: "memory");
      }
      STAMPC(true, 12);
      float s0 = ((as_f(y[0][0]) + as_f(y[1][0])) + (as_f(y[2][0]) + as_f(y[3][0]))) + ((as_f(y[4][0]) + as_f(y[5][0])) + (as_f(y[6][0]) + as_f(y[7][0])));
      float s1 = ((as_f(y[0][2]) + as_f(y[1][2])) + (as_f(y[2][2]) + as_f(y[3][2]))) + ((as_f(y[4][2]) + as_f(y[5][2])) + (as_f(y[6][2]) + as_f(y[7][2])));
      s0 += dppf<ROR8>(s0); s1 += dppf<ROR8>(s1);
      s0 = xrow_sum(s0); s1 = xrow_sum(s1);
      const unsigned hp2 = pk_bf16(lo_f(xres) + rbf(s0), hi_f(xres) + rbf(s1));
      __builtin_amdgcn_raw_buffer_store_b64(v2u{hp2, tag}, rws, (lane >> 3) < HREP ? (lane >> 3) * 10240 + (lane & 7) * 8 : 0x40000000, WS_H + (rb * 320 + cb * 8) * 8, AUX_SC1);
      STAMPC(true, 4);
      v4u z[7];
      const int zs = WS_Z + (rb * 40 + cb) * (NFFN * 128);
      const bool has7 = lane < NFFN * 8 - 6 * 64;
      for (unsigned spin = 0;; ++spin) {
#pragma unroll
        for (int k = 0; k < 6; ++k) z[k] = __builtin_amdgcn_raw_buffer_load_b128(rws, lane * 16, zs + k * 1024, AUX_SC1);
        z[6] = __builtin_amdgcn_raw_buffer_load_b128(rws, has7 ? lane * 16 + 6144 : lane * 16, zs, AUX_SC1);
        SB();
        unsigned bad = 0;
#pragma unroll
        for (int k = 0; k < 7; ++k) bad |= TAGBAD(z[k]);
        if (__all(bad == 0) || spin > SPIN_CAP) break;
        asm volatile("" ::: "memory");
      }
      STAMPC(true, 14);
      s0 = ((as_f(z[0][0]) + as_f(z[1][0])) + (as_f(z[2][0]) + as_f(z[3][0]))) + ((as_f(z[4][0]) + as_f(z[5][0])) + (has7 ? as_f(z[6][0]) : 0.f));
      s1 = ((as_f(z[0][2]) + as_f(z[1][2])) + (as_f(z[2][2]) + as_f(z[3][2]))) + ((as_f(z[4][2]) + as_f(z[5][2])) + (has7 ? as_f(z[6][2]) : 0.f));
      s0 += dppf<ROR8>(s0); s1 += dppf<ROR8>(s1);
      s0 = xrow_sum(s0); s1 = xrow_sum(s1);
      const rsrc_t rout = mk_rsrc(out, NB * DM * 2);
      __builtin_amdgcn_raw_buffer_store_b32(pk_bf16(lo_f(hp2) + rbf(s0), hi_f(hp2) + rbf(s1)), rout, lane < 8 ? lane * 4 : 0x40000000, ri * 32, AUX_OUT);
      if (bid == 0) __builtin_amdgcn_raw_buffer_store_b32(tag, rws, lane == 0 ? 0 : 0x40000000, WS_EPOCH, AUX_SC1);
      STAMPC(true, 7);
    } else if (is_ffn) {
      const int lane = fresh_lane();
      const rsrc_t rd = mk_rsrc(wd, DM * FF * 2);
      v4u D[20];
#pragma unroll
      for (int h2 = 0; h2 < 2; ++h2) {
#pragma unroll
        for (int n = 0; n < 20; ++n) D[n] = __builtin_amdgcn_raw_buffer_load_b128(rd, lane * 16, fi * 32 * (DM * 2) + (h2 * 20 + n) * 1024, AUX_W);
        SB();
        if (h2 == 0) LWAIT(2, NSW, 1);
#pragma unroll
        for (int n = 0; n < 20; ++n) *(v4u*)(lds + L_F4 + (h2 * 20 + n) * 1024 + lane * 16) = D[n];
        SB();
      }
    }
  }

  if (is_ffn) {
    FRESH_IDS();
    if (GULATE) { STAGE_GU(16 / PG - GULATE, 16 / PG, ) LADD(9); }
    v4u BG[20], BD[10];
    const f32x4 zero = {0.f, 0.f, 0.f, 0.f};
    unsigned char* gpw = lds + L_GP + (w * 4 + r) * GP_S + (c & 7) * 32 + (c >> 3) * 8;
    if (w < 4) {
      LWAIT(9, NHW, 0);
#pragma unroll
      for (int s = 0; s < 5; ++s) {
        v2u t[8];
#pragma unroll
        for (int e = 0; e < 8; ++e) t[e] = *(const v2u*)(lds + L_BG + (160 * w + (s < 4 ? 32 * r + 4 * s + (e & 3) + 16 * (e >> 2) : 128 + 8 * r + e)) * 128 + c * 8);
#pragma unroll
        for (int d = 0; d < 4; ++d) {
          BG[4 * s + 0][d] = perm_lo(t[2 * d][0], t[2 * d + 1][0]); BG[4 * s + 1][d] = perm_hi(t[2 * d][0], t[2 * d + 1][0]);
          BG[4 * s + 2][d] = perm_lo(t[2 * d][1], t[2 * d + 1][1]); BG[4 * s + 3][d] = perm_hi(t[2 * d][1], t[2 * d + 1][1]);
        }
      }
      const v4u ones = {0x3f803f80u, 0x3f803f80u, 0x3f803f80u, 0x3f803f80u};
      f32x4 cs[4] = {zero, zero, zero, zero};
#pragma unroll
      for (int s = 0; s < 5; ++s)
#pragma unroll
        for (int f = 0; f < 4; ++f) cs[f] = mfma(ones, BG[4 * s + f], cs[f]);
      *(f32x2*)gpw = f32x2{cs[0][0], cs[1][0]};
      *(f32x2*)(gpw + 16) = f32x2{cs[2][0], cs[3][0]};
    }
    BAR();
    if (w == 4) { BAR();
      return;
    } else {
    const unsigned char* gpr = lds + L_GP + (c >> 2) * GP_S + (4 * r + (c & 3)) * 16;
    f32x4 CS = (*(const f32x4*)gpr + *(const f32x4*)(gpr + 4 * GP_S)) + (*(const f32x4*)(gpr + 8 * GP_S) + *(const f32x4*)(gpr + 12 * GP_S));
#pragma unroll
    for (int s = 0; s < 5; ++s) {
      unsigned t[8];
#pragma unroll
      for (int e = 0; e < 8; ++e) t[e] = *(const unsigned*)(lds + L_F4 + (8 * r + e) * (DM * 2) + w * 320 + s * 64 + c * 4);
#pragma unroll
      for (int d = 0; d < 4; ++d) { BD[2 * s][d] = perm_lo(t[2 * d], t[2 * d + 1]); BD[2 * s + 1][d] = perm_hi(t[2 * d], t[2 * d + 1]); }
    }
#pragma unroll
    for (int n = 0; n < 20; ++n) asm volatile("" : "+v"(BG[n]));
#pragma unroll
    for (int n = 0; n < 10; ++n) asm volatile("" : "+v"(BD[n]));
    asm volatile("" : "+v"(CS));
    BAR();
    STAMPC(w == 0, 15);
    v4u g0, g1, g2;
    {
      const int o0 = (c >> 2) * 2560 + r * 128 + (c & 3) * 16, o1 = (c >> 2) * 2560 + 512 + r * 32 + (c & 1) * 16, hs = WS_H + (xs % HREP) * 10240 + w * 640;
      for (unsigned spin = 0;; ++spin) {
        g0 = __builtin_amdgcn_raw_buffer_load_b128(rws, o0, hs, AUX_SC1);
        g1 = __builtin_amdgcn_raw_buffer_load_b128(rws, o0 + 64, hs, AUX_SC1);
        g2 = __builtin_amdgcn_raw_buffer_load_b128(rws, o1, hs, AUX_SC1);
        SB();
        const unsigned bad = TAGBAD(g0) | TAGBAD(g1) | TAGBAD(g2);
        if (__all(bad == 0) || spin > SPIN_CAP) break;
        asm volatile("" ::: "memory");
      }
    }
    STAMPC(true, 28 + w);
    const v4u a0 = {g0[0], g0[2], g1[0], g1[2]}, a1 = {dppu<0x00>(g2[0]), dppu<0x00>(g2[2]), dppu<0x55>(g2[0]), dppu<0x55>(g2[2])};
    f32x4 dd[4][4];
#pragma unroll
    for (int s = 0; s < 4; ++s)
#pragma unroll
      for (int f = 0; f < 4; ++f) dd[s][f] = mfma(a0, BG[4 * s + f], zero);
#pragma unroll
    for (int f = 0; f < 4; ++f) dd[0][f] = mfma(a1, BG[16 + f], dd[0][f]);
    {
      const unsigned ax = (c & 2) ? g2[2] : g2[0];
      float s1 = dot2(ax, 0x3f803f80u, 0.f), s2 = dot2(ax, ax, 0.f);
#pragma unroll
      for (int d = 0; d < 4; ++d) { s1 = dot2(a0[d], 0x3f803f80u, s1); s2 = dot2(a0[d], a0[d], s2); }
      float ss = rs32(s1, s2);
      ss = rs16(ss, ss);
      ss += dppf<XOR1>(ss); ss += dppf<XOR2>(ss);
      *(float*)(lds + L_ST + (c >> 2) * 32 + (lane >> 5) * 16 + w * 4) = ss;
    }
    float pv[4];
#pragma unroll
    for (int f = 0; f < 4; ++f) pv[f] = (dd[0][f][0] + dd[1][f][1]) + (dd[2][f][2] + dd[3][f][3]);
    *(f32x2*)gpw = f32x2{pv[0], pv[1]};
    *(f32x2*)(gpw + 16) = f32x2{pv[2], pv[3]};
    BAR();
    STAMPC(w == 0, 5);
    const f32x4 t1 = *(const f32x4*)(lds + L_ST + (c >> 2) * 32), t2 = *(const f32x4*)(lds + L_ST + (c >> 2) * 32 + 16);
    f32x4 gu[4];
#pragma unroll
    for (int ww = 0; ww < 4; ++ww) gu[ww] = *(const f32x4*)(gpr + ww * 4 * GP_S);
    SB();
    const float e1 = ((t1[0] + t1[1]) + (t1[2] + t1[3])) * (1.0f / DM), e2 = ((t2[0] + t2[1]) + (t2[2] + t2[3])) * (1.0f / DM);
    const float var = fmaxf(fmaf(-e1, e1, e2), 0.f);
    const float mb = rbf(e1), rden = __builtin_amdgcn_rcpf(rbf(__builtin_amdgcn_sqrtf(rbf(rbf(var) + 1e-5f))));
    const f32x4 GU = (((gu[0] + gu[1]) + (gu[2] + gu[3])) - mb * CS) * rden;
    float av[2];
#pragma unroll
    for (int j = 0; j < 2; ++j) {
      const float g = rbf(GU[j]), u = rbf(GU[2 + j]);
      const float u2 = 2.0f * 0.7978845608028654f * LOG2E * (g + 0.044715f * g * g * g);
      av[j] = rbf(g * __builtin_amdgcn_rcpf(1.0f + __builtin_amdgcn_exp2f(-u2))) * u;
    }
    const unsigned ap = pk_bf16(av[0], av[1]);
    const v4u af = {dppu<0x00>(ap), dppu<0x55>(ap), dppu<0xAA>(ap), dppu<0xFF>(ap)};
    f32x4 z[10];
#pragma unroll
    for (int f = 0; f < 10; ++f) z[f] = mfma(af, BD[f], zero);
#pragma unroll
    for (int s = 0; s < 5; ++s)
      st128_sc1(v4u{as_u(z[2 * s][0]), tag, as_u(z[2 * s + 1][0]), tag}, rws, (r * 40 + (c >> 3)) * (NFFN * 128) + (c & 7) * 16, WS_Z + ((10 * w + 2 * s) * NFFN + fi) * 128);
    }
  }
  STAMPC(w == 0, 6);

  HOLD_UNTIL(1, 100);
  return;
  }
  {
    const int w = wv - NHW;
    const int lane = threadIdx.x & 63, r = lane >> 4, c = lane & 15;
    const rsrc_t rk = mk_rsrc(kc, NB * NH * SK * DH * 2), rv = mk_rsrc(vc, NB * NH * SK * DH * 2);
    const int voff = lane * 16;
    if (is_q) __builtin_amdgcn_s_barrier();
    v4u B[NPRE][8];
    int so = kv0;
    PREFETCH(NPRE, if (!is_q) __builtin_amdgcn_s_barrier();)
    *(f32x2*)(lds + L_SC + w * SC_S + 14 * 128 + lane * 8) = f32x2{-INFINITY, -INFINITY};
    LFLAG(10 + w) = 1u;
    STAMPC(w == 0, 11);
    LWAIT(1, 1, 1);
    const unsigned tag = __builtin_amdgcn_readfirstlane(LFLAG(5));
    const v4u q = *(const v4u*)(lds + L_Q + c * 16);
    STAMPC(w == 0, 1);

    int thr = __builtin_amdgcn_readfirstlane((half && pair_ffn) ? LAGF : is_q ? LAGQ : ns > 16 ? LAGP : LAGN);
    unsigned char* sc = lds + L_SC + w * SC_S + r * 32 + (c >> 1) * 4;
    for (int t = 0; t < naux; ++t) so = NEXT(so);
    KSTEP(0, 1, rk, AUX_K, so)
    KSTEP(1, 0, rk, AUX_K, 0)
    KSTEP(2, 0, rk, AUX_K, 0)
    KSTEP(3, 0, rk, AUX_K, 0)
    sc += naux * 128;
    for (int it = NPRE + naux; it < ns - 1; ++it) { so = NEXT(so); KSTEP(0, 2, rk, AUX_K, so) STAMPC(w == 0 && it == 7, 20); }
    so = kv0;
    KSTEP(0, 2, rv, AUX_V, so)
    STAMPC(w == 0, 21);
    float mw;
    {
      if (NAUX && naux) LWAIT(10 + w, 2, 1);
      asm volatile("" ::: "memory");
      const f32x4 s0 = *(const f32x4*)(lds + L_SC + w * SC_S + lane * 32), s1 = *(const f32x4*)(lds + L_SC + w * SC_S + lane * 32 + 16);
      const f32x4 s2 = *(const f32x4*)(lds + L_SC + w * SC_S + 2048 + (lane & 7) * 32), s3 = *(const f32x4*)(lds + L_SC + w * SC_S + 2048 + (lane & 7) * 32 + 16);
      float mx = -INFINITY;
#pragma unroll
      for (int j = 0; j < 4; ++j) mx = fmaxf(fmaxf(mx, fmaxf(s0[j], s1[j])), fmaxf(s2[j], s3[j]));
      mx = fmaxf(mx, dppf<ROR8>(mx)); mx = fmaxf(mx, dppf<ROR4>(mx)); mx = fmaxf(mx, dppf<ROR2>(mx)); mx = fmaxf(mx, dppf<ROR1>(mx));
      mw = xrow_max(mx) * LOG2E;
      float p[8], e[8];
#pragma unroll
      for (int j = 0; j < 4; ++j) { p[j] = __builtin_amdgcn_exp2f(fmaf(s0[j], LOG2E, -mw)); p[4 + j] = __builtin_amdgcn_exp2f(fmaf(s1[j], LOG2E, -mw));
                                    e[j] = __builtin_amdgcn_exp2f(fmaf(s2[j], LOG2E, -mw)); e[4 + j] = __builtin_amdgcn_exp2f(fmaf(s3[j], LOG2E, -mw)); }
      asm volatile("" ::: "memory");
      *(v4u*)(lds + L_SC + w * SC_S + lane * 16) = v4u{pk_bf16(p[0], p[1]), pk_bf16(p[2], p[3]), pk_bf16(p[4], p[5]), pk_bf16(p[6], p[7])};
      *(v4u*)(lds + L_SC + w * SC_S + 1024 + (lane & 7) * 16) = v4u{pk_bf16(e[0], e[1]), pk_bf16(e[2], e[3]), pk_bf16(e[4], e[5]), pk_bf16(e[6], e[7])};
      const float le = ((e[0] + e[1]) + (e[2] + e[3])) + ((e[4] + e[5]) + (e[6] + e[7]));
      float l = ((p[0] + p[1]) + (p[2] + p[3])) + ((p[4] + p[5]) + (p[6] + p[7])) + (lane < 8 ? le : 0.f);
      l += dppf<ROR8>(l); l += dppf<ROR4>(l); l += dppf<ROR2>(l); l += dppf<ROR1>(l);
      l = xrow_sum(l);
      if (lane == 0) *(f32x2*)(lds + L_ML + w * 8) = f32x2{mw, l};
      LADD(6);
      SB();
    }
    float acc[8];
#pragma unroll
    for (int e = 0; e < 8; ++e) acc[e] = 0.f;
    const unsigned char* pp = lds + L_SC + w * SC_S + r * 16;
#define VSTEP(MODE, NXT) { \
    const v4u pw = *(const v4u*)pp; pp += 64; \
    _Pragma("unroll") for (int j = 0; j < 8; ++j) { \
      const unsigned u = pw[j >> 1], pl = (j & 1) ? u >> 16 : u & 0xffffu, ph = (j & 1) ? u & 0xffff0000u : u << 16; \
      _Pragma("unroll") for (int d = 0; d < 4; ++d) { acc[2 * d] = dot2(B[0][j][d], pl, acc[2 * d]); acc[2 * d + 1] = dot2(B[0][j][d], ph, acc[2 * d + 1]); } \
      asm volatile("" : "+v"(acc[0]), "+v"(acc[1]), "+v"(acc[2]), "+v"(acc[3]), "+v"(acc[4]), "+v"(acc[5]), "+v"(acc[6]), "+v"(acc[7])); SB(); \
      REFILL(j, MODE, rv, AUX_V, NXT) } }
    for (int it = 0; it < ns - 1; ++it) { so = NEXT(so); thr = it + BOOST >= ns ? 0 : thr; VSTEP(2, so) STAMPC(w == 0 && it == 7, 22); }
    if (w == 0) LFLAG(4) = 1u;
    LWAIT(3, 1, 1);
    LWAIT(7, 1, 1);
#pragma unroll
    for (int n = 0; n < 8; ++n) B[1][n] = *(const v4u*)(lds + L_F4 + (w * 8 + n) * 1024 + lane * 16);
    const f32x2 gl = *(const f32x2*)(lds + L_ML + 32);
    float f = __builtin_amdgcn_exp2f(mw - gl.x) * gl.y;
    asm volatile("" : "+v"(f));
    SB();
    VSTEP(0, 0)
    STAMPC(true, 16 + w);
    {
      float t[4];
#pragma unroll
      for (int j = 0; j < 4; ++j) t[j] = rs32(acc[j], acc[j + 4]);
      *(unsigned*)(lds + L_OP + w * OP_S + (4 * c + r) * 4) = pk_bf16(rs16(t[0], t[2]) * f, rs16(t[1], t[3]) * f);
    }
    LADD(2);
    LWAIT(2, NSW, 0);
    {
      const rsrc_t rws = mk_rsrc(ws, WS_END);
      v4u a[4];
#pragma unroll
      for (int s = 0; s < 4; ++s) a[s] = *(const v4u*)(lds + L_OP + (c >> 2) * OP_S + s * 64 + r * 16);
      f32x4 d0 = {0.f, 0.f, 0.f, 0.f}, d1 = {0.f, 0.f, 0.f, 0.f};
#pragma unroll
      for (int s = 0; s < 4; ++s) { d0 = mfma(a[s], B[1][2 * s], d0); d1 = mfma(a[s], B[1][2 * s + 1], d1); }
      const float y0 = xrow_sum(d0[0]), y1 = xrow_sum(d1[0]);
      st128_sc1(v4u{as_u(y0), tag, as_u(y1), tag}, rws, lane < 16 ? (c >> 3) * 8192 + (c & 7) * 16 : 0x40000000, WS_Y + ((b * 40 + 32 + 2 * w) * 64 + head * 2 + half) * 128);
    }
  }
}

extern "C" void kernel_launch(void* const* d_in, const int* in_sizes, int n_in, void* d_out, int out_size, void* d_ws, size_t ws_size, hipStream_t stream) {
  fused_block<<<NWG, NT, 0, stream>>>((const u16*)d_in[1], (const u16*)d_in[2], d_ws, (const u16*)d_in[0], (const u16*)d_in[3], (const u16*)d_in[4],
                                      (const u16*)d_in[6], (const u16*)d_in[7], (const u16*)d_in[8], (u16*)d_out);
}
```

```cpp
#define XLO 3950
#define XLO2 3850
#include <hip/hip_runtime.h>
#include <hip/hip_bf16.h>
#include <stdint.h>

typedef unsigned short u16;
typedef unsigned v4u __attribute__((ext_vector_type(4)));
typedef unsigned v2u __attribute__((ext_vector_type(2)));
typedef float f32x2 __attribute__((ext_vector_type(2)));
typedef float f32x4 __attribute__((ext_vector_type(4)));
typedef __bf16 bf16x2 __attribute__((ext_vector_type(2)));
typedef __bf16 bf16x8 __attribute__((ext_vector_type(8)));
typedef __amdgpu_buffer_rsrc_t rsrc_t;
#define GAS __attribute__((address_space(1)))
#define LAS __attribute__((address_space(3)))

#define LAGN 2
#define LAGF 0
#define SKEW 0
#define PW 4
#define PG 2
#define GU_FIRST 0
#define AUX_X 0
#define DPRE 8
#define QDELAY 0
#define AUX_QP 16
#define AUX_EP 16
#define ABL 0
#define PRE0 4
#define AUX_K 2
#define AUX_V 2
#define AUX_W 0
#define AUX_OUT 17
#define PWN 2
#define AUX_WQ 2
#define SHF 4
#define SHQ 4
#define LAGQ 1
#define LAGP 2
#define WAITQ 0
#define NAUX 4
#define LAGPQ 2
#define BOOST 4
#define HREP 8
#define ROTA 4
#define ROTB 8
#define MVF 1
#define MVQ 1
#define GULATE 0
#define SWZ 0
#define TAU 113u
#define PT1 20
#define XHI 100000
constexpr int NPRE = 4;

constexpr int NB = 4, DM = 640, NH = 32, SK = 4096, DH = 128, FF = 1728;
constexpr int NSW = 4, NHW = 5, NT = 64 * (NSW + NHW), NWG = 256;
constexpr int NRED = 160, NFFN = 54;
constexpr int WS_EPOCH = 0;
constexpr int WS_ML = 32768;
constexpr int WS_H_OLD = 8192;
constexpr int WS_Q = 65536;
constexpr int WS_Y = WS_Q + NB * NH * 512;
constexpr int WS_Z = WS_Y + NB * 40 * 64 * 128;
constexpr int WS_H = WS_Z + NB * 40 * NFFN * 128;
constexpr int WS_END = WS_H + 8 * 10240;
constexpr int WS_STAMP = WS_END;
constexpr int AUX_NT = 2, AUX_SC1 = 16;
constexpr unsigned SPIN_CAP = 1u << 18;
constexpr float LOG2E = 1.4426950408889634f;

#define STAMPC(cond, i) do { } while (0)

__device__ __forceinline__ rsrc_t mk_rsrc(const void* p, int bytes) { return __builtin_amdgcn_make_buffer_rsrc((void*)p, 0, bytes, 0x00020000); }
__device__ __forceinline__ float as_f(unsigned u) { return __builtin_bit_cast(float, u); }
__device__ __forceinline__ unsigned as_u(float f) { return __builtin_bit_cast(unsigned, f); }
__device__ __forceinline__ unsigned pk_bf16(float lo, float hi) { f32x2 v = {lo, hi}; return __builtin_bit_cast(unsigned, __builtin_convertvector(v, bf16x2)); }
__device__ __forceinline__ float rbf(float x) { return as_f(pk_bf16(0.f, x)); }
__device__ __forceinline__ float lo_f(unsigned u) { return as_f(u << 16); }
__device__ __forceinline__ float hi_f(unsigned u) { return as_f(u & 0xffff0000u); }
__device__ __forceinline__ float dot2(unsigned a, unsigned b, float c) { return __builtin_amdgcn_fdot2_f32_bf16(__builtin_bit_cast(bf16x2, a), __builtin_bit_cast(bf16x2, b), c, false); }
__device__ __forceinline__ f32x4 mfma(v4u a, v4u b, f32x4 c) { return __builtin_amdgcn_mfma_f32_16x16x32_bf16(__builtin_bit_cast(bf16x8, a), __builtin_bit_cast(bf16x8, b), c, 0, 0, 0); }
template <int CTRL> __device__ __forceinline__ unsigned dppu(unsigned x) { return __builtin_amdgcn_mov_dpp((int)x, CTRL, 0xf, 0xf, true); }
template <int CTRL> __device__ __forceinline__ float dppf(float x) { return as_f(dppu<CTRL>(as_u(x))); }
constexpr int ROR1 = 0x121, ROR2 = 0x122, ROR4 = 0x124, ROR8 = 0x128, XOR1 = 0xB1, XOR2 = 0x4E, XOR7 = 0x141, BCAST = 0x150;
__device__ __forceinline__ unsigned perm_lo(unsigned a, unsigned b) { return __builtin_amdgcn_perm(b, a, 0x05040100u); }
__device__ __forceinline__ unsigned perm_hi(unsigned a, unsigned b) { return __builtin_amdgcn_perm(b, a, 0x07060302u); }
__device__ __forceinline__ float rs16(float a, float b) { auto s = __builtin_amdgcn_permlane16_swap(as_u(a), as_u(b), false, false); return as_f(s[0]) + as_f(s[1]); }
__device__ __forceinline__ float rs32(float a, float b) { auto s = __builtin_amdgcn_permlane32_swap(as_u(a), as_u(b), false, false); return as_f(s[0]) + as_f(s[1]); }
__device__ __forceinline__ float xrow_sum(float x) { x = rs16(x, x); return rs32(x, x); }
__device__ __forceinline__ float xrow_max(float x) {
  auto s = __builtin_amdgcn_permlane16_swap(as_u(x), as_u(x), false, false); x = fmaxf(as_f(s[0]), as_f(s[1]));
  auto t = __builtin_amdgcn_permlane32_swap(as_u(x), as_u(x), false, false); return fmaxf(as_f(t[0]), as_f(t[1]));
}
__device__ __forceinline__ void st128_sc1(v4u v, rsrc_t rs, int voff, int soff) {
  __builtin_amdgcn_raw_buffer_store_b128(v, rs, voff, soff, AUX_SC1);
  asm volatile("s_nop 1" :: "v"(v) : "memory");
}
__device__ __forceinline__ int fresh_lane() { int l; asm volatile("v_mbcnt_lo_u32_b32 %0, -1, 0\n\tv_mbcnt_hi_u32_b32 %0, -1, %0" : "=v"(l)); return l; }
__device__ __forceinline__ unsigned ln_pair(unsigned v, float mb, float rden) { return pk_bf16(rbf(lo_f(v) - mb) * rden, rbf(hi_f(v) - mb) * rden); }
__device__ __forceinline__ f32x2 ln_stats(const unsigned (&hv)[5]) {
  const float piv = as_f(__builtin_amdgcn_readfirstlane(hv[0] << 16));
  float s1 = 0.f, s2 = 0.f;
#pragma unroll
  for (int j = 0; j < 5; ++j) { const float d0 = lo_f(hv[j]) - piv, d1 = hi_f(hv[j]) - piv; s1 += d0 + d1; s2 = fmaf(d0, d0, s2); s2 = fmaf(d1, d1, s2); }
  float ss = rs32(s1, s2);
  ss = rs16(ss, ss);
  ss += dppf<ROR8>(ss); ss += dppf<ROR4>(ss); ss += dppf<ROR2>(ss); ss += dppf<ROR1>(ss);
  const float e1 = as_f(__builtin_amdgcn_readlane(as_u(ss), 0)) * (1.0f / DM), e2 = as_f(__builtin_amdgcn_readlane(as_u(ss), 32)) * (1.0f / DM);
  const float var = fmaxf(fmaf(-e1, e1, e2), 0.f);
  return f32x2{rbf(piv + e1), __builtin_amdgcn_rcpf(rbf(__builtin_amdgcn_sqrtf(rbf(rbf(var) + 1e-5f))))};
}
#define SB() __builtin_amdgcn_sched_barrier(0)
#define VM0() do { SB(); asm volatile("s_waitcnt vmcnt(0)" ::: "memory"); SB(); } while (0)
#define BAR() do { asm volatile("s_waitcnt lgkmcnt(0)" ::: "memory"); __builtin_amdgcn_s_barrier(); asm volatile("" ::: "memory"); } while (0)
#define TAGBAD(g) (((g)[1] ^ tag) | ((g)[3] ^ tag))
#define FRESH_IDS() const int lane = fresh_lane(); const int r = lane >> 4, c = lane & 15, htid = w * 64 + lane; (void)r; (void)c; (void)htid

constexpr int L_FLAG = 0;
constexpr int L_ML = 64;
constexpr int L_Q = 128;
constexpr int OP_S = 320;
constexpr int L_ST = 384;
constexpr int L_PACE = 512;
constexpr int L_OP = 576;
constexpr int GP_S = 384;
constexpr int L_GP = L_OP + 4 * OP_S;
constexpr int L_BG = L_GP + 5 * 4 * GP_S;
constexpr int L_F4 = L_BG + 640 * 128;
constexpr int L_SC = L_F4 + 32 * 1024;
constexpr int SC_S = 2304;
constexpr int L_END = L_SC + 4 * SC_S;
static_assert(L_BG % 16 == 0 && L_OP % 16 == 0 && L_GP % 16 == 0, "align");
#define LFLAG(i) (*(volatile LAS unsigned*)(LAS unsigned char*)(lds + L_FLAG + 4 * (i)))
#define LWAIT(i, n, SLP) do { unsigned spin_ = 0; _Pragma("nounroll") while (LFLAG(i) < (unsigned)(n) && ++spin_ < (SPIN_CAP << 4)) { if (SLP) __builtin_amdgcn_s_sleep(SLP); } asm volatile("" ::: "memory"); } while (0)
#define LADD(i) do { asm volatile("s_waitcnt lgkmcnt(0)" ::: "memory"); if (fresh_lane() == 0) __hip_atomic_fetch_add((LAS unsigned*)(LAS unsigned char*)(lds + L_FLAG + 4 * (i)), 1u, __ATOMIC_RELAXED, __HIP_MEMORY_SCOPE_WORKGROUP); asm volatile("" ::: "memory"); } while (0)

__device__ __forceinline__ int rank_of(int bid) { return (bid & 7) + 8 * (bid >> 4); }
__device__ __forceinline__ int ffn_of_rank(int j) { const int s = j - 64; if (s < 0) return -1; const int k = (s & 7) + 8 * (s >> 4); return k < 27 ? 2 * k + ((s >> 3) & 1) : -1; }

__global__ void __launch_bounds__(NT, 3) fused_block(const u16* __restrict__ kc, const u16* __restrict__ vc, void* ws, const u16* __restrict__ x,
                                                     const u16* __restrict__ wq, const u16* __restrict__ wo, const u16* __restrict__ wg,
                                                     const u16* __restrict__ wu, const u16* __restrict__ wd, u16* __restrict__ out) {
  __shared__ __attribute__((aligned(16))) unsigned char lds[L_END + 256];
  const int wv = __builtin_amdgcn_readfirstlane(threadIdx.x >> 6);
  const int bid = blockIdx.x, xs = bid & 7, rr = bid >> 3;
  const int head = xs + 8 * (rr >> 3), b = (rr >> 1) & 3, half = rr & 1;
  STAMPC(wv == 0, 0);
#define HOLD_UNTIL(k, us) do { } while (0)

  constexpr int STEP = 2 * NSW * 4096;
  const int rank = rank_of(bid);
  const bool pair_ffn = ffn_of_rank(rank) >= 0, is_q = !half && rank < 64;
  const int sw = wv >= NHW ? wv - NHW : wv;
  const int mv = pair_ffn ? MVF : rank < 64 ? -MVQ : 0;
  const int ns = half ? 16 - mv : 16 + mv;
  const int kvbase = (b * NH + head) * (SK * DH * 2) + (half ? (16 + mv) * STEP : 0) + sw * 4096;
  const int naux = (is_q || (half && pair_ffn)) ? 0 : NAUX;
  const int kvend = kvbase + ns * STEP, kv0 = kvbase + (ROTA * (head & 1) + ROTB * ((head >> 2) & 1)) * STEP;
#define NEXT(so) ((so) + STEP == kvend ? kvbase : (so) + STEP)
  const int t2off = NSW * 4096 + (SWZ ? ((sw ^ SWZ) - sw) * 4096 : 0);
#define LD(RS, AUX, i, so) __builtin_amdgcn_raw_buffer_load_b128(RS, voff, (so) + ((i) >> 2) * t2off + ((i) & 3) * 1024, AUX)
#define PREFETCH(N, AT8) \
    _Pragma("unroll") for (int t = 0; t < N; ++t, so = NEXT(so)) \
      _Pragma("unroll") for (int i = 0; i < 8; ++i) { \
        B[t][i] = LD(rk, AUX_K, i, so); \
        if (t == 0 && i == 7) { SB(); AT8 SB(); } \
        if (8 * t + i + 1 >= DPRE && 8 * t + i + 1 < 8 * N) { SB(); asm volatile("s_waitcnt vmcnt(%0)" :: "i"(DPRE - 1) : "memory"); SB(); } } \
    SB();
#define REFILL(i, MODE, RS, AUX, NXT) if (MODE) { if (MODE == 2) THROTTLE(); B[0][i] = LD(RS, AUX, i, NXT); SB(); }
#define KSTEP(T, MODE, RS, AUX, NXT) { \
    float sx[8]; \
    _Pragma("unroll") for (int j = 0; j < 8; ++j) { \
      float d = dot2(B[T][j][0], q[0], 0.f); d = dot2(B[T][j][1], q[1], d); d = dot2(B[T][j][2], q[2], d); d = dot2(B[T][j][3], q[3], d); \
      asm volatile("" : "+v"(d)); sx[j] = d; SB(); REFILL(j, MODE, RS, AUX, NXT) } \
    float t4[4], t2[2]; \
    _Pragma("unroll") for (int j = 0; j < 4; ++j) { const float lo = sx[j] + dppf<ROR8>(sx[j]), hi = sx[j + 4] + dppf<ROR8>(sx[j + 4]); t4[j] = (c & 8) ? hi : lo; } \
    _Pragma("unroll") for (int j = 0; j < 2; ++j) { const float lo = t4[j] + dppf<XOR7>(t4[j]), hi = t4[j + 2] + dppf<XOR7>(t4[j + 2]); t2[j] = (c & 4) ? hi : lo; } \
    const float lo = t2[0] + dppf<XOR2>(t2[0]), hi = t2[1] + dppf<XOR2>(t2[1]); \
    float s = (c & 2) ? hi : lo; \
    s += dppf<XOR1>(s); \
    *(float*)sc = rbf(rbf(s) * 0.08838834764831845f); sc += 128; SB(); }

#define THROTTLE() asm volatile("s_cmp_lt_u32 %0, 1\n\ts_cbranch_scc1 1f\n\ts_waitcnt vmcnt(6)\n\ts_cmp_lt_u32 %0, 2\n\ts_cbranch_scc1 1f\n\ts_waitcnt vmcnt(5)\n\ts_cmp_lt_u32 %0, 3\n\ts_cbranch_scc1 1f\n\ts_waitcnt vmcnt(4)\n\ts_cmp_lt_u32 %0, 4\n\ts_cbranch_scc1 1f\n\ts_waitcnt vmcnt(3)\n1:" :: "s"(thr) : "scc", "memory")
  if (wv < NHW) {
  const int w = wv;
  const int ri = half * 128 + rank_of(bid);
  __builtin_amdgcn_s_setprio(2);
  if (threadIdx.x < 16) { *(unsigned*)(lds + L_FLAG + threadIdx.x * 4) = 0u; *(unsigned*)(lds + L_PACE + threadIdx.x * 4) = 0u; }
  const rsrc_t rws = mk_rsrc(ws, WS_END);
  unsigned tag, xres;
  {
    FRESH_IDS();
    const rsrc_t rx = mk_rsrc(x, NB * DM * 2);
    if (ri < 64) {
      const rsrc_t rwq = mk_rsrc(wq, ABL & 4 ? 0 : DM * NH * DH * 2);
      const v4u x8 = __builtin_amdgcn_raw_buffer_load_b128(rx, lane * 16, w * (DM * 2), AUX_X);
      const unsigned x2 = __builtin_amdgcn_raw_buffer_load_b32(rx, lane * 4 + 1024, w * (DM * 2), AUX_X);
      v4u xa[4];
#pragma unroll
      for (int s = 0; s < 4; ++s) xa[s] = __builtin_amdgcn_raw_buffer_load_b128(rx, (c >> 2) * (DM * 2) + r * 16, (128 * w + 32 * s) * 2, AUX_X);
      const unsigned epoch_v = __builtin_amdgcn_raw_buffer_load_b32(rws, 0, WS_EPOCH, AUX_SC1);
      xres = __builtin_amdgcn_raw_buffer_load_b32(rx, (lane & 7) * 4, ri * 32, AUX_X);
      SB();
      v2u Wq[32];
#define WQLD(n) Wq[n] = __builtin_amdgcn_raw_buffer_load_b64(rwq, (8 * r) * 8192 + c * 8, (128 * w + 32 * ((n) >> 3) + ((n) & 7)) * 8192 + ri * 128, AUX_WQ)
#pragma unroll
      for (int n = 0; n < 12; ++n) WQLD(n);
      SB();
      BAR();
      if (w < 4) {
        const unsigned hv[5] = {x8[0], x8[1], x8[2], x8[3], x2};
        *(f32x2*)(lds + L_ST + w * 8) = ln_stats(hv);
        LADD(8);
      }
      SB();
#pragma unroll
      for (int n = 12; n < 20; ++n) WQLD(n);
      SB();
      STAMPC(w == 0, 8);
      LWAIT(8, 4, 0);
      const f32x2 st = *(const f32x2*)(lds + L_ST + (c >> 2) * 8);
#pragma unroll
      for (int s = 0; s < 4; ++s) {
        xa[s] = v4u{ln_pair(xa[s][0], st.x, st.y), ln_pair(xa[s][1], st.x, st.y), ln_pair(xa[s][2], st.x, st.y), ln_pair(xa[s][3], st.x, st.y)}; asm volatile("" : "+v"(xa[s]));
        SB();
#pragma unroll
        for (int n = 20 + 3 * s; n < 23 + 3 * s; ++n) WQLD(n);
        SB();
      }
      LADD(9);
      STAMPC(w == 0, 27);
      const f32x4 zero = {0.f, 0.f, 0.f, 0.f};
      f32x4 dq[4] = {zero, zero, zero, zero};
#pragma unroll
      for (int s = 0; s < 4; ++s) {
#pragma unroll
        for (int f = 0; f < 4; ++f) {
          v4u bf;
#pragma unroll
          for (int d = 0; d < 4; ++d) bf[d] = (f & 1) ? perm_hi(Wq[8 * s + 2 * d][f >> 1], Wq[8 * s + 2 * d + 1][f >> 1]) : perm_lo(Wq[8 * s + 2 * d][f >> 1], Wq[8 * s + 2 * d + 1][f >> 1]);
          dq[f] = mfma(xa[s], bf, dq[f]);
        }
        SB();
      }
      *(f32x4*)(lds + L_GP + w * 1024 + lane * 16) = f32x4{dq[0][0], dq[1][0], dq[2][0], dq[3][0]};
      tag = __builtin_amdgcn_readfirstlane(epoch_v) + 1u;
      LADD(0);
      STAMPC(w == 0, 24);
      if (w == 1) {
        LWAIT(0, NHW, 0);
        f32x4 pq[NHW];
#pragma unroll
        for (int ww = 0; ww < NHW; ++ww) pq[ww] = *(const f32x4*)(lds + L_GP + ww * 1024 + lane * 16);
        const f32x4 sq = ((pq[0] + pq[1]) + (pq[2] + pq[3])) + pq[4];
        st128_sc1(v4u{pk_bf16(sq[0], sq[1]), tag, pk_bf16(sq[2], sq[3]), tag}, rws, r * (NH * 512) + c * 16, WS_Q + ri * 256);
        STAMPC(true, 25);
      }
    } else {
      const unsigned epoch_v = __builtin_amdgcn_raw_buffer_load_b32(rws, 0, WS_EPOCH, AUX_SC1);
      xres = __builtin_amdgcn_raw_buffer_load_b32(rx, (lane & 7) * 4, ri * 32, AUX_X);
      SB();
      BAR();
      tag = __builtin_amdgcn_readfirstlane(epoch_v) + 1u;
    }
    if (w == 4) {
      v4u g;
      for (unsigned spin = 0;; ++spin) {
        g = __builtin_amdgcn_raw_buffer_load_b128(rws, (lane & 31) * 16, WS_Q + (b * NH + head) * 512, AUX_SC1);
        if (__all(TAGBAD(g) == 0) || spin > SPIN_CAP) break;
        asm volatile("" ::: "memory");
      }
      *(v2u*)(lds + L_Q + (lane & 31) * 8) = v2u{g[0], g[2]};
      LFLAG(5) = tag;
      asm volatile("s_waitcnt lgkmcnt(0)" ::: "memory");
      LFLAG(1) = 1u;
      STAMPC(true, 26);
    } else if (NAUX && naux) {
      const rsrc_t rk = mk_rsrc(kc, NB * NH * SK * DH * 2);
      const int voff = lane * 16;
      const int thr = 0;
      v4u B[NAUX ? NAUX : 1][8];
      LWAIT(10 + w, 1, 1);
      int so = kv0;
#pragma unroll
      for (int t = 0; t < NPRE; ++t) so = NEXT(so);
      PREFETCH(NAUX, )
      LWAIT(1, 1, 1);
      const v4u q = *(const v4u*)(lds + L_Q + c * 16);
      unsigned char* sc = lds + L_SC + w * SC_S + NPRE * 128 + r * 32 + (c >> 1) * 4;
#pragma unroll
      for (int t = 0; t < NAUX; ++t) KSTEP(t, 0, rk, AUX_K, 0)
      asm volatile("s_waitcnt lgkmcnt(0)" ::: "memory");
      LFLAG(10 + w) = 2u;
    }
  }

  const bool is_red = ri < NRED;
  const int rb = ri / 40, cb = ri % 40;
  const int fi = half ? ffn_of_rank(rank_of(bid)) : -1;
  const bool is_ffn = fi >= 0;

#define STAGE_GU(S0, S1, HOOK) if (is_ffn) { \
    const int ln = fresh_lane(); \
    const GAS unsigned char* gp = (const GAS unsigned char*)((ln & 4) ? wu : wg) + (size_t)((128 * w + (ln >> 3)) * (FF * 2) + fi * 64 + (ln & 3) * 16) + (size_t)(S0) * 8 * PG * (FF * 2); \
    _Pragma("nounroll") for (int s = (S0); s < (S1); ++s) { \
      HOOK \
      v4u GU[PG]; \
      _Pragma("unroll") for (int n = 0; n < PG; ++n) GU[n] = AUX_W ? __builtin_nontemporal_load((const GAS v4u*)(gp + 8 * n * (FF * 2))) : *(const GAS v4u*)(gp + 8 * n * (FF * 2)); \
      gp += 8 * PG * (FF * 2); \
      _Pragma("unroll") for (int n = 0; n < PG; ++n) *(v4u*)(lds + L_BG + (w * 16 + s * PG + n) * 1024 + ln * 16) = GU[n]; \
    } }
#define WO_STEP(s, OUT) { \
    v4u T[8]; \
    _Pragma("unroll") for (int e = 0; e < 8; ++e) { T[e] = __builtin_amdgcn_raw_buffer_load_b128(rwo, vo, so + (32 * (s) + 4 * e) * (DM * 2), AUX_W); if (e % 4 == 3) VM0(); else if (e % PWN == PWN - 1) { SB(); asm volatile("s_cmp_lt_i32 %0, 0\n\ts_cbranch_scc0 1f\n\ts_waitcnt vmcnt(0)\n1:" :: "s"(fi) : "scc", "memory"); SB(); } } \
    _Pragma("unroll") for (int e = 0; e < 8; ++e) *(v4u*)(scr + e * 1024 + lane * 16) = T[e]; \
    asm volatile("" ::: "memory"); \
    _Pragma("unroll") for (int t = 0; t < 4; ++t) { \
      unsigned u[8]; \
      _Pragma("unroll") for (int e = 0; e < 8; ++e) u[e] = *(const unsigned*)(scr + (8 * r + e) * 256 + t * 64 + c * 4); \
      v4u f0, f1; \
      _Pragma("unroll") for (int d = 0; d < 4; ++d) { f0[d] = perm_lo(u[2 * d], u[2 * d + 1]); f1[d] = perm_hi(u[2 * d], u[2 * d + 1]); } \
      OUT(s, t, f0, f1) } \
    asm volatile("" ::: "memory"); SB(); }
  if (w < 4) {
    v4u F[32];
    {
      FRESH_IDS();
      const rsrc_t rwo = mk_rsrc(wo, ABL & 1 ? 0 : DM * NH * DH * 2);
      const int vo = r * (DM * 2) + c * 16, so = head * DH * (DM * 2) + w * 256;
      unsigned char* scr = lds + L_BG + w * 16384;
#define OUT_REG(s, t, f0, f1) F[8 * (s) + 2 * (t)] = f0; F[8 * (s) + 2 * (t) + 1] = f1; asm volatile("" : "+v"(F[8 * (s) + 2 * (t)]), "+v"(F[8 * (s) + 2 * (t) + 1]));
      WO_STEP(0, OUT_REG) WO_STEP(1, OUT_REG) WO_STEP(2, OUT_REG) WO_STEP(3, OUT_REG)
    }
    STAMPC(w == 0, 9);
    STAGE_GU(0, 16 / PG - GULATE, )
    if (!GULATE && is_ffn) LADD(9);
    STAMPC(w == 0, 10);
    HOLD_UNTIL(2, 80);
    LWAIT(4, 1, 4);
    LWAIT(2, NSW, 0);
    STAMPC(w == 0, 2);
    FRESH_IDS();
    v4u a[4];
#pragma unroll
    for (int s = 0; s < 4; ++s) a[s] = *(const v4u*)(lds + L_OP + (c >> 2) * OP_S + s * 64 + r * 16);
    const f32x4 zero = {0.f, 0.f, 0.f, 0.f};
    f32x4 d[8];
#pragma unroll
    for (int f = 0; f < 8; ++f) d[f] = mfma(a[0], F[f], zero);
#pragma unroll
    for (int s = 1; s < 4; ++s)
#pragma unroll
      for (int f = 0; f < 8; ++f) d[f] = mfma(a[s], F[8 * s + f], d[f]);
    float t[4];
#pragma unroll
    for (int j = 0; j < 4; ++j) t[j] = rs32(d[j][0], d[j + 4][0]);
    const float y0 = rs16(t[0], t[2]), y1 = rs16(t[1], t[3]);
    st128_sc1(v4u{as_u(y0), tag, as_u(y1), tag}, rws, (lane >> 3) * 8192 + (lane & 7) * 16, WS_Y + ((b * 40 + 8 * w) * 64 + head * 2 + half) * 128);
    STAMPC(w == 0, 3);
    if (!is_ffn) return;
  } else {
    {
      FRESH_IDS();
      const rsrc_t rwo = mk_rsrc(wo, ABL & 1 ? 0 : DM * NH * DH * 2);
      const int vo = r * (DM * 2) + c * 16, so = head * DH * (DM * 2) + w * 256;
      unsigned char* scr = lds + L_BG + w * 16384;
#define OUT_LDS(s, t, f0, f1) *(v4u*)(lds + L_F4 + ((t) * 8 + 2 * (s)) * 1024 + lane * 16) = f0; *(v4u*)(lds + L_F4 + ((t) * 8 + 2 * (s) + 1) * 1024 + lane * 16) = f1;
#pragma nounroll
      for (int s = 0; s < 4; ++s) WO_STEP(s, OUT_LDS)
      asm volatile("s_waitcnt lgkmcnt(0)" ::: "memory");
      LFLAG(3) = 1u;
    }
    float Mo = 0.f, Lo = 0.f;
    bool sent = false;
    const int mls = WS_ML + (b * NH + head) * 256;
#define SEND_ML() { \
      const f32x4 m01 = *(const f32x4*)(lds + L_ML), m23 = *(const f32x4*)(lds + L_ML + 16); \
      Mo = fmaxf(fmaxf(m01[0], m01[2]), fmaxf(m23[0], m23[2])); \
      Lo = fmaf(__builtin_amdgcn_exp2f(m01[0] - Mo), m01[1], __builtin_amdgcn_exp2f(m01[2] - Mo) * m01[3]) + fmaf(__builtin_amdgcn_exp2f(m23[0] - Mo), m23[1], __builtin_amdgcn_exp2f(m23[2] - Mo) * m23[3]); \
      st128_sc1(v4u{as_u(Mo), tag, as_u(Lo), tag}, rws, fresh_lane() == 0 ? 0 : 0x40000000, mls + half * 128); sent = true; }
    STAGE_GU(0, 16 / PG - GULATE, if (!sent && __builtin_amdgcn_readfirstlane(LFLAG(6)) >= (unsigned)NSW) { asm volatile("" ::: "memory"); SEND_ML() })
    if (!GULATE && is_ffn) LADD(9);
    if (!sent) { LWAIT(6, NSW, 8); SEND_ML() }
    {
      v4u g;
      for (unsigned spin = 0;; ++spin) {
        g = __builtin_amdgcn_raw_buffer_load_b128(rws, 0, mls + (half ^ 1) * 128, AUX_SC1);
        if (__all(TAGBAD(g) == 0) || spin > SPIN_CAP) break;
        __builtin_amdgcn_s_sleep(16);
        asm volatile("" ::: "memory");
      }
      const float Mg = fmaxf(Mo, as_f(g[0]));
      const float Lg = __builtin_amdgcn_exp2f(Mo - Mg) * Lo + __builtin_amdgcn_exp2f(as_f(g[0]) - Mg) * as_f(g[2]);
      *(f32x2*)(lds + L_ML + 32) = f32x2{Mg, 1.0f / Lg};
      asm volatile("s_waitcnt lgkmcnt(0)" ::: "memory");
      LFLAG(7) = 1u;
      STAMPC(true, 13);
    }
    LWAIT(4, 1, 4);
    if (is_red) {
      FRESH_IDS();
      const int ys = WS_Y + (rb * 40 + cb) * 8192;
      v4u y[8];
      for (unsigned spin = 0;; ++spin) {
#pragma unroll
        for (int k = 0; k < 8; ++k) y[k] = __builtin_amdgcn_raw_buffer_load_b128(rws, lane * 16, ys + k * 1024, AUX_SC1);
        SB();
        unsigned bad = 0;
#pragma unroll
        for (int k = 0; k < 8; ++k) bad |= TAGBAD(y[k]);
        if (__all(bad == 0) || spin > SPIN_CAP) break;
        asm volatile("" ::: "memory");
      }
      STAMPC(true, 12);
      float s0 = ((as_f(y[0][0]) + as_f(y[1][0])) + (as_f(y[2][0]) + as_f(y[3][0]))) + ((as_f(y[4][0]) + as_f(y[5][0])) + (as_f(y[6][0]) + as_f(y[7][0])));
      float s1 = ((as_f(y[0][2]) + as_f(y[1][2])) + (as_f(y[2][2]) + as_f(y[3][2]))) + ((as_f(y[4][2]) + as_f(y[5][2])) + (as_f(y[6][2]) + as_f(y[7][2])));
      s0 += dppf<ROR8>(s0); s1 += dppf<ROR8>(s1);
      s0 = xrow_sum(s0); s1 = xrow_sum(s1);
      const unsigned hp2 = pk_bf16(lo_f(xres) + rbf(s0), hi_f(xres) + rbf(s1));
      __builtin_amdgcn_raw_buffer_store_b64(v2u{hp2, tag}, rws, (lane >> 3) < HREP ? (lane >> 3) * 10240 + (lane & 7) * 8 : 0x40000000, WS_H + (rb * 320 + cb * 8) * 8, AUX_SC1);
      STAMPC(true, 4);
      v4u z[7];
      const int zs = WS_Z + (rb * 40 + cb) * (NFFN * 128);
      const bool has7 = lane < NFFN * 8 - 6 * 64;
      for (unsigned spin = 0;; ++spin) {
#pragma unroll
        for (int k = 0; k < 6; ++k) z[k] = __builtin_amdgcn_raw_buffer_load_b128(rws, lane * 16, zs + k * 1024, AUX_SC1);
        z[6] = __builtin_amdgcn_raw_buffer_load_b128(rws, has7 ? lane * 16 + 6144 : lane * 16, zs, AUX_SC1);
        SB();
        unsigned bad = 0;
#pragma unroll
        for (int k = 0; k < 7; ++k) bad |= TAGBAD(z[k]);
        if (__all(bad == 0) || spin > SPIN_CAP) break;
        asm volatile("" ::: "memory");
      }
      STAMPC(true, 14);
      s0 = ((as_f(z[0][0]) + as_f(z[1][0])) + (as_f(z[2][0]) + as_f(z[3][0]))) + ((as_f(z[4][0]) + as_f(z[5][0])) + (has7 ? as_f(z[6][0]) : 0.f));
      s1 = ((as_f(z[0][2]) + as_f(z[1][2])) + (as_f(z[2][2]) + as_f(z[3][2]))) + ((as_f(z[4][2]) + as_f(z[5][2])) + (has7 ? as_f(z[6][2]) : 0.f));
      s0 += dppf<ROR8>(s0); s1 += dppf<ROR8>(s1);
      s0 = xrow_sum(s0); s1 = xrow_sum(s1);
      const rsrc_t rout = mk_rsrc(out, NB * DM * 2);
      __builtin_amdgcn_raw_buffer_store_b32(pk_bf16(lo_f(hp2) + rbf(s0), hi_f(hp2) + rbf(s1)), rout, lane < 8 ? lane * 4 : 0x40000000, ri * 32, AUX_OUT);
      if (bid == 0) __builtin_amdgcn_raw_buffer_store_b32(tag, rws, lane == 0 ? 0 : 0x40000000, WS_EPOCH, AUX_SC1);
      STAMPC(true, 7);
    } else if (is_ffn) {
      const int lane = fresh_lane();
      const rsrc_t rd = mk_rsrc(wd, DM * FF * 2);
      v4u D[20];
#pragma unroll
      for (int h2 = 0; h2 < 2; ++h2) {
#pragma unroll
        for (int n = 0; n < 20; ++n) D[n] = __builtin_amdgcn_raw_buffer_load_b128(rd, lane * 16, fi * 32 * (DM * 2) + (h2 * 20 + n) * 1024, AUX_W);
        SB();
        if (h2 == 0) LWAIT(2, NSW, 1);
#pragma unroll
        for (int n = 0; n < 20; ++n) *(v4u*)(lds + L_F4 + (h2 * 20 + n) * 1024 + lane * 16) = D[n];
        SB();
      }
    }
  }

  if (is_ffn) {
    FRESH_IDS();
    if (GULATE) { STAGE_GU(16 / PG - GULATE, 16 / PG, ) LADD(9); }
    v4u BG[20], BD[10];
    const f32x4 zero = {0.f, 0.f, 0.f, 0.f};
    unsigned char* gpw = lds + L_GP + (w * 4 + r) * GP_S + (c & 7) * 32 + (c >> 3) * 8;
    if (w < 4) {
      LWAIT(9, NHW, 0);
#pragma unroll
      for (int s = 0; s < 5; ++s) {
        v2u t[8];
#pragma unroll
        for (int e = 0; e < 8; ++e) t[e] = *(const v2u*)(lds + L_BG + (160 * w + (s < 4 ? 32 * r + 4 * s + (e & 3) + 16 * (e >> 2) : 128 + 8 * r + e)) * 128 + c * 8);
#pragma unroll
        for (int d = 0; d < 4; ++d) {
          BG[4 * s + 0][d] = perm_lo(t[2 * d][0], t[2 * d + 1][0]); BG[4 * s + 1][d] = perm_hi(t[2 * d][0], t[2 * d + 1][0]);
          BG[4 * s + 2][d] = perm_lo(t[2 * d][1], t[2 * d + 1][1]); BG[4 * s + 3][d] = perm_hi(t[2 * d][1], t[2 * d + 1][1]);
        }
      }
      const v4u ones = {0x3f803f80u, 0x3f803f80u, 0x3f803f80u, 0x3f803f80u};
      f32x4 cs[4] = {zero, zero, zero, zero};
#pragma unroll
      for (int s = 0; s < 5; ++s)
#pragma unroll
        for (int f = 0; f < 4; ++f) cs[f] = mfma(ones, BG[4 * s + f], cs[f]);
      *(f32x2*)gpw = f32x2{cs[0][0], cs[1][0]};
      *(f32x2*)(gpw + 16) = f32x2{cs[2][0], cs[3][0]};
    }
    BAR();
    if (w == 4) { BAR();
      return;
    } else {
    const unsigned char* gpr = lds + L_GP + (c >> 2) * GP_S + (4 * r + (c & 3)) * 16;
    f32x4 CS = (*(const f32x4*)gpr + *(const f32x4*)(gpr + 4 * GP_S)) + (*(const f32x4*)(gpr + 8 * GP_S) + *(const f32x4*)(gpr + 12 * GP_S));
#pragma unroll
    for (int s = 0; s < 5; ++s) {
      unsigned t[8];
#pragma unroll
      for (int e = 0; e < 8; ++e) t[e] = *(const unsigned*)(lds + L_F4 + (8 * r + e) * (DM * 2) + w * 320 + s * 64 + c * 4);
#pragma unroll
      for (int d = 0; d < 4; ++d) { BD[2 * s][d] = perm_lo(t[2 * d], t[2 * d + 1]); BD[2 * s + 1][d] = perm_hi(t[2 * d], t[2 * d + 1]); }
    }
#pragma unroll
    for (int n = 0; n < 20; ++n) asm volatile("" : "+v"(BG[n]));
#pragma unroll
    for (int n = 0; n < 10; ++n) asm volatile("" : "+v"(BD[n]));
    asm volatile("" : "+v"(CS));
    BAR();
    STAMPC(w == 0, 15);
    v4u g0, g1, g2;
    {
      const int o0 = (c >> 2) * 2560 + r * 128 + (c & 3) * 16, o1 = (c >> 2) * 2560 + 512 + r * 32 + (c & 1) * 16, hs = WS_H + (xs % HREP) * 10240 + w * 640;
      for (unsigned spin = 0;; ++spin) {
        g0 = __builtin_amdgcn_raw_buffer_load_b128(rws, o0, hs, AUX_SC1);
        g1 = __builtin_amdgcn_raw_buffer_load_b128(rws, o0 + 64, hs, AUX_SC1);
        g2 = __builtin_amdgcn_raw_buffer_load_b128(rws, o1, hs, AUX_SC1);
        SB();
        const unsigned bad = TAGBAD(g0) | TAGBAD(g1) | TAGBAD(g2);
        if (__all(bad == 0) || spin > SPIN_CAP) break;
        asm volatile("" ::: "memory");
      }
    }
    STAMPC(true, 28 + w);
    const v4u a0 = {g0[0], g0[2], g1[0], g1[2]}, a1 = {dppu<0x00>(g2[0]), dppu<0x00>(g2[2]), dppu<0x55>(g2[0]), dppu<0x55>(g2[2])};
    f32x4 dd[4][4];
#pragma unroll
    for (int s = 0; s < 4; ++s)
#pragma unroll
      for (int f = 0; f < 4; ++f) dd[s][f] = mfma(a0, BG[4 * s + f], zero);
#pragma unroll
    for (int f = 0; f < 4; ++f) dd[0][f] = mfma(a1, BG[16 + f], dd[0][f]);
    {
      const unsigned ax = (c & 2) ? g2[2] : g2[0];
      float s1 = dot2(ax, 0x3f803f80u, 0.f), s2 = dot2(ax, ax, 0.f);
#pragma unroll
      for (int d = 0; d < 4; ++d) { s1 = dot2(a0[d], 0x3f803f80u, s1); s2 = dot2(a0[d], a0[d], s2); }
      float ss = rs32(s1, s2);
      ss = rs16(ss, ss);
      ss += dppf<XOR1>(ss); ss += dppf<XOR2>(ss);
      *(float*)(lds + L_ST + (c >> 2) * 32 + (lane >> 5) * 16 + w * 4) = ss;
    }
    float pv[4];
#pragma unroll
    for (int f = 0; f < 4; ++f) pv[f] = (dd[0][f][0] + dd[1][f][1]) + (dd[2][f][2] + dd[3][f][3]);
    *(f32x2*)gpw = f32x2{pv[0], pv[1]};
    *(f32x2*)(gpw + 16) = f32x2{pv[2], pv[3]};
    BAR();
    STAMPC(w == 0, 5);
    const f32x4 t1 = *(const f32x4*)(lds + L_ST + (c >> 2) * 32), t2 = *(const f32x4*)(lds + L_ST + (c >> 2) * 32 + 16);
    f32x4 gu[4];
#pragma unroll
    for (int ww = 0; ww < 4; ++ww) gu[ww] = *(const f32x4*)(gpr + ww * 4 * GP_S);
    SB();
    const float e1 = ((t1[0] + t1[1]) + (t1[2] + t1[3])) * (1.0f / DM), e2 = ((t2[0] + t2[1]) + (t2[2] + t2[3])) * (1.0f / DM);
    const float var = fmaxf(fmaf(-e1, e1, e2), 0.f);
    const float mb = rbf(e1), rden = __builtin_amdgcn_rcpf(rbf(__builtin_amdgcn_sqrtf(rbf(rbf(var) + 1e-5f))));
    const f32x4 GU = (((gu[0] + gu[1]) + (gu[2] + gu[3])) - mb * CS) * rden;
    float av[2];
#pragma unroll
    for (int j = 0; j < 2; ++j) {
      const float g = rbf(GU[j]), u = rbf(GU[2 + j]);
      const float u2 = 2.0f * 0.7978845608028654f * LOG2E * (g + 0.044715f * g * g * g);
      av[j] = rbf(g * __builtin_amdgcn_rcpf(1.0f + __builtin_amdgcn_exp2f(-u2))) * u;
    }
    const unsigned ap = pk_bf16(av[0], av[1]);
    const v4u af = {dppu<0x00>(ap), dppu<0x55>(ap), dppu<0xAA>(ap), dppu<0xFF>(ap)};
    f32x4 z[10];
#pragma unroll
    for (int f = 0; f < 10; ++f) z[f] = mfma(af, BD[f], zero);
#pragma unroll
    for (int s = 0; s < 5; ++s)
      st128_sc1(v4u{as_u(z[2 * s][0]), tag, as_u(z[2 * s + 1][0]), tag}, rws, (r * 40 + (c >> 3)) * (NFFN * 128) + (c & 7) * 16, WS_Z + ((10 * w + 2 * s) * NFFN + fi) * 128);
    }
  }
  STAMPC(w == 0, 6);

  HOLD_UNTIL(1, 100);
  return;
  }
  {
    const int w = wv - NHW;
    const unsigned tstart = (unsigned)__builtin_amdgcn_s_memrealtime();
    const int lane = threadIdx.x & 63, r = lane >> 4, c = lane & 15;
    const rsrc_t rk = mk_rsrc(kc, NB * NH * SK * DH * 2), rv = mk_rsrc(vc, NB * NH * SK * DH * 2);
    const int voff = lane * 16;
    if (is_q) { __builtin_amdgcn_s_barrier(); LWAIT(9, NHW, 1); }
    v4u B[NPRE][8];
    int so = kv0;
    PREFETCH(NPRE, if (!is_q) __builtin_amdgcn_s_barrier();)
    *(f32x2*)(lds + L_SC + w * SC_S + 14 * 128 + lane * 8) = f32x2{-INFINITY, -INFINITY};
    LFLAG(10 + w) = 1u;
    STAMPC(w == 0, 11);
    LWAIT(1, 1, 1);
    const unsigned tag = __builtin_amdgcn_readfirstlane(LFLAG(5));
    const v4u q = *(const v4u*)(lds + L_Q + c * 16);
    STAMPC(w == 0, 1);

    int thr = __builtin_amdgcn_readfirstlane((half && pair_ffn) ? LAGF : is_q ? LAGQ : ns > 16 ? (half ? LAGPQ : LAGP) : LAGN);
    const int thr0 = thr;
#define PACE(rem) { \
      const unsigned fw_ = ((unsigned)__builtin_amdgcn_s_memrealtime() + (unsigned)(rem) * TAU) | 1u; \
      const unsigned ot_ = *(volatile LAS unsigned*)(LAS unsigned char*)(lds + L_PACE + (lane & 3) * 4); \
      *(volatile LAS unsigned*)(LAS unsigned char*)(lds + L_PACE + w * 4) = fw_; \
      int dl_ = ot_ == 0u ? 0 : (int)(fw_ - ot_); \
      dl_ += (int)dppu<XOR1>((unsigned)dl_); dl_ += (int)dppu<XOR2>((unsigned)dl_); \
      dl_ = __builtin_amdgcn_readfirstlane(dl_); \
      thr = dl_ > 4 * PT1 ? (thr0 > 0 ? thr0 - 1 : 0) : dl_ < -4 * PT1 ? thr0 + 1 : thr0; \
      thr = __builtin_amdgcn_readfirstlane(thr + ((int)(fw_ - tstart) < XLO ? 1 : 0) + ((int)(fw_ - tstart) < XLO2 ? 1 : 0) - ((int)(fw_ - tstart) > XHI && thr > 0 ? 1 : 0)); SB(); }
    unsigned char* sc = lds + L_SC + w * SC_S + r * 32 + (c >> 1) * 4;
    for (int t = 0; t < naux; ++t) so = NEXT(so);
    KSTEP(0, 1, rk, AUX_K, so)
    KSTEP(1, 0, rk, AUX_K, 0)
    KSTEP(2, 0, rk, AUX_K, 0)
    KSTEP(3, 0, rk, AUX_K, 0)
    sc += naux * 128;
    for (int it = NPRE + naux; it < ns - 1; ++it) { so = NEXT(so); KSTEP(0, 2, rk, AUX_K, so) PACE(2 * ns - 1 - it) STAMPC(w == 0 && it == 7, 20); }
    so = kv0;
    KSTEP(0, 2, rv, AUX_V, so)
    STAMPC(w == 0, 21);
    float mw;
    {
      if (NAUX && naux) LWAIT(10 + w, 2, 1);
      asm volatile("" ::: "memory");
      const f32x4 s0 = *(const f32x4*)(lds + L_SC + w * SC_S + lane * 32), s1 = *(const f32x4*)(lds + L_SC + w * SC_S + lane * 32 + 16);
      const f32x4 s2 = *(const f32x4*)(lds + L_SC + w * SC_S + 2048 + (lane & 7) * 32), s3 = *(const f32x4*)(lds + L_SC + w * SC_S + 2048 + (lane & 7) * 32 + 16);
      float mx = -INFINITY;
#pragma unroll
      for (int j = 0; j < 4; ++j) mx = fmaxf(fmaxf(mx, fmaxf(s0[j], s1[j])), fmaxf(s2[j], s3[j]));
      mx = fmaxf(mx, dppf<ROR8>(mx)); mx = fmaxf(mx, dppf<ROR4>(mx)); mx = fmaxf(mx, dppf<ROR2>(mx)); mx = fmaxf(mx, dppf<ROR1>(mx));
      mw = xrow_max(mx) * LOG2E;
      float p[8], e[8];
#pragma unroll
      for (int j = 0; j < 4; ++j) { p[j] = __builtin_amdgcn_exp2f(fmaf(s0[j], LOG2E, -mw)); p[4 + j] = __builtin_amdgcn_exp2f(fmaf(s1[j], LOG2E, -mw));
                                    e[j] = __builtin_amdgcn_exp2f(fmaf(s2[j], LOG2E, -mw)); e[4 + j] = __builtin_amdgcn_exp2f(fmaf(s3[j], LOG2E, -mw)); }
      asm volatile("" ::: "memory");
      *(v4u*)(lds + L_SC + w * SC_S + lane * 16) = v4u{pk_bf16(p[0], p[1]), pk_bf16(p[2], p[3]), pk_bf16(p[4], p[5]), pk_bf16(p[6], p[7])};
      *(v4u*)(lds + L_SC + w * SC_S + 1024 + (lane & 7) * 16) = v4u{pk_bf16(e[0], e[1]), pk_bf16(e[2], e[3]), pk_bf16(e[4], e[5]), pk_bf16(e[6], e[7])};
      const float le = ((e[0] + e[1]) + (e[2] + e[3])) + ((e[4] + e[5]) + (e[6] + e[7]));
      float l = ((p[0] + p[1]) + (p[2] + p[3])) + ((p[4] + p[5]) + (p[6] + p[7])) + (lane < 8 ? le : 0.f);
      l += dppf<ROR8>(l); l += dppf<ROR4>(l); l += dppf<ROR2>(l); l += dppf<ROR1>(l);
      l = xrow_sum(l);
      if (lane == 0) *(f32x2*)(lds + L_ML + w * 8) = f32x2{mw, l};
      LADD(6);
      SB();
    }
    float acc[8];
#pragma unroll
    for (int e = 0; e < 8; ++e) acc[e] = 0.f;
    const unsigned char* pp = lds + L_SC + w * SC_S + r * 16;
#define VSTEP(MODE, NXT) { \
    const v4u pw = *(const v4u*)pp; pp += 64; \
    _Pragma("unroll") for (int j = 0; j < 8; ++j) { \
      const unsigned u = pw[j >> 1], pl = (j & 1) ? u >> 16 : u & 0xffffu, ph = (j & 1) ? u & 0xffff0000u : u << 16; \
      _Pragma("unroll") for (int d = 0; d < 4; ++d) { acc[2 * d] = dot2(B[0][j][d], pl, acc[2 * d]); acc[2 * d + 1] = dot2(B[0][j][d], ph, acc[2 * d + 1]); } \
      asm volatile("" : "+v"(acc[0]), "+v"(acc[1]), "+v"(acc[2]), "+v"(acc[3]), "+v"(acc[4]), "+v"(acc[5]), "+v"(acc[6]), "+v"(acc[7])); SB(); \
      REFILL(j, MODE, rv, AUX_V, NXT) } }
    for (int it = 0; it < ns - 1; ++it) { so = NEXT(so); thr = __builtin_amdgcn_readfirstlane(it + BOOST >= ns ? (thr > thr0 ? 1 : 0) : thr); VSTEP(2, so) PACE(ns - 1 - it) STAMPC(w == 0 && it == 7, 22); }
    if (w == 0) LFLAG(4) = 1u;
    LWAIT(3, 1, 1);
    LWAIT(7, 1, 1);
#pragma unroll
    for (int n = 0; n < 8; ++n) B[1][n] = *(const v4u*)(lds + L_F4 + (w * 8 + n) * 1024 + lane * 16);
    const f32x2 gl = *(const f32x2*)(lds + L_ML + 32);
    float f = __builtin_amdgcn_exp2f(mw - gl.x) * gl.y;
    asm volatile("" : "+v"(f));
    SB();
    VSTEP(0, 0)
    STAMPC(true, 16 + w);
    {
      float t[4];
#pragma unroll
      for (int j = 0; j < 4; ++j) t[j] = rs32(acc[j], acc[j + 4]);
      *(unsigned*)(lds + L_OP + w * OP_S + (4 * c + r) * 4) = pk_bf16(rs16(t[0], t[2]) * f, rs16(t[1], t[3]) * f);
    }
    LADD(2);
    LWAIT(2, NSW, 0);
    {
      const rsrc_t rws = mk_rsrc(ws, WS_END);
      v4u a[4];
#pragma unroll
      for (int s = 0; s < 4; ++s) a[s] = *(const v4u*)(lds + L_OP + (c >> 2) * OP_S + s * 64 + r * 16);
      f32x4 d0 = {0.f, 0.f, 0.f, 0.f}, d1 = {0.f, 0.f, 0.f, 0.f};
#pragma unroll
      for (int s = 0; s < 4; ++s) { d0 = mfma(a[s], B[1][2 * s], d0); d1 = mfma(a[s], B[1][2 * s + 1], d1); }
      const float y0 = xrow_sum(d0[0]), y1 = xrow_sum(d1[0]);
      st128_sc1(v4u{as_u(y0), tag, as_u(y1), tag}, rws, lane < 16 ? (c >> 3) * 8192 + (c & 7) * 16 : 0x40000000, WS_Y + ((b * 40 + 32 + 2 * w) * 64 + head * 2 + half) * 128);
    }
  }
}

extern "C" void kernel_launch(void* const* d_in, const int* in_sizes, int n_in, void* d_out, int out_size, void* d_ws, size_t ws_size, hipStream_t stream) {
  fused_block<<<NWG, NT, 0, stream>>>((const u16*)d_in[1], (const u16*)d_in[2], d_ws, (const u16*)d_in[0], (const u16*)d_in[3], (const u16*)d_in[4],
                                      (const u16*)d_in[6], (const u16*)d_in[7], (const u16*)d_in[8], (u16*)d_out);
}
```

```cpp
#include <hip/hip_runtime.h>
#include <stdint.h>

typedef unsigned short u16;
typedef unsigned v4u __attribute__((ext_vector_type(4)));
typedef unsigned v2u __attribute__((ext_vector_type(2)));
typedef float f32x2 __attribute__((ext_vector_type(2)));
typedef float f32x4 __attribute__((ext_vector_type(4)));
typedef __bf16 bf16x2 __attribute__((ext_vector_type(2)));
typedef __bf16 bf16x8 __attribute__((ext_vector_type(8)));
typedef __amdgpu_buffer_rsrc_t rsrc_t;
#define GAS __attribute__((address_space(1)))
#define LAS __attribute__((address_space(3)))

constexpr int NB = 4, DM = 640, NH = 32, SK = 4096, DH = 128, FF = 1728;
constexpr int NSW = 4, NHW = 5, NT = 64 * (NSW + NHW), NWG = 256;
constexpr int NRED = 80, NFFN = 54;
constexpr int NPRE = 4, NAUX = 6;
constexpr int STEP = 2 * NSW * 4096;
constexpr unsigned TAU = 113;
constexpr int PLEAD = 48, SCHED1 = 3930, SCHED2 = 3875, BOOST = 3;

constexpr int WS_EPOCH = 0;
constexpr int WS_ML = 32768;
constexpr int WS_Q = 65536;
constexpr int WS_Y = WS_Q + NB * NH * 512;
constexpr int WS_Z = WS_Y + NB * 20 * 64 * 128;
constexpr int WS_H = WS_Z + NB * 20 * NFFN * 128;
constexpr int WS_END = WS_H + 8 * 10240;
constexpr int AUX_NT = 2, AUX_SC1 = 16;
constexpr int OOB = 0x40000000;
constexpr unsigned SPIN_CAP = 1u << 18;
constexpr float LOG2E = 1.4426950408889634f;

enum { F_QPART = 0, F_QRDY = 1, F_ODONE = 2, F_FRAG = 3, F_LAST = 4, F_TAG = 5, F_KDONE = 6, F_NORM = 7, F_STAT = 8, F_WGT = 9, F_AUX = 10 };
constexpr int L_FLAG = 0;
constexpr int L_ML = 64;
constexpr int L_Q = 128;
constexpr int L_ST = 384;
constexpr int L_PACE = 512;
constexpr int OP_S = 320;
constexpr int L_OP = 576;
constexpr int GP_S = 384;
constexpr int L_GP = L_OP + 4 * OP_S;
constexpr int L_BG = L_GP + 5 * 4 * GP_S;
constexpr int L_F4 = L_BG + 640 * 128;
constexpr int L_SC = L_F4 + 32 * 1024;
constexpr int SC_S = 2304;
constexpr int L_END = L_SC + 4 * SC_S;
static_assert(L_BG % 16 == 0 && L_OP % 16 == 0 && L_GP % 16 == 0 && L_END - L_F4 >= 32 * DM * 2 && L_END <= 160 * 1024, "LDS map");

__device__ __forceinline__ rsrc_t mk_rsrc(const void* p, int bytes) { return __builtin_amdgcn_make_buffer_rsrc((void*)p, 0, bytes, 0x00020000); }
__device__ __forceinline__ float as_f(unsigned u) { return __builtin_bit_cast(float, u); }
__device__ __forceinline__ unsigned as_u(float f) { return __builtin_bit_cast(unsigned, f); }
__device__ __forceinline__ unsigned pk_bf16(float lo, float hi) { f32x2 v = {lo, hi}; return __builtin_bit_cast(unsigned, __builtin_convertvector(v, bf16x2)); }
__device__ __forceinline__ float rbf(float x) { return as_f(pk_bf16(0.f, x)); }
__device__ __forceinline__ float lo_f(unsigned u) { return as_f(u << 16); }
__device__ __forceinline__ float hi_f(unsigned u) { return as_f(u & 0xffff0000u); }
__device__ __forceinline__ float dot2(unsigned a, unsigned b, float c) { return __builtin_amdgcn_fdot2_f32_bf16(__builtin_bit_cast(bf16x2, a), __builtin_bit_cast(bf16x2, b), c, false); }
__device__ __forceinline__ f32x4 mfma(v4u a, v4u b, f32x4 c) { return __builtin_amdgcn_mfma_f32_16x16x32_bf16(__builtin_bit_cast(bf16x8, a), __builtin_bit_cast(bf16x8, b), c, 0, 0, 0); }
template <int CTRL> __device__ __forceinline__ unsigned dppu(unsigned x) { return __builtin_amdgcn_mov_dpp((int)x, CTRL, 0xf, 0xf, true); }
template <int CTRL> __device__ __forceinline__ float dppf(float x) { return as_f(dppu<CTRL>(as_u(x))); }
constexpr int ROR1 = 0x121, ROR2 = 0x122, ROR4 = 0x124, ROR8 = 0x128, XOR1 = 0xB1, XOR2 = 0x4E, XOR7 = 0x141;
__device__ __forceinline__ unsigned perm_lo(unsigned a, unsigned b) { return __builtin_amdgcn_perm(b, a, 0x05040100u); }
__device__ __forceinline__ unsigned perm_hi(unsigned a, unsigned b) { return __builtin_amdgcn_perm(b, a, 0x07060302u); }
__device__ __forceinline__ float rs16(float a, float b) { auto s = __builtin_amdgcn_permlane16_swap(as_u(a), as_u(b), false, false); return as_f(s[0]) + as_f(s[1]); }
__device__ __forceinline__ float rs32(float a, float b) { auto s = __builtin_amdgcn_permlane32_swap(as_u(a), as_u(b), false, false); return as_f(s[0]) + as_f(s[1]); }
__device__ __forceinline__ float xrow_sum(float x) { x = rs16(x, x); return rs32(x, x); }
__device__ __forceinline__ float xrow_max(float x) {
  auto s = __builtin_amdgcn_permlane16_swap(as_u(x), as_u(x), false, false); x = fmaxf(as_f(s[0]), as_f(s[1]));
  auto t = __builtin_amdgcn_permlane32_swap(as_u(x), as_u(x), false, false); return fmaxf(as_f(t[0]), as_f(t[1]));
}
__device__ __forceinline__ void st128_sc1(v4u v, rsrc_t rs, int voff, int soff) {
  __builtin_amdgcn_raw_buffer_store_b128(v, rs, voff, soff, AUX_SC1);
  asm volatile("s_nop 1" :: "v"(v) : "memory");
}
__device__ __forceinline__ v4u ld128_sc1(rsrc_t rs, int voff, int soff) { return __builtin_amdgcn_raw_buffer_load_b128(rs, voff, soff, AUX_SC1); }
__device__ __forceinline__ int fresh_lane() { int l; asm volatile("v_mbcnt_lo_u32_b32 %0, -1, 0\n\tv_mbcnt_hi_u32_b32 %0, -1, %0" : "=v"(l)); return l; }
__device__ __forceinline__ unsigned ln_pair(unsigned v, float mb, float rden) { return pk_bf16(rbf(lo_f(v) - mb) * rden, rbf(hi_f(v) - mb) * rden); }
__device__ __forceinline__ float ln_rden(float var) { return __builtin_amdgcn_rcpf(rbf(__builtin_amdgcn_sqrtf(rbf(rbf(var) + 1e-5f)))); }
__device__ __forceinline__ f32x2 ln_stats(const unsigned (&hv)[5]) {
  const float piv = as_f(__builtin_amdgcn_readfirstlane(hv[0] << 16));
  float s1 = 0.f, s2 = 0.f;
#pragma unroll
  for (int j = 0; j < 5; ++j) { const float d0 = lo_f(hv[j]) - piv, d1 = hi_f(hv[j]) - piv; s1 += d0 + d1; s2 = fmaf(d0, d0, s2); s2 = fmaf(d1, d1, s2); }
  float ss = rs32(s1, s2);
  ss = rs16(ss, ss);
  ss += dppf<ROR8>(ss); ss += dppf<ROR4>(ss); ss += dppf<ROR2>(ss); ss += dppf<ROR1>(ss);
  const float e1 = as_f(__builtin_amdgcn_readlane(as_u(ss), 0)) * (1.0f / DM), e2 = as_f(__builtin_amdgcn_readlane(as_u(ss), 32)) * (1.0f / DM);
  return f32x2{rbf(piv + e1), ln_rden(fmaxf(fmaf(-e1, e1, e2), 0.f))};
}
__device__ __forceinline__ void frag4(const v2u* t, v4u* f) {
#pragma unroll
  for (int d = 0; d < 4; ++d) {
    f[0][d] = perm_lo(t[2 * d][0], t[2 * d + 1][0]); f[1][d] = perm_hi(t[2 * d][0], t[2 * d + 1][0]);
    f[2][d] = perm_lo(t[2 * d][1], t[2 * d + 1][1]); f[3][d] = perm_hi(t[2 * d][1], t[2 * d + 1][1]);
  }
}
#define SB() __builtin_amdgcn_sched_barrier(0)
#define FENCE() asm volatile("" ::: "memory")
#define LGKM0() asm volatile("s_waitcnt lgkmcnt(0)" ::: "memory")
#define VMWAIT(n) do { SB(); asm volatile("s_waitcnt vmcnt(%0)" :: "i"(n) : "memory"); SB(); } while (0)
#define BAR() do { LGKM0(); __builtin_amdgcn_s_barrier(); FENCE(); } while (0)
#define TAGBAD(g) (((g)[1] ^ tag) | ((g)[3] ^ tag))
#define FRESH_IDS() const int lane = fresh_lane(); const int r = lane >> 4, c = lane & 15; (void)r; (void)c
#define LWORD(off) (*(volatile LAS unsigned*)(LAS unsigned char*)(lds + (off)))
#define LFLAG(i) LWORD(L_FLAG + 4 * (i))
#define LWAIT(i, n, SLP) do { unsigned spin_ = 0; _Pragma("nounroll") while (LFLAG(i) < (unsigned)(n) && ++spin_ < (SPIN_CAP << 4)) { if (SLP) __builtin_amdgcn_s_sleep(SLP); } FENCE(); } while (0)
#define LADD(i) do { LGKM0(); if (fresh_lane() == 0) __hip_atomic_fetch_add((LAS unsigned*)(LAS unsigned char*)(lds + L_FLAG + 4 * (i)), 1u, __ATOMIC_RELAXED, __HIP_MEMORY_SCOPE_WORKGROUP); FENCE(); } while (0)

__device__ __forceinline__ int ffn_of_rank(int j) { const int s = j - 64; if (s < 0) return -1; const int k = (s & 7) + 8 * (s >> 4); return k < 27 ? 2 * k + ((s >> 3) & 1) : -1; }

__global__ void __launch_bounds__(NT, 3) fused_block(const u16* __restrict__ kc, const u16* __restrict__ vc, void* ws, const u16* __restrict__ x,
                                                     const u16* __restrict__ wq, const u16* __restrict__ wo, const u16* __restrict__ wg,
                                                     const u16* __restrict__ wu, const u16* __restrict__ wd, u16* __restrict__ out) {
  __shared__ __attribute__((aligned(16))) unsigned char lds[L_END];
  const int wv = __builtin_amdgcn_readfirstlane(threadIdx.x >> 6);
  const int bid = blockIdx.x, xs = bid & 7, rr = bid >> 3;
  const int head = xs + 8 * (rr >> 3), b = (rr >> 1) & 3, half = rr & 1;
  const int rank = xs + 8 * (bid >> 4);
  const bool pair_ffn = ffn_of_rank(rank) >= 0, is_q = !half && rank < 64;

  const int sw = wv >= NHW ? wv - NHW : wv;
  const int mv = pair_ffn ? 1 : rank < 64 ? -1 : 0;
  const int ns = half ? 16 - mv : 16 + mv;
  const int kvbase = (b * NH + head) * (SK * DH * 2) + (half ? (16 + mv) * STEP : 0) + sw * 4096;
  const int naux = (is_q || (half && pair_ffn)) ? 0 : NAUX;
  const int kvend = kvbase + ns * STEP, kv0 = kvbase + (4 * (head & 1) + 8 * ((head >> 3) & 1)) * STEP;
#define NEXT(so) ((so) + STEP == kvend ? kvbase : (so) + STEP)
#define LD(RS, i, so) __builtin_amdgcn_raw_buffer_load_b128(RS, voff, (so) + ((i) >> 2) * (NSW * 4096) + ((i) & 3) * 1024, AUX_NT)
#define THROTTLE() asm volatile("s_cmp_lt_u32 %0, 1\n\ts_cbranch_scc1 1f\n\ts_waitcnt vmcnt(6)\n\ts_cmp_lt_u32 %0, 2\n\ts_cbranch_scc1 1f\n\ts_waitcnt vmcnt(5)\n\t" \
                                "s_cmp_lt_u32 %0, 3\n\ts_cbranch_scc1 1f\n\ts_waitcnt vmcnt(4)\n\ts_cmp_lt_u32 %0, 4\n\ts_cbranch_scc1 1f\n\ts_waitcnt vmcnt(3)\n1:" :: "s"(thr) : "scc", "memory")
#define REFILL(i, MODE, RS, NXT) if (MODE) { if (MODE == 2) THROTTLE(); B[0][i] = LD(RS, i, NXT); SB(); }
#define KSTEP(T, MODE, RS, NXT) { \
    float sx[8]; \
    _Pragma("unroll") for (int j = 0; j < 8; ++j) { \
      float d = dot2(B[T][j][0], q[0], 0.f); d = dot2(B[T][j][1], q[1], d); d = dot2(B[T][j][2], q[2], d); d = dot2(B[T][j][3], q[3], d); \
      asm volatile("" : "+v"(d)); sx[j] = d; SB(); REFILL(j, MODE, RS, NXT) } \
    float t4[4], t2[2]; \
    _Pragma("unroll") for (int j = 0; j < 4; ++j) { const float lo = sx[j] + dppf<ROR8>(sx[j]), hi = sx[j + 4] + dppf<ROR8>(sx[j + 4]); t4[j] = (c & 8) ? hi : lo; } \
    _Pragma("unroll") for (int j = 0; j < 2; ++j) { const float lo = t4[j] + dppf<XOR7>(t4[j]), hi = t4[j + 2] + dppf<XOR7>(t4[j + 2]); t2[j] = (c & 4) ? hi : lo; } \
    const float lo = t2[0] + dppf<XOR2>(t2[0]), hi = t2[1] + dppf<XOR2>(t2[1]); \
    float s = (c & 2) ? hi : lo; \
    s += dppf<XOR1>(s); \
    *(float*)sc = rbf(rbf(s) * 0.08838834764831845f); sc += 128; SB(); }

  if (wv < NHW) {
    const int w = wv;
    const int rj = half ? (rank < 64 ? rank : 255) : (rank >= 64 && rank < NRED ? rank : 255);
    __builtin_amdgcn_s_setprio(2);
    if (threadIdx.x < 16) { *(unsigned*)(lds + L_FLAG + threadIdx.x * 4) = 0u; *(unsigned*)(lds + L_PACE + threadIdx.x * 4) = 0u; }
    const rsrc_t rws = mk_rsrc(ws, WS_END);
    const f32x4 zero = {0.f, 0.f, 0.f, 0.f};
    unsigned tag, xres;
    {
      FRESH_IDS();
      const rsrc_t rx = mk_rsrc(x, NB * DM * 2);
      if (is_q) {
        const rsrc_t rwq = mk_rsrc(wq, DM * NH * DH * 2);
        const v4u x8 = __builtin_amdgcn_raw_buffer_load_b128(rx, lane * 16, w * (DM * 2), 0);
        const unsigned x2 = __builtin_amdgcn_raw_buffer_load_b32(rx, lane * 4 + 1024, w * (DM * 2), 0);
        v4u xa[4];
#pragma unroll
        for (int s = 0; s < 4; ++s) xa[s] = __builtin_amdgcn_raw_buffer_load_b128(rx, (c >> 2) * (DM * 2) + r * 16, (128 * w + 32 * s) * 2, 0);
        const unsigned epoch_v = __builtin_amdgcn_raw_buffer_load_b32(rws, 0, WS_EPOCH, AUX_SC1);
        xres = 0u;
        SB();
        v2u Wq[32];
#define WQLD(n) Wq[n] = __builtin_amdgcn_raw_buffer_load_b64(rwq, (8 * r) * 8192 + c * 8, (128 * w + 32 * ((n) >> 3) + ((n) & 7)) * 8192 + rank * 128, AUX_NT)
#pragma unroll
        for (int n = 0; n < 12; ++n) WQLD(n);
        SB();
        BAR();
        if (w < 4) {
          const unsigned hv[5] = {x8[0], x8[1], x8[2], x8[3], x2};
          *(f32x2*)(lds + L_ST + w * 8) = ln_stats(hv);
          LADD(F_STAT);
        }
        SB();
#pragma unroll
        for (int n = 12; n < 20; ++n) WQLD(n);
        SB();
        LWAIT(F_STAT, 4, 0);
        const f32x2 st = *(const f32x2*)(lds + L_ST + (c >> 2) * 8);
#pragma unroll
        for (int s = 0; s < 4; ++s) {
          xa[s] = v4u{ln_pair(xa[s][0], st.x, st.y), ln_pair(xa[s][1], st.x, st.y), ln_pair(xa[s][2], st.x, st.y), ln_pair(xa[s][3], st.x, st.y)};
          asm volatile("" : "+v"(xa[s]));
          SB();
#pragma unroll
          for (int n = 20 + 3 * s; n < 23 + 3 * s; ++n) WQLD(n);
          SB();
        }
        LADD(F_WGT);
        f32x4 dq[4] = {zero, zero, zero, zero};
#pragma unroll
        for (int s = 0; s < 4; ++s) {
          v4u bf[4];
          frag4(Wq + 8 * s, bf);
#pragma unroll
          for (int f = 0; f < 4; ++f) dq[f] = mfma(xa[s], bf[f], dq[f]);
          SB();
        }
        *(f32x4*)(lds + L_GP + w * 1024 + lane * 16) = f32x4{dq[0][0], dq[1][0], dq[2][0], dq[3][0]};
        tag = __builtin_amdgcn_readfirstlane(epoch_v) + 1u;
        LADD(F_QPART);
        if (w == 1) {
          LWAIT(F_QPART, NHW, 0);
          f32x4 pq[NHW];
#pragma unroll
          for (int ww = 0; ww < NHW; ++ww) pq[ww] = *(const f32x4*)(lds + L_GP + ww * 1024 + lane * 16);
          const f32x4 sq = ((pq[0] + pq[1]) + (pq[2] + pq[3])) + pq[4];
          st128_sc1(v4u{pk_bf16(sq[0], sq[1]), tag, pk_bf16(sq[2], sq[3]), tag}, rws, r * (NH * 512) + c * 16, WS_Q + rank * 256);
        }
      } else {
        const unsigned epoch_v = __builtin_amdgcn_raw_buffer_load_b32(rws, 0, WS_EPOCH, AUX_SC1);
        xres = __builtin_amdgcn_raw_buffer_load_b32(rx, (lane & 7) * 8 + (lane >> 5) * 4, rj * 64, 0);
        SB();
        BAR();
        tag = __builtin_amdgcn_readfirstlane(epoch_v) + 1u;
      }
      if (w == 4) {
        v4u g;
        for (unsigned spin = 0;; ++spin) {
          g = ld128_sc1(rws, (lane & 31) * 16, WS_Q + (b * NH + head) * 512);
          if (__all(TAGBAD(g) == 0) || spin > SPIN_CAP) break;
          __builtin_amdgcn_s_sleep(16);
          FENCE();
        }
        *(v2u*)(lds + L_Q + (lane & 31) * 8) = v2u{g[0], g[2]};
        LFLAG(F_TAG) = tag;
        LGKM0();
        LFLAG(F_QRDY) = 1u;
      } else if (naux) {
        const rsrc_t rk = mk_rsrc(kc, NB * NH * SK * DH * 2);
        const int voff = lane * 16, thr = 0;
        v4u B[4][8];
        unsigned char* park = lds + L_BG + w * 16384 + lane * 16;
        LWAIT(F_AUX + w, 1, 1);
        int so = kv0;
#pragma unroll
        for (int t = 0; t < NPRE; ++t) so = NEXT(so);
#pragma unroll
        for (int t = 0; t < NAUX; ++t, so = NEXT(so))
#pragma unroll
          for (int i = 0; i < 8; ++i) {
            if (t >= 1 && t - 1 < NAUX - 4) { *(v4u*)(park + ((t - 1) * 8 + i) * 1024) = B[(t - 1) & 3][i]; SB(); }
            B[t & 3][i] = LD(rk, i, so);
            if (8 * t + i >= 7 && 8 * t + i + 1 < 8 * NAUX) VMWAIT(7);
          }
        SB();
        LWAIT(F_QRDY, 1, 1);
        const v4u q = *(const v4u*)(lds + L_Q + c * 16);
        unsigned char* sc = lds + L_SC + w * SC_S + (NPRE + NAUX - 4) * 128 + r * 32 + (c >> 1) * 4;
#pragma unroll
        for (int t = NAUX - 4; t < NAUX; ++t) KSTEP(t & 3, 0, rk, 0)
        sc -= NAUX * 128;
#pragma unroll
        for (int t = 0; t < NAUX - 4; ++t) {
#pragma unroll
          for (int i = 0; i < 8; ++i) B[0][i] = *(const v4u*)(park + (t * 8 + i) * 1024);
          KSTEP(0, 0, rk, 0)
        }
        LGKM0();
        LFLAG(F_AUX + w) = 2u;
      }
    }

    const bool is_red = rj < NRED;
    const int rb = rj / 20, cb = rj % 20;
    const int fi = half ? ffn_of_rank(rank) : -1;
    const bool is_ffn = fi >= 0;

#define STAGE_GU(HOOK) if (is_ffn) { \
      const int ln = fresh_lane(); \
      const GAS unsigned char* gp = (const GAS unsigned char*)((ln & 4) ? wu : wg) + (size_t)((128 * w + (ln >> 3)) * (FF * 2) + fi * 64 + (ln & 3) * 16); \
      _Pragma("nounroll") for (int s = 0; s < 7; ++s) { \
        HOOK \
        const v4u g0 = *(const GAS v4u*)gp, g1 = *(const GAS v4u*)(gp + 8 * (FF * 2)); \
        gp += 16 * (FF * 2); \
        *(v4u*)(lds + L_BG + (w * 16 + 2 * s) * 1024 + ln * 16) = g0; \
        *(v4u*)(lds + L_BG + (w * 16 + 2 * s + 1) * 1024 + ln * 16) = g1; \
      } }
#define WO_STEP(s, OUT) { \
      v4u T[8]; \
      _Pragma("unroll") for (int e = 0; e < 8; ++e) { \
        T[e] = __builtin_amdgcn_raw_buffer_load_b128(rwo, vo, so + (32 * (s) + 4 * e) * (DM * 2), 0); \
        if (e == 7) VMWAIT(0); \
        else if (e & 1) { SB(); asm volatile("s_cmp_lt_i32 %0, 0\n\ts_cbranch_scc0 1f\n\ts_waitcnt vmcnt(0)\n1:" :: "s"(fi) : "scc", "memory"); SB(); } } \
      _Pragma("unroll") for (int e = 0; e < 8; ++e) *(v4u*)(scr + e * 1024 + lane * 16) = T[e]; \
      FENCE(); \
      _Pragma("unroll") for (int t = 0; t < 4; ++t) { \
        unsigned u[8]; \
        _Pragma("unroll") for (int e = 0; e < 8; ++e) u[e] = *(const unsigned*)(scr + (8 * r + e) * 256 + t * 64 + c * 4); \
        v4u f0, f1; \
        _Pragma("unroll") for (int d = 0; d < 4; ++d) { f0[d] = perm_lo(u[2 * d], u[2 * d + 1]); f1[d] = perm_hi(u[2 * d], u[2 * d + 1]); } \
        OUT(s, t, f0, f1) } \
      FENCE(); SB(); }

    if (w < 4) {
      v4u F[32];
      {
        FRESH_IDS();
        const rsrc_t rwo = mk_rsrc(wo, DM * NH * DH * 2);
        const int vo = r * (DM * 2) + c * 16, so = head * DH * (DM * 2) + w * 256;
        unsigned char* scr = lds + L_BG + w * 16384;
#define OUT_REG(s, t, f0, f1) F[8 * (s) + 2 * (t)] = f0; F[8 * (s) + 2 * (t) + 1] = f1; asm volatile("" : "+v"(F[8 * (s) + 2 * (t)]), "+v"(F[8 * (s) + 2 * (t) + 1]));
        WO_STEP(0, OUT_REG) WO_STEP(1, OUT_REG) WO_STEP(2, OUT_REG) WO_STEP(3, OUT_REG)
      }
      STAGE_GU()
      if (is_ffn) LADD(F_WGT);
      LWAIT(F_LAST, 1, 4);
      LWAIT(F_ODONE, NSW, 0);
      FRESH_IDS();
      v4u a[4];
#pragma unroll
      for (int s = 0; s < 4; ++s) a[s] = *(const v4u*)(lds + L_OP + (c >> 2) * OP_S + s * 64 + r * 16);
      f32x4 d[8];
#pragma unroll
      for (int f = 0; f < 8; ++f) d[f] = mfma(a[0], F[f], zero);
#pragma unroll
      for (int s = 1; s < 4; ++s)
#pragma unroll
        for (int f = 0; f < 8; ++f) d[f] = mfma(a[s], F[8 * s + f], d[f]);
      float t[4];
#pragma unroll
      for (int j = 0; j < 4; ++j) t[j] = rs32(d[j][0], d[j + 4][0]);
      const unsigned yp = pk_bf16(rs16(t[0], t[2]), rs16(t[1], t[3]));
      st128_sc1(v4u{yp, tag, dppu<XOR1>(yp), tag}, rws, (lane & 1) ? OOB : r * 8192 + c * 8, WS_Y + ((b * 20 + 4 * w) * 64 + head * 2 + half) * 128);
      if (!is_ffn) return;
    } else {
      {
        FRESH_IDS();
        const rsrc_t rwo = mk_rsrc(wo, DM * NH * DH * 2);
        const int vo = r * (DM * 2) + c * 16, so = head * DH * (DM * 2) + w * 256;
        unsigned char* scr = lds + L_BG + w * 16384;
#define OUT_LDS(s, t, f0, f1) *(v4u*)(lds + L_F4 + ((t) * 8 + 2 * (s)) * 1024 + lane * 16) = f0; *(v4u*)(lds + L_F4 + ((t) * 8 + 2 * (s) + 1) * 1024 + lane * 16) = f1;
#pragma nounroll
        for (int s = 0; s < 4; ++s) WO_STEP(s, OUT_LDS)
        LGKM0();
        LFLAG(F_FRAG) = 1u;
      }
      float Mo = 0.f, Lo = 0.f;
      bool sent = false;
      const int mls = WS_ML + (b * NH + head) * 256;
#define SEND_ML() { \
        const f32x4 m01 = *(const f32x4*)(lds + L_ML), m23 = *(const f32x4*)(lds + L_ML + 16); \
        Mo = fmaxf(fmaxf(m01[0], m01[2]), fmaxf(m23[0], m23[2])); \
        Lo = fmaf(__builtin_amdgcn_exp2f(m01[0] - Mo), m01[1], __builtin_amdgcn_exp2f(m01[2] - Mo) * m01[3]) + fmaf(__builtin_amdgcn_exp2f(m23[0] - Mo), m23[1], __builtin_amdgcn_exp2f(m23[2] - Mo) * m23[3]); \
        st128_sc1(v4u{as_u(Mo), tag, as_u(Lo), tag}, rws, fresh_lane() == 0 ? 0 : OOB, mls + half * 128); sent = true; }
      STAGE_GU(if (!sent && __builtin_amdgcn_readfirstlane(LFLAG(F_KDONE)) >= (unsigned)NSW) { FENCE(); SEND_ML() })
      if (!sent) { LWAIT(F_KDONE, NSW, 8); SEND_ML() }
      {
        v4u g;
        for (unsigned spin = 0;; ++spin) {
          g = ld128_sc1(rws, 0, mls + (half ^ 1) * 128);
          if (__all(TAGBAD(g) == 0) || spin > SPIN_CAP) break;
          __builtin_amdgcn_s_sleep(16);
          FENCE();
        }
        const float Mg = fmaxf(Mo, as_f(g[0]));
        const float Lg = __builtin_amdgcn_exp2f(Mo - Mg) * Lo + __builtin_amdgcn_exp2f(as_f(g[0]) - Mg) * as_f(g[2]);
        *(f32x2*)(lds + L_ML + 32) = f32x2{Mg, 1.0f / Lg};
        LGKM0();
        LFLAG(F_NORM) = 1u;
      }
      LWAIT(F_LAST, 1, 4);
      if (is_red) {
        const int lane = fresh_lane();
        unsigned sel_lo = 0x00003f80u, sel_hi = 0x3f800000u;
        asm volatile("" : "+v"(sel_lo), "+v"(sel_hi));
#define SUM_PIECES(N, g) { \
          float u0 = 0.f, u1 = 0.f, u2 = 0.f, u3 = 0.f; \
          _Pragma("unroll") for (int k = 0; k < N; ++k) { u0 = dot2(g[k][0], sel_lo, u0); u1 = dot2(g[k][0], sel_hi, u1); u2 = dot2(g[k][2], sel_lo, u2); u3 = dot2(g[k][2], sel_hi, u3); } \
          s0 = rs32(u0, u2); s1 = rs32(u1, u3); \
          s0 = rs16(s0, s0); s1 = rs16(s1, s1); \
          s0 += dppf<ROR8>(s0); s1 += dppf<ROR8>(s1); }
        float s0, s1;
        const int ys = WS_Y + (rb * 20 + cb) * 8192;
        v4u y[8];
        for (unsigned spin = 0;; ++spin) {
#pragma unroll
          for (int k = 0; k < 8; ++k) y[k] = ld128_sc1(rws, lane * 16, ys + k * 1024);
          SB();
          unsigned bad = 0;
#pragma unroll
          for (int k = 0; k < 8; ++k) bad |= TAGBAD(y[k]);
          if (__all(bad == 0) || spin > SPIN_CAP) break;
          FENCE();
        }
        SUM_PIECES(8, y)
        const unsigned hp2 = pk_bf16(lo_f(xres) + rbf(s0), hi_f(xres) + rbf(s1));
        {
          auto hh = __builtin_amdgcn_permlane32_swap(hp2, hp2, false, false);
          st128_sc1(v4u{hh[0], tag, hh[1], tag}, rws, (lane >> 3) * 10240 + (lane & 7) * 16, WS_H + rb * 2560 + cb * 128);
        }
        v4u z[7];
        const int zs = WS_Z + (rb * 20 + cb) * (NFFN * 128);
        const bool has7 = lane < NFFN * 8 - 6 * 64;
        for (unsigned spin = 0;; ++spin) {
#pragma unroll
          for (int k = 0; k < 6; ++k) z[k] = ld128_sc1(rws, lane * 16, zs + k * 1024);
          z[6] = ld128_sc1(rws, has7 ? lane * 16 + 6144 : lane * 16, zs);
          SB();
          unsigned bad = 0;
#pragma unroll
          for (int k = 0; k < 7; ++k) bad |= TAGBAD(z[k]);
          if (__all(bad == 0) || spin > SPIN_CAP) break;
          FENCE();
        }
        z[6][0] = has7 ? z[6][0] : 0u; z[6][2] = has7 ? z[6][2] : 0u;
        SUM_PIECES(7, z)
        __builtin_amdgcn_raw_buffer_store_b32(pk_bf16(lo_f(hp2) + rbf(s0), hi_f(hp2) + rbf(s1)), mk_rsrc(out, NB * DM * 2),
                                              (lane & 24) ? OOB : (lane & 7) * 8 + (lane >> 5) * 4, rj * 64, AUX_SC1);
        if (rj == 0) __builtin_amdgcn_raw_buffer_store_b32(tag, rws, lane == 0 ? 0 : OOB, WS_EPOCH, AUX_SC1);
      } else if (is_ffn) {
        const int lane = fresh_lane();
        {
          const GAS unsigned char* gp = (const GAS unsigned char*)((lane & 4) ? wu : wg) + (size_t)((112 + (lane >> 3)) * (FF * 2) + fi * 64 + (lane & 3) * 16);
          v4u GL[10];
#pragma unroll
          for (int n = 0; n < 10; ++n) GL[n] = *(const GAS v4u*)(gp + (128 * (n >> 1) + 8 * (n & 1)) * (FF * 2));
          SB();
#pragma unroll
          for (int n = 0; n < 10; ++n) *(v4u*)(lds + L_BG + ((n >> 1) * 16 + 14 + (n & 1)) * 1024 + lane * 16) = GL[n];
          LADD(F_WGT);
        }
        const rsrc_t rd = mk_rsrc(wd, DM * FF * 2);
        v4u D[20];
#pragma unroll
        for (int h2 = 0; h2 < 2; ++h2) {
#pragma unroll
          for (int n = 0; n < 20; ++n) D[n] = __builtin_amdgcn_raw_buffer_load_b128(rd, lane * 16, fi * 32 * (DM * 2) + (h2 * 20 + n) * 1024, 0);
          SB();
          if (h2 == 0) LWAIT(F_ODONE, NSW, 1);
#pragma unroll
          for (int n = 0; n < 20; ++n) *(v4u*)(lds + L_F4 + (h2 * 20 + n) * 1024 + lane * 16) = D[n];
          SB();
        }
        BAR();
        BAR();
      }
      return;
    }

    FRESH_IDS();
    v4u BG[20], BD[12];
    unsigned char* gpw = lds + L_GP + (w * 4 + r) * GP_S + (c & 7) * 32 + (c >> 3) * 8;
    const unsigned char* gpr = lds + L_GP + (c >> 2) * GP_S + (4 * r + (c & 3)) * 16;
    LWAIT(F_WGT, NHW, 0);
#pragma unroll
    for (int s = 0; s < 5; ++s) {
      v2u t[8];
#pragma unroll
      for (int e = 0; e < 8; ++e) t[e] = *(const v2u*)(lds + L_BG + (160 * w + (s < 4 ? 32 * r + 4 * s + (e & 3) + 16 * (e >> 2) : 128 + 8 * r + e)) * 128 + c * 8);
      frag4(t, BG + 4 * s);
    }
    {
      const v4u ones = {0x3f803f80u, 0x3f803f80u, 0x3f803f80u, 0x3f803f80u};
      f32x4 cs[4] = {zero, zero, zero, zero};
#pragma unroll
      for (int s = 0; s < 5; ++s)
#pragma unroll
        for (int f = 0; f < 4; ++f) cs[f] = mfma(ones, BG[4 * s + f], cs[f]);
      *(f32x2*)gpw = f32x2{cs[0][0], cs[1][0]};
      *(f32x2*)(gpw + 16) = f32x2{cs[2][0], cs[3][0]};
    }
    BAR();
    f32x4 CS = (*(const f32x4*)gpr + *(const f32x4*)(gpr + 4 * GP_S)) + (*(const f32x4*)(gpr + 8 * GP_S) + *(const f32x4*)(gpr + 12 * GP_S));
#pragma unroll
    for (int s = 0; s < 3; ++s) {
      v2u t[8];
#pragma unroll
      for (int e = 0; e < 8; ++e) t[e] = *(const v2u*)(lds + L_F4 + (8 * r + e) * (DM * 2) + (s < 2 ? w + 4 * s : 8 + (w & 1)) * 128 + c * 8);
      frag4(t, BD + 4 * s);
    }
#pragma unroll
    for (int n = 0; n < 20; ++n) asm volatile("" : "+v"(BG[n]));
#pragma unroll
    for (int n = 0; n < 12; ++n) asm volatile("" : "+v"(BD[n]));
    asm volatile("" : "+v"(CS));
    BAR();
    v4u g0, g1, g2;
    {
      const int o0 = (c >> 2) * 2560 + r * 128 + (c & 3) * 16, o1 = (c >> 2) * 2560 + 512 + r * 32 + (c & 1) * 16, hs = WS_H + xs * 10240 + w * 640;
      for (unsigned spin = 0;; ++spin) {
        g0 = ld128_sc1(rws, o0, hs);
        g1 = ld128_sc1(rws, o0 + 64, hs);
        g2 = ld128_sc1(rws, o1, hs);
        SB();
        const unsigned bad = TAGBAD(g0) | TAGBAD(g1) | TAGBAD(g2);
        if (__all(bad == 0) || spin > SPIN_CAP) break;
        FENCE();
      }
    }
    const v4u a0 = {g0[0], g0[2], g1[0], g1[2]}, a1 = {dppu<0x00>(g2[0]), dppu<0x00>(g2[2]), dppu<0x55>(g2[0]), dppu<0x55>(g2[2])};
    f32x4 dd[4][4];
#pragma unroll
    for (int s = 0; s < 4; ++s)
#pragma unroll
      for (int f = 0; f < 4; ++f) dd[s][f] = mfma(a0, BG[4 * s + f], zero);
#pragma unroll
    for (int f = 0; f < 4; ++f) dd[0][f] = mfma(a1, BG[16 + f], dd[0][f]);
    {
      const unsigned ax = (c & 2) ? g2[2] : g2[0];
      float s1 = dot2(ax, 0x3f803f80u, 0.f), s2 = dot2(ax, ax, 0.f);
#pragma unroll
      for (int d = 0; d < 4; ++d) { s1 = dot2(a0[d], 0x3f803f80u, s1); s2 = dot2(a0[d], a0[d], s2); }
      float ss = rs32(s1, s2);
      ss = rs16(ss, ss);
      ss += dppf<XOR1>(ss); ss += dppf<XOR2>(ss);
      *(float*)(lds + L_ST + (c >> 2) * 32 + (lane >> 5) * 16 + w * 4) = ss;
    }
    float pv[4];
#pragma unroll
    for (int f = 0; f < 4; ++f) pv[f] = (dd[0][f][0] + dd[1][f][1]) + (dd[2][f][2] + dd[3][f][3]);
    *(f32x2*)gpw = f32x2{pv[0], pv[1]};
    *(f32x2*)(gpw + 16) = f32x2{pv[2], pv[3]};
    BAR();
    const f32x4 t1 = *(const f32x4*)(lds + L_ST + (c >> 2) * 32), t2 = *(const f32x4*)(lds + L_ST + (c >> 2) * 32 + 16);
    f32x4 gu[4];
#pragma unroll
    for (int ww = 0; ww < 4; ++ww) gu[ww] = *(const f32x4*)(gpr + ww * 4 * GP_S);
    SB();
    const float e1 = ((t1[0] + t1[1]) + (t1[2] + t1[3])) * (1.0f / DM), e2 = ((t2[0] + t2[1]) + (t2[2] + t2[3])) * (1.0f / DM);
    const float mb = rbf(e1), rden = ln_rden(fmaxf(fmaf(-e1, e1, e2), 0.f));
    const f32x4 GU = (((gu[0] + gu[1]) + (gu[2] + gu[3])) - mb * CS) * rden;
    float av[2];
#pragma unroll
    for (int j = 0; j < 2; ++j) {
      const float g = rbf(GU[j]), u = rbf(GU[2 + j]);
      const float t2x = 2.0f * 0.7978845608028654f * LOG2E * (g + 0.044715f * g * g * g);
      av[j] = rbf(g * __builtin_amdgcn_rcpf(1.0f + __builtin_amdgcn_exp2f(-t2x))) * u;
    }
    const unsigned ap = pk_bf16(av[0], av[1]);
    const v4u af = {dppu<0x00>(ap), dppu<0x55>(ap), dppu<0xAA>(ap), dppu<0xFF>(ap)};
    f32x4 z[12];
#pragma unroll
    for (int f = 0; f < 12; ++f) z[f] = mfma(af, BD[f], zero);
#pragma unroll
    for (int s = 0; s < 3; ++s)
      st128_sc1(v4u{pk_bf16(z[4 * s][0], z[4 * s + 1][0]), tag, pk_bf16(z[4 * s + 2][0], z[4 * s + 3][0]), tag}, rws,
                (r * 20 + (c >> 3)) * (NFFN * 128) + (c & 7) * 16, (s < 2 || w < 2) ? WS_Z + (2 * (w + 4 * s) * NFFN + fi) * 128 : OOB);
    return;
  }

  const int w = wv - NHW;
  const unsigned tstart = (unsigned)__builtin_amdgcn_s_memrealtime();
  const int lane = threadIdx.x & 63, r = lane >> 4, c = lane & 15;
  const rsrc_t rk = mk_rsrc(kc, NB * NH * SK * DH * 2), rv = mk_rsrc(vc, NB * NH * SK * DH * 2);
  const int voff = lane * 16;
  if (is_q) { __builtin_amdgcn_s_barrier(); LWAIT(F_WGT, NHW, 1); }
  v4u B[NPRE][8];
  int so = kv0;
#pragma unroll
  for (int t = 0; t < NPRE; ++t, so = NEXT(so))
#pragma unroll
    for (int i = 0; i < 8; ++i) {
      B[t][i] = LD(rk, i, so);
      if (t == 0 && i == 7) { SB(); if (!is_q) __builtin_amdgcn_s_barrier(); SB(); }
      if (8 * t + i >= 7 && 8 * t + i + 1 < 8 * NPRE) VMWAIT(7);
    }
  SB();
  *(f32x2*)(lds + L_SC + w * SC_S + 14 * 128 + lane * 8) = f32x2{-INFINITY, -INFINITY};
  LFLAG(F_AUX + w) = 1u;
  LWAIT(F_QRDY, 1, 1);
  const unsigned tag = __builtin_amdgcn_readfirstlane(LFLAG(F_TAG));
  const v4u q = *(const v4u*)(lds + L_Q + c * 16);

  const int thr0 = __builtin_amdgcn_readfirstlane((half && pair_ffn) ? 0 : is_q ? 1 : 2);
  int thr = thr0;
#define PACE(rem) { \
    const unsigned fw_ = ((unsigned)__builtin_amdgcn_s_memrealtime() + (unsigned)(rem) * TAU) | 1u; \
    const unsigned ot_ = LWORD(L_PACE + (lane & 3) * 4); \
    LWORD(L_PACE + w * 4) = fw_; \
    int dl_ = ot_ == 0u ? 0 : (int)(fw_ - ot_); \
    dl_ += (int)dppu<XOR1>((unsigned)dl_); dl_ += (int)dppu<XOR2>((unsigned)dl_); \
    dl_ = __builtin_amdgcn_readfirstlane(dl_); \
    thr = dl_ > PLEAD ? (thr0 > 0 ? thr0 - 1 : 0) : dl_ < -PLEAD ? thr0 + 1 : thr0; \
    thr = __builtin_amdgcn_readfirstlane(thr + ((int)(fw_ - tstart) < SCHED1 ? 1 : 0) + ((int)(fw_ - tstart) < SCHED2 ? 1 : 0)); SB(); }

  unsigned char* sc = lds + L_SC + w * SC_S + r * 32 + (c >> 1) * 4;
  for (int t = 0; t < naux; ++t) so = NEXT(so);
  KSTEP(0, 1, rk, so)
  KSTEP(1, 0, rk, 0)
  KSTEP(2, 0, rk, 0)
  KSTEP(3, 0, rk, 0)
  sc += naux * 128;
  for (int it = NPRE + naux; it < ns - 1; ++it) { so = NEXT(so); KSTEP(0, 2, rk, so) PACE(2 * ns - 1 - it) }
  so = kv0;
  KSTEP(0, 2, rv, so)
  float mw;
  {
    if (naux) LWAIT(F_AUX + w, 2, 1);
    FENCE();
    const unsigned char* sp = lds + L_SC + w * SC_S;
    const f32x4 s0 = *(const f32x4*)(sp + lane * 32), s1 = *(const f32x4*)(sp + lane * 32 + 16);
    const f32x4 s2 = *(const f32x4*)(sp + 2048 + (lane & 7) * 32), s3 = *(const f32x4*)(sp + 2048 + (lane & 7) * 32 + 16);
    float mx = -INFINITY;
#pragma unroll
    for (int j = 0; j < 4; ++j) mx = fmaxf(fmaxf(mx, fmaxf(s0[j], s1[j])), fmaxf(s2[j], s3[j]));
    mx = fmaxf(mx, dppf<ROR8>(mx)); mx = fmaxf(mx, dppf<ROR4>(mx)); mx = fmaxf(mx, dppf<ROR2>(mx)); mx = fmaxf(mx, dppf<ROR1>(mx));
    mw = xrow_max(mx) * LOG2E;
    float p[8], e[8];
#pragma unroll
    for (int j = 0; j < 4; ++j) {
      p[j] = __builtin_amdgcn_exp2f(fmaf(s0[j], LOG2E, -mw)); p[4 + j] = __builtin_amdgcn_exp2f(fmaf(s1[j], LOG2E, -mw));
      e[j] = __builtin_amdgcn_exp2f(fmaf(s2[j], LOG2E, -mw)); e[4 + j] = __builtin_amdgcn_exp2f(fmaf(s3[j], LOG2E, -mw));
    }
    FENCE();
    *(v4u*)(lds + L_SC + w * SC_S + lane * 16) = v4u{pk_bf16(p[0], p[1]), pk_bf16(p[2], p[3]), pk_bf16(p[4], p[5]), pk_bf16(p[6], p[7])};
    *(v4u*)(lds + L_SC + w * SC_S + 1024 + (lane & 7) * 16) = v4u{pk_bf16(e[0], e[1]), pk_bf16(e[2], e[3]), pk_bf16(e[4], e[5]), pk_bf16(e[6], e[7])};
    const float le = ((e[0] + e[1]) + (e[2] + e[3])) + ((e[4] + e[5]) + (e[6] + e[7]));
    float l = ((p[0] + p[1]) + (p[2] + p[3])) + ((p[4] + p[5]) + (p[6] + p[7])) + (lane < 8 ? le : 0.f);
    l += dppf<ROR8>(l); l += dppf<ROR4>(l); l += dppf<ROR2>(l); l += dppf<ROR1>(l);
    l = xrow_sum(l);
    if (lane == 0) *(f32x2*)(lds + L_ML + w * 8) = f32x2{mw, l};
    LADD(F_KDONE);
    SB();
  }
  float acc[8];
#pragma unroll
  for (int e = 0; e < 8; ++e) acc[e] = 0.f;
  const unsigned char* pp = lds + L_SC + w * SC_S + r * 16;
#define VSTEP(MODE, NXT) { \
    const v4u pw = *(const v4u*)pp; pp += 64; \
    _Pragma("unroll") for (int j = 0; j < 8; ++j) { \
      const unsigned u = pw[j >> 1], pl = (j & 1) ? u >> 16 : u & 0xffffu, ph = (j & 1) ? u & 0xffff0000u : u << 16; \
      _Pragma("unroll") for (int d = 0; d < 4; ++d) { acc[2 * d] = dot2(B[0][j][d], pl, acc[2 * d]); acc[2 * d + 1] = dot2(B[0][j][d], ph, acc[2 * d + 1]); } \
      asm volatile("" : "+v"(acc[0]), "+v"(acc[1]), "+v"(acc[2]), "+v"(acc[3]), "+v"(acc[4]), "+v"(acc[5]), "+v"(acc[6]), "+v"(acc[7])); SB(); \
      REFILL(j, MODE, rv, NXT) } }
  for (int it = 0; it < ns - 1; ++it) {
    so = NEXT(so);
    thr = __builtin_amdgcn_readfirstlane(it + BOOST >= ns ? (thr > thr0 ? 1 : 0) : thr);
    VSTEP(2, so)
    PACE(ns - 1 - it)
  }
  if (w == 0) LFLAG(F_LAST) = 1u;
  LWAIT(F_FRAG, 1, 1);
  LWAIT(F_NORM, 1, 1);
#pragma unroll
  for (int n = 0; n < 8; ++n) B[1][n] = *(const v4u*)(lds + L_F4 + (w * 8 + n) * 1024 + lane * 16);
  const f32x2 gl = *(const f32x2*)(lds + L_ML + 32);
  float f = __builtin_amdgcn_exp2f(mw - gl.x) * gl.y;
  asm volatile("" : "+v"(f));
  SB();
  VSTEP(0, 0)
  {
    float t[4];
#pragma unroll
    for (int j = 0; j < 4; ++j) t[j] = rs32(acc[j], acc[j + 4]);
    *(unsigned*)(lds + L_OP + w * OP_S + (4 * c + r) * 4) = pk_bf16(rs16(t[0], t[2]) * f, rs16(t[1], t[3]) * f);
  }
  LADD(F_ODONE);
  LWAIT(F_ODONE, NSW, 0);
  {
    v4u a[4];
#pragma unroll
    for (int s = 0; s < 4; ++s) a[s] = *(const v4u*)(lds + L_OP + (c >> 2) * OP_S + s * 64 + r * 16);
    f32x4 d0 = {0.f, 0.f, 0.f, 0.f}, d1 = {0.f, 0.f, 0.f, 0.f};
#pragma unroll
    for (int s = 0; s < 4; ++s) { d0 = mfma(a[s], B[1][2 * s], d0); d1 = mfma(a[s], B[1][2 * s + 1], d1); }
    const unsigned yp = pk_bf16(xrow_sum(d0[0]), xrow_sum(d1[0]));
    st128_sc1(v4u{yp, tag, dppu<XOR1>(yp), tag}, mk_rsrc(ws, WS_END), (lane & 49) ? OOB : c * 8, WS_Y + ((b * 20 + 16 + w) * 64 + head * 2 + half) * 128);
  }
}

extern "C" void kernel_launch(void* const* d_in, const int* in_sizes, int n_in, void* d_out, int out_size, void* d_ws, size_t ws_size, hipStream_t stream) {
  (void)in_sizes; (void)n_in; (void)out_size; (void)ws_size;
  fused_block<<<NWG, NT, 0, stream>>>((const u16*)d_in[1], (const u16*)d_in[2], d_ws, (const u16*)d_in[0], (const u16*)d_in[3], (const u16*)d_in[4],
                                      (const u16*)d_in[6], (const u16*)d_in[7], (const u16*)d_in[8], (u16*)d_out);
}
```

```cpp
#include <hip/hip_runtime.h>
#include <stdint.h>

typedef unsigned short u16;
typedef unsigned v4u __attribute__((ext_vector_type(4)));
typedef unsigned v2u __attribute__((ext_vector_type(2)));
typedef float f32x2 __attribute__((ext_vector_type(2)));
typedef float f32x4 __attribute__((ext_vector_type(4)));
typedef __bf16 bf16x2 __attribute__((ext_vector_type(2)));
typedef __bf16 bf16x8 __attribute__((ext_vector_type(8)));
typedef __amdgpu_buffer_rsrc_t rsrc_t;
#define GAS __attribute__((address_space(1)))
#define LAS __attribute__((address_space(3)))

constexpr int NB = 4, DM = 640, NH = 32, SK = 4096, DH = 128, FF = 1728;
constexpr int NSW = 4, NHW = 5, NT = 64 * (NSW + NHW), NWG = 256;
constexpr int NRED = 80, NFFN = 54;
constexpr int NPRE = 4, NAUX = 6;
constexpr int STEP = 2 * NSW * 4096;
constexpr unsigned TAU = 113;
constexpr int PLEAD = 48, SCHED0 = 4040, SCHED1 = 3960, SCHED2 = 3905, BOOST = 3;

constexpr int WS_EPOCH = 0;
constexpr int WS_ML = 32768;
constexpr int WS_Q = 65536;
constexpr int WS_Y = WS_Q + NB * NH * 512;
constexpr int WS_Z = WS_Y + NB * 20 * 64 * 128;
constexpr int WS_H = WS_Z + NB * 20 * NFFN * 128;
constexpr int WS_END = WS_H + 8 * 10240;
constexpr int AUX_NT = 2, AUX_SC1 = 16;
constexpr int OOB = 0x40000000;
constexpr unsigned SPIN_CAP = 1u << 18;
constexpr float LOG2E = 1.4426950408889634f;

enum { F_QPART = 0, F_QRDY = 1, F_FRAG = 3, F_LAST = 4, F_TAG = 5, F_KDONE = 6, F_NORM = 7, F_STAT = 8, F_WGT = 9, F_AUX = 10 };
constexpr int L_FLAG = 0;
constexpr int L_ML = 64;
constexpr int L_Q = 128;
constexpr int L_ST = 384;
constexpr int L_PACE = 512;
constexpr int L_DONE = L_PACE + 32;
constexpr int OP_S = 320;
constexpr int L_OP = 576;
constexpr int GP_S = 384;
constexpr int L_GP = L_OP + 4 * OP_S;
constexpr int L_BG = L_GP + 5 * 4 * GP_S;
constexpr int L_F4 = L_BG + 640 * 128;
constexpr int L_SC = L_F4 + 32 * 1024;
constexpr int SC_S = 2304;
constexpr int L_END = L_SC + 4 * SC_S;
static_assert(L_BG % 16 == 0 && L_OP % 16 == 0 && L_GP % 16 == 0 && L_END - L_F4 >= 32 * DM * 2 && L_END <= 160 * 1024, "LDS map");

__device__ __forceinline__ rsrc_t mk_rsrc(const void* p, int bytes) { return __builtin_amdgcn_make_buffer_rsrc((void*)p, 0, bytes, 0x00020000); }
__device__ __forceinline__ float as_f(unsigned u) { return __builtin_bit_cast(float, u); }
__device__ __forceinline__ unsigned as_u(float f) { return __builtin_bit_cast(unsigned, f); }
__device__ __forceinline__ unsigned pk_bf16(float lo, float hi) { f32x2 v = {lo, hi}; return __builtin_bit_cast(unsigned, __builtin_convertvector(v, bf16x2)); }
__device__ __forceinline__ float rbf(float x) { return as_f(pk_bf16(0.f, x)); }
__device__ __forceinline__ float lo_f(unsigned u) { return as_f(u << 16); }
__device__ __forceinline__ float hi_f(unsigned u) { return as_f(u & 0xffff0000u); }
__device__ __forceinline__ float dot2(unsigned a, unsigned b, float c) { return __builtin_amdgcn_fdot2_f32_bf16(__builtin_bit_cast(bf16x2, a), __builtin_bit_cast(bf16x2, b), c, false); }
__device__ __forceinline__ f32x4 mfma(v4u a, v4u b, f32x4 c) { return __builtin_amdgcn_mfma_f32_16x16x32_bf16(__builtin_bit_cast(bf16x8, a), __builtin_bit_cast(bf16x8, b), c, 0, 0, 0); }
template <int CTRL> __device__ __forceinline__ unsigned dppu(unsigned x) { return __builtin_amdgcn_mov_dpp((int)x, CTRL, 0xf, 0xf, true); }
template <int CTRL> __device__ __forceinline__ float dppf(float x) { return as_f(dppu<CTRL>(as_u(x))); }
constexpr int ROR1 = 0x121, ROR2 = 0x122, ROR4 = 0x124, ROR8 = 0x128, XOR1 = 0xB1, XOR2 = 0x4E, XOR7 = 0x141;
__device__ __forceinline__ unsigned perm_lo(unsigned a, unsigned b) { return __builtin_amdgcn_perm(b, a, 0x05040100u); }
__device__ __forceinline__ unsigned perm_hi(unsigned a, unsigned b) { return __builtin_amdgcn_perm(b, a, 0x07060302u); }
__device__ __forceinline__ float rs16(float a, float b) { auto s = __builtin_amdgcn_permlane16_swap(as_u(a), as_u(b), false, false); return as_f(s[0]) + as_f(s[1]); }
__device__ __forceinline__ float rs32(float a, float b) { auto s = __builtin_amdgcn_permlane32_swap(as_u(a), as_u(b), false, false); return as_f(s[0]) + as_f(s[1]); }
__device__ __forceinline__ float xrow_sum(float x) { x = rs16(x, x); return rs32(x, x); }
__device__ __forceinline__ float xrow_max(float x) {
  auto s = __builtin_amdgcn_permlane16_swap(as_u(x), as_u(x), false, false); x = fmaxf(as_f(s[0]), as_f(s[1]));
  auto t = __builtin_amdgcn_permlane32_swap(as_u(x), as_u(x), false, false); return fmaxf(as_f(t[0]), as_f(t[1]));
}
__device__ __forceinline__ void st128_sc1(v4u v, rsrc_t rs, int voff, int soff) {
  __builtin_amdgcn_raw_buffer_store_b128(v, rs, voff, soff, AUX_SC1);
  asm volatile("s_nop 1" :: "v"(v) : "memory");
}
__device__ __forceinline__ v4u ld128_sc1(rsrc_t rs, int voff, int soff) { return __builtin_amdgcn_raw_buffer_load_b128(rs, voff, soff, AUX_SC1); }
__device__ __forceinline__ unsigned tagacc(unsigned acc, unsigned x, unsigned t) { return __builtin_amdgcn_bitop3_b32(acc, x, t, 0xF6); }
__device__ __forceinline__ int fresh_lane() { int l; asm volatile("v_mbcnt_lo_u32_b32 %0, -1, 0\n\tv_mbcnt_hi_u32_b32 %0, -1, %0" : "=v"(l)); return l; }
__device__ __forceinline__ unsigned ln_pair(unsigned v, float mb, float rden) { return pk_bf16(rbf(lo_f(v) - mb) * rden, rbf(hi_f(v) - mb) * rden); }
__device__ __forceinline__ float ln_rden(float var) { return __builtin_amdgcn_rcpf(rbf(__builtin_amdgcn_sqrtf(rbf(rbf(var) + 1e-5f)))); }
__device__ __forceinline__ f32x2 ln_stats(const unsigned (&hv)[5]) {
  const float piv = as_f(__builtin_amdgcn_readfirstlane(hv[0] << 16));
  float s1 = 0.f, s2 = 0.f;
#pragma unroll
  for (int j = 0; j < 5; ++j) { const float d0 = lo_f(hv[j]) - piv, d1 = hi_f(hv[j]) - piv; s1 += d0 + d1; s2 = fmaf(d0, d0, s2); s2 = fmaf(d1, d1, s2); }
  float ss = rs32(s1, s2);
  ss = rs16(ss, ss);
  ss += dppf<ROR8>(ss); ss += dppf<ROR4>(ss); ss += dppf<ROR2>(ss); ss += dppf<ROR1>(ss);
  const float e1 = as_f(__builtin_amdgcn_readlane(as_u(ss), 0)) * (1.0f / DM), e2 = as_f(__builtin_amdgcn_readlane(as_u(ss), 32)) * (1.0f / DM);
  return f32x2{rbf(piv + e1), ln_rden(fmaxf(fmaf(-e1, e1, e2), 0.f))};
}
__device__ __forceinline__ void frag4(const v2u* t, v4u* f) {
#pragma unroll
  for (int d = 0; d < 4; ++d) {
    f[0][d] = perm_lo(t[2 * d][0], t[2 * d + 1][0]); f[1][d] = perm_hi(t[2 * d][0], t[2 * d + 1][0]);
    f[2][d] = perm_lo(t[2 * d][1], t[2 * d + 1][1]); f[3][d] = perm_hi(t[2 * d][1], t[2 * d + 1][1]);
  }
}
#define SB() __builtin_amdgcn_sched_barrier(0)
#define FENCE() asm volatile("" ::: "memory")
#define LGKM0() asm volatile("s_waitcnt lgkmcnt(0)" ::: "memory")
#define VMWAIT(n) do { SB(); asm volatile("s_waitcnt vmcnt(%0)" :: "i"(n) : "memory"); SB(); } while (0)
#define BAR() do { LGKM0(); __builtin_amdgcn_s_barrier(); FENCE(); } while (0)
#define TAGBAD(g) (((g)[1] ^ tag) | ((g)[3] ^ tag))
#define FRESH_IDS() const int lane = fresh_lane(); const int r = lane >> 4, c = lane & 15; (void)r; (void)c
#define LWORD(off) (*(volatile LAS unsigned*)(LAS unsigned char*)(lds + (off)))
#define LFLAG(i) LWORD(L_FLAG + 4 * (i))
#define LWAIT(i, n, SLP) do { unsigned spin_ = 0; _Pragma("nounroll") while (LFLAG(i) < (unsigned)(n) && ++spin_ < (SPIN_CAP << 4)) { if (SLP) __builtin_amdgcn_s_sleep(SLP); } FENCE(); } while (0)
#define LADD(i) do { LGKM0(); if (fresh_lane() == 0) __hip_atomic_fetch_add((LAS unsigned*)(LAS unsigned char*)(lds + L_FLAG + 4 * (i)), 1u, __ATOMIC_RELAXED, __HIP_MEMORY_SCOPE_WORKGROUP); FENCE(); } while (0)

#define WAIT_O(a) do { \
    const int ao_ = L_OP + (c >> 2) * OP_S + r * 16; \
    _Pragma("nounroll") for (unsigned spin_ = 0; spin_ < (SPIN_CAP << 4); ++spin_) { \
      const v4u fl_ = *(volatile LAS v4u*)(LAS unsigned char*)(lds + L_DONE); \
      _Pragma("unroll") for (int s_ = 0; s_ < 4; ++s_) a[s_] = *(volatile LAS v4u*)(LAS unsigned char*)(lds + ao_ + s_ * 64); \
      if (__builtin_amdgcn_readfirstlane(fl_[0] & fl_[1] & fl_[2] & fl_[3]) != 0u) break; } } while (0)

__device__ __forceinline__ int ffn_of_rank(int j) { const int s = j - 64; if (s < 0) return -1; const int k = (s & 7) + 8 * (s >> 4); return k < 27 ? 2 * k + ((s >> 3) & 1) : -1; }

__global__ void __launch_bounds__(NT, 3) fused_block(const u16* __restrict__ kc, const u16* __restrict__ vc, void* ws, const u16* __restrict__ x,
                                                     const u16* __restrict__ wq, const u16* __restrict__ wo, const u16* __restrict__ wg,
                                                     const u16* __restrict__ wu, const u16* __restrict__ wd, u16* __restrict__ out) {
  __shared__ __attribute__((aligned(16))) unsigned char lds[L_END];
  const int wv = __builtin_amdgcn_readfirstlane(threadIdx.x >> 6);
  const int bid = blockIdx.x, xs = bid & 7, rr = bid >> 3;
  const int head = xs + 8 * (rr >> 3), b = (rr >> 1) & 3, half = rr & 1;
  const int rank = xs + 8 * (bid >> 4);
  const bool pair_ffn = ffn_of_rank(rank) >= 0, is_q = !half && rank < 64;

  const int sw = wv >= NHW ? wv - NHW : wv;
  const int mv = pair_ffn ? 1 : rank < 64 ? -1 : 0;
  const int ns = half ? 16 - mv : 16 + mv;
  const int kvbase = (b * NH + head) * (SK * DH * 2) + (half ? (16 + mv) * STEP : 0) + sw * 4096;
  const int naux = (is_q || (half && pair_ffn)) ? 0 : NAUX;
  const int kvend = kvbase + ns * STEP, kv0 = kvbase + (4 * (head & 1) + 8 * ((head >> 3) & 1)) * STEP;
#define NEXT(so) ((so) + STEP == kvend ? kvbase : (so) + STEP)
#define LD(RS, i, so) __builtin_amdgcn_raw_buffer_load_b128(RS, voff, (so) + ((i) >> 2) * (NSW * 4096) + ((i) & 3) * 1024, AUX_NT)
#define THROTTLE() asm volatile("s_cmp_lt_u32 %0, 1\n\ts_cbranch_scc1 1f\n\ts_waitcnt vmcnt(6)\n\ts_cmp_lt_u32 %0, 2\n\ts_cbranch_scc1 1f\n\ts_waitcnt vmcnt(5)\n\t" \
                                "s_cmp_lt_u32 %0, 3\n\ts_cbranch_scc1 1f\n\ts_waitcnt vmcnt(4)\n\ts_cmp_lt_u32 %0, 4\n\ts_cbranch_scc1 1f\n\ts_waitcnt vmcnt(3)\n1:" :: "s"(thr) : "scc", "memory")
#define REFILL(i, MODE, RS, NXT) if (MODE) { if (MODE == 2) THROTTLE(); B[0][i] = LD(RS, i, NXT); SB(); }
#define KSTEP(T, MODE, RS, NXT) { \
    float sx[8]; \
    _Pragma("unroll") for (int j = 0; j < 8; ++j) { \
      float d = dot2(B[T][j][0], q[0], 0.f); d = dot2(B[T][j][1], q[1], d); d = dot2(B[T][j][2], q[2], d); d = dot2(B[T][j][3], q[3], d); \
      asm volatile("" : "+v"(d)); sx[j] = d; SB(); REFILL(j, MODE, RS, NXT) } \
    float t4[4], t2[2]; \
    _Pragma("unroll") for (int j = 0; j < 4; ++j) { const float lo = sx[j] + dppf<ROR8>(sx[j]), hi = sx[j + 4] + dppf<ROR8>(sx[j + 4]); t4[j] = (c & 8) ? hi : lo; } \
    _Pragma("unroll") for (int j = 0; j < 2; ++j) { const float lo = t4[j] + dppf<XOR7>(t4[j]), hi = t4[j + 2] + dppf<XOR7>(t4[j + 2]); t2[j] = (c & 4) ? hi : lo; } \
    const float lo = t2[0] + dppf<XOR2>(t2[0]), hi = t2[1] + dppf<XOR2>(t2[1]); \
    float s = (c & 2) ? hi : lo; \
    s += dppf<XOR1>(s); \
    *(float*)sc = rbf(rbf(s) * 0.08838834764831845f); sc += 128; SB(); }

  if (wv < NHW) {
    const int w = wv;
    const int rj = half ? (rank < 64 ? rank : 255) : (rank >= 64 && rank < NRED ? rank : 255);
    __builtin_amdgcn_s_setprio(2);
    if (threadIdx.x < 16) { *(unsigned*)(lds + L_FLAG + threadIdx.x * 4) = 0u; *(unsigned*)(lds + L_PACE + threadIdx.x * 4) = 0u; }
    const rsrc_t rws = mk_rsrc(ws, WS_END);
    const f32x4 zero = {0.f, 0.f, 0.f, 0.f};
    unsigned tag, xres;
    {
      FRESH_IDS();
      const rsrc_t rx = mk_rsrc(x, NB * DM * 2);
      if (is_q) {
        const rsrc_t rwq = mk_rsrc(wq, DM * NH * DH * 2);
        const v4u x8 = __builtin_amdgcn_raw_buffer_load_b128(rx, lane * 16, w * (DM * 2), 0);
        const unsigned x2 = __builtin_amdgcn_raw_buffer_load_b32(rx, lane * 4 + 1024, w * (DM * 2), 0);
        v4u xa[4];
#pragma unroll
        for (int s = 0; s < 4; ++s) xa[s] = __builtin_amdgcn_raw_buffer_load_b128(rx, (c >> 2) * (DM * 2) + r * 16, (128 * w + 32 * s) * 2, 0);
        const unsigned epoch_v = __builtin_amdgcn_raw_buffer_load_b32(rws, 0, WS_EPOCH, AUX_SC1);
        xres = 0u;
        SB();
        v2u Wq[32];
#define WQLD(n) Wq[n] = __builtin_amdgcn_raw_buffer_load_b64(rwq, (8 * r) * 8192 + c * 8, (128 * w + 32 * ((n) >> 3) + ((n) & 7)) * 8192 + rank * 128, AUX_NT)
#pragma unroll
        for (int n = 0; n < 12; ++n) WQLD(n);
        SB();
        BAR();
        if (w < 4) {
          const unsigned hv[5] = {x8[0], x8[1], x8[2], x8[3], x2};
          *(f32x2*)(lds + L_ST + w * 8) = ln_stats(hv);
          LADD(F_STAT);
        }
        SB();
#pragma unroll
        for (int n = 12; n < 20; ++n) WQLD(n);
        SB();
        LWAIT(F_STAT, 4, 0);
        const f32x2 st = *(const f32x2*)(lds + L_ST + (c >> 2) * 8);
#pragma unroll
        for (int s = 0; s < 4; ++s) {
          xa[s] = v4u{ln_pair(xa[s][0], st.x, st.y), ln_pair(xa[s][1], st.x, st.y), ln_pair(xa[s][2], st.x, st.y), ln_pair(xa[s][3], st.x, st.y)};
          asm volatile("" : "+v"(xa[s]));
          SB();
#pragma unroll
          for (int n = 20 + 3 * s; n < 23 + 3 * s; ++n) WQLD(n);
          SB();
        }
        LADD(F_WGT);
        f32x4 dq[4] = {zero, zero, zero, zero};
#pragma unroll
        for (int s = 0; s < 4; ++s) {
          v4u bf[4];
          frag4(Wq + 8 * s, bf);
#pragma unroll
          for (int f = 0; f < 4; ++f) dq[f] = mfma(xa[s], bf[f], dq[f]);
          SB();
        }
        *(f32x4*)(lds + L_GP + w * 1024 + lane * 16) = f32x4{dq[0][0], dq[1][0], dq[2][0], dq[3][0]};
        tag = __builtin_amdgcn_readfirstlane(epoch_v) + 1u;
        LADD(F_QPART);
        if (w == 1) {
          LWAIT(F_QPART, NHW, 0);
          f32x4 pq[NHW];
#pragma unroll
          for (int ww = 0; ww < NHW; ++ww) pq[ww] = *(const f32x4*)(lds + L_GP + ww * 1024 + lane * 16);
          const f32x4 sq = ((pq[0] + pq[1]) + (pq[2] + pq[3])) + pq[4];
          st128_sc1(v4u{pk_bf16(sq[0], sq[1]), tag, pk_bf16(sq[2], sq[3]), tag}, rws, r * (NH * 512) + c * 16, WS_Q + rank * 256);
        }
      } else {
        const unsigned epoch_v = __builtin_amdgcn_raw_buffer_load_b32(rws, 0, WS_EPOCH, AUX_SC1);
        xres = __builtin_amdgcn_raw_buffer_load_b32(rx, (lane & 7) * 8 + (lane >> 5) * 4, rj * 64, 0);
        SB();
        BAR();
        tag = __builtin_amdgcn_readfirstlane(epoch_v) + 1u;
      }
      if (w == 4) {
        v4u g;
        for (unsigned spin = 0;; ++spin) {
          g = ld128_sc1(rws, (lane & 31) * 16, WS_Q + (b * NH + head) * 512);
          if (__all(TAGBAD(g) == 0) || spin > SPIN_CAP) break;
          __builtin_amdgcn_s_sleep(16);
          FENCE();
        }
        *(v2u*)(lds + L_Q + (lane & 31) * 8) = v2u{g[0], g[2]};
        LFLAG(F_TAG) = tag;
        LGKM0();
        LFLAG(F_QRDY) = 1u;
      } else if (naux) {
        const rsrc_t rk = mk_rsrc(kc, NB * NH * SK * DH * 2);
        const int voff = lane * 16, thr = 0;
        v4u B[4][8];
        unsigned char* park = lds + L_BG + w * 16384 + lane * 16;
        LWAIT(F_AUX + w, 1, 1);
        int so = kv0;
#pragma unroll
        for (int t = 0; t < NPRE; ++t) so = NEXT(so);
#pragma unroll
        for (int t = 0; t < NAUX; ++t, so = NEXT(so))
#pragma unroll
          for (int i = 0; i < 8; ++i) {
            if (t >= 1 && t - 1 < NAUX - 4) { *(v4u*)(park + ((t - 1) * 8 + i) * 1024) = B[(t - 1) & 3][i]; SB(); }
            B[t & 3][i] = LD(rk, i, so);
            if (8 * t + i >= 7 && 8 * t + i + 1 < 8 * NAUX) VMWAIT(7);
          }
        SB();
        LWAIT(F_QRDY, 1, 1);
        const v4u q = *(const v4u*)(lds + L_Q + c * 16);
        unsigned char* sc = lds + L_SC + w * SC_S + (NPRE + NAUX - 4) * 128 + r * 32 + (c >> 1) * 4;
#pragma unroll
        for (int t = NAUX - 4; t < NAUX; ++t) KSTEP(t & 3, 0, rk, 0)
        sc -= NAUX * 128;
#pragma unroll
        for (int t = 0; t < NAUX - 4; ++t) {
#pragma unroll
          for (int i = 0; i < 8; ++i) B[0][i] = *(const v4u*)(park + (t * 8 + i) * 1024);
          KSTEP(0, 0, rk, 0)
        }
        LGKM0();
        LFLAG(F_AUX + w) = 2u;
      }
    }

    const bool is_red = rj < NRED;
    const int rb = rj / 20, cb = rj % 20;
    const int fi = half ? ffn_of_rank(rank) : -1;
    const bool is_ffn = fi >= 0;

#define STAGE_GU(HOOK) if (is_ffn) { \
      const int ln = fresh_lane(); \
      const GAS unsigned char* gp = (const GAS unsigned char*)((ln & 4) ? wu : wg) + (size_t)((128 * w + (ln >> 3)) * (FF * 2) + fi * 64 + (ln & 3) * 16); \
      _Pragma("nounroll") for (int s = 0; s < 7; ++s) { \
        HOOK \
        const v4u g0 = *(const GAS v4u*)gp, g1 = *(const GAS v4u*)(gp + 8 * (FF * 2)); \
        gp += 16 * (FF * 2); \
        *(v4u*)(lds + L_BG + (w * 16 + 2 * s) * 1024 + ln * 16) = g0; \
        *(v4u*)(lds + L_BG + (w * 16 + 2 * s + 1) * 1024 + ln * 16) = g1; \
      } }
#define WO_STEP(s, OUT) { \
      v4u T[8]; \
      _Pragma("unroll") for (int e = 0; e < 8; ++e) { \
        T[e] = __builtin_amdgcn_raw_buffer_load_b128(rwo, vo, so + (32 * (s) + 4 * e) * (DM * 2), 0); \
        if (e == 7) VMWAIT(0); \
        else if (e & 1) { SB(); asm volatile("s_cmp_lt_i32 %0, 0\n\ts_cbranch_scc0 1f\n\ts_waitcnt vmcnt(0)\n1:" :: "s"(fi) : "scc", "memory"); SB(); } } \
      _Pragma("unroll") for (int e = 0; e < 8; ++e) *(v4u*)(scr + e * 1024 + lane * 16) = T[e]; \
      FENCE(); \
      _Pragma("unroll") for (int t = 0; t < 4; ++t) { \
        unsigned u[8]; \
        _Pragma("unroll") for (int e = 0; e < 8; ++e) u[e] = *(const unsigned*)(scr + (8 * r + e) * 256 + t * 64 + c * 4); \
        v4u f0, f1; \
        _Pragma("unroll") for (int d = 0; d < 4; ++d) { f0[d] = perm_lo(u[2 * d], u[2 * d + 1]); f1[d] = perm_hi(u[2 * d], u[2 * d + 1]); } \
        OUT(s, t, f0, f1) } \
      FENCE(); SB(); }

    if (w < 4) {
      v4u F[32];
      {
        FRESH_IDS();
        const rsrc_t rwo = mk_rsrc(wo, DM * NH * DH * 2);
        const int vo = r * (DM * 2) + c * 16, so = head * DH * (DM * 2) + w * 256;
        unsigned char* scr = lds + L_BG + w * 16384;
#define OUT_REG(s, t, f0, f1) F[8 * (s) + 2 * (t)] = f0; F[8 * (s) + 2 * (t) + 1] = f1; asm volatile("" : "+v"(F[8 * (s) + 2 * (t)]), "+v"(F[8 * (s) + 2 * (t) + 1]));
        WO_STEP(0, OUT_REG) WO_STEP(1, OUT_REG) WO_STEP(2, OUT_REG) WO_STEP(3, OUT_REG)
      }
      STAGE_GU()
      if (is_ffn) LADD(F_WGT);
      LWAIT(F_LAST, 1, 4);
      FRESH_IDS();
      int yvo = (lane & 1) ? OOB : r * 8192 + c * 8, yso = WS_Y + ((b * 20 + 4 * w) * 64 + head * 2 + half) * 128;
      asm volatile("" : "+v"(yvo), "+s"(yso));
      v4u a[4];
      WAIT_O(a);
      f32x4 d[8];
#pragma unroll
      for (int f = 0; f < 8; ++f) d[f] = mfma(a[0], F[f], zero);
      SB();
#pragma unroll
      for (int s = 1; s < 4; ++s) {
#pragma unroll
        for (int f = 0; f < 8; ++f) d[(f >> 1) + 4 * (f & 1)] = mfma(a[s], F[8 * s + (f >> 1) + 4 * (f & 1)], d[(f >> 1) + 4 * (f & 1)]);
        if (s < 3) SB();
      }
      float t[4];
#pragma unroll
      for (int j = 0; j < 4; ++j) t[j] = rs32(d[j][0], d[j + 4][0]);
      const unsigned yp = pk_bf16(rs16(t[0], t[2]), rs16(t[1], t[3]));
      st128_sc1(v4u{yp, tag, dppu<XOR1>(yp), tag}, rws, yvo, yso);
      if (!is_ffn) return;
    } else {
      {
        FRESH_IDS();
        const rsrc_t rwo = mk_rsrc(wo, DM * NH * DH * 2);
        const int vo = r * (DM * 2) + c * 16, so = head * DH * (DM * 2) + w * 256;
        unsigned char* scr = lds + L_BG + w * 16384;
#define OUT_LDS(s, t, f0, f1) *(v4u*)(lds + L_F4 + ((t) * 8 + 2 * (s)) * 1024 + lane * 16) = f0; *(v4u*)(lds + L_F4 + ((t) * 8 + 2 * (s) + 1) * 1024 + lane * 16) = f1;
#pragma nounroll
        for (int s = 0; s < 4; ++s) WO_STEP(s, OUT_LDS)
        LGKM0();
        LFLAG(F_FRAG) = 1u;
      }
      float Mo = 0.f, Lo = 0.f;
      bool sent = false;
      const int mls = WS_ML + (b * NH + head) * 256;
#define SEND_ML() { \
        const f32x4 m01 = *(const f32x4*)(lds + L_ML), m23 = *(const f32x4*)(lds + L_ML + 16); \
        Mo = fmaxf(fmaxf(m01[0], m01[2]), fmaxf(m23[0], m23[2])); \
        Lo = fmaf(__builtin_amdgcn_exp2f(m01[0] - Mo), m01[1], __builtin_amdgcn_exp2f(m01[2] - Mo) * m01[3]) + fmaf(__builtin_amdgcn_exp2f(m23[0] - Mo), m23[1], __builtin_amdgcn_exp2f(m23[2] - Mo) * m23[3]); \
        st128_sc1(v4u{as_u(Mo), tag, as_u(Lo), tag}, rws, fresh_lane() == 0 ? 0 : OOB, mls + half * 128); sent = true; }
      STAGE_GU(if (!sent && __builtin_amdgcn_readfirstlane(LFLAG(F_KDONE)) >= (unsigned)NSW) { FENCE(); SEND_ML() })
      if (!sent) { LWAIT(F_KDONE, NSW, 8); SEND_ML() }
      {
        v4u g;
        for (unsigned spin = 0;; ++spin) {
          g = ld128_sc1(rws, 0, mls + (half ^ 1) * 128);
          if (__all(TAGBAD(g) == 0) || spin > SPIN_CAP) break;
          __builtin_amdgcn_s_sleep(16);
          FENCE();
        }
        const float Mg = fmaxf(Mo, as_f(g[0]));
        const float Lg = __builtin_amdgcn_exp2f(Mo - Mg) * Lo + __builtin_amdgcn_exp2f(as_f(g[0]) - Mg) * as_f(g[2]);
        *(f32x2*)(lds + L_ML + 32) = f32x2{Mg, 1.0f / Lg};
        LGKM0();
        LFLAG(F_NORM) = 1u;
      }
      LWAIT(F_LAST, 1, 4);
      if (is_red) {
        const int lane = fresh_lane();
        unsigned sel_lo = 0x00003f80u, sel_hi = 0x3f800000u;
        asm volatile("" : "+v"(sel_lo), "+v"(sel_hi));
#define SWEEP(N, LOADK, SELK) \
        for (unsigned spin = 0;; ++spin) { \
          v4u g[N]; \
          _Pragma("unroll") for (int k = 0; k < N; ++k) g[k] = LOADK; \
          SB(); \
          unsigned bad = 0; u0 = 0.f; u1 = 0.f; u2 = 0.f; u3 = 0.f; \
          _Pragma("unroll") for (int k = 0; k < N; ++k) { \
            bad = tagacc(tagacc(bad, g[k][1], tag), g[k][3], tag); \
            const unsigned sl = SELK(sel_lo), sh = SELK(sel_hi); \
            u0 = dot2(g[k][0], sl, u0); u1 = dot2(g[k][0], sh, u1); u2 = dot2(g[k][2], sl, u2); u3 = dot2(g[k][2], sh, u3); \
            asm volatile("" : "+v"(bad), "+v"(u0), "+v"(u1), "+v"(u2), "+v"(u3)); SB(); } \
          if (__builtin_amdgcn_ballot_w64(bad != 0u) == 0ull) break; \
          if (spin > SPIN_CAP) break; \
          FENCE(); } \
        s0 = rs32(u0, u2); s1 = rs32(u1, u3); \
        s0 = rs16(s0, s0); s1 = rs16(s1, s1); \
        s0 += dppf<ROR8>(s0); s1 += dppf<ROR8>(s1);
        float s0, s1, u0, u1, u2, u3;
        const int ys = WS_Y + (rb * 20 + cb) * 8192;
        int hvo = (lane >> 3) * 10240 + (lane & 7) * 16, ovo = (lane & 24) ? OOB : (lane & 7) * 8 + (lane >> 5) * 4;
        int hso = WS_H + rb * 2560 + cb * 128;
        asm volatile("" : "+v"(hvo), "+v"(ovo), "+s"(hso));
#define SEL_ALL(v) (v)
        SWEEP(8, ld128_sc1(rws, lane * 16, ys + k * 1024), SEL_ALL)
        const unsigned hp2 = pk_bf16(lo_f(xres) + rbf(s0), hi_f(xres) + rbf(s1));
        {
          auto hh = __builtin_amdgcn_permlane32_swap(hp2, hp2, false, false);
          st128_sc1(v4u{hh[0], tag, hh[1], tag}, rws, hvo, hso);
        }
        const int zs = WS_Z + (rb * 20 + cb) * (NFFN * 128);
        const bool has7 = lane < NFFN * 8 - 6 * 64;
        const int zo0 = has7 ? lane * 16 + 6144 : lane * 16;
#define SEL_Z(v) (k == 0 && !has7 ? 0u : (v))
        SWEEP(7, (k == 0 ? ld128_sc1(rws, zo0, zs) : ld128_sc1(rws, lane * 16, zs + (k - 1) * 1024)), SEL_Z)
        __builtin_amdgcn_raw_buffer_store_b32(pk_bf16(lo_f(hp2) + rbf(s0), hi_f(hp2) + rbf(s1)), mk_rsrc(out, NB * DM * 2),
                                              ovo, rj * 64, AUX_SC1);
        if (rj == 0) __builtin_amdgcn_raw_buffer_store_b32(tag, rws, lane == 0 ? 0 : OOB, WS_EPOCH, AUX_SC1);
      } else if (is_ffn) {
        const int lane = fresh_lane();
        {
          const GAS unsigned char* gp = (const GAS unsigned char*)((lane & 4) ? wu : wg) + (size_t)((112 + (lane >> 3)) * (FF * 2) + fi * 64 + (lane & 3) * 16);
          v4u GL[10];
#pragma unroll
          for (int n = 0; n < 10; ++n) GL[n] = *(const GAS v4u*)(gp + (128 * (n >> 1) + 8 * (n & 1)) * (FF * 2));
          SB();
#pragma unroll
          for (int n = 0; n < 10; ++n) *(v4u*)(lds + L_BG + ((n >> 1) * 16 + 14 + (n & 1)) * 1024 + lane * 16) = GL[n];
          LADD(F_WGT);
        }
        const rsrc_t rd = mk_rsrc(wd, DM * FF * 2);
        v4u D[20];
#pragma unroll
        for (int h2 = 0; h2 < 2; ++h2) {
#pragma unroll
          for (int n = 0; n < 20; ++n) D[n] = __builtin_amdgcn_raw_buffer_load_b128(rd, lane * 16, fi * 32 * (DM * 2) + (h2 * 20 + n) * 1024, 0);
          SB();
          if (h2 == 0) {
            _Pragma("nounroll") for (unsigned spin = 0; spin < (SPIN_CAP << 4); ++spin) {
              const v4u fl = *(volatile LAS v4u*)(LAS unsigned char*)(lds + L_DONE);
              if (__builtin_amdgcn_readfirstlane(fl[0] & fl[1] & fl[2] & fl[3]) != 0u) break;
              __builtin_amdgcn_s_sleep(1);
            }
            FENCE();
          }
#pragma unroll
          for (int n = 0; n < 20; ++n) *(v4u*)(lds + L_F4 + (h2 * 20 + n) * 1024 + lane * 16) = D[n];
          SB();
        }
        BAR();
        BAR();
      }
      return;
    }

    FRESH_IDS();
    v4u BG[20], BD[12];
    unsigned char* gpw = lds + L_GP + (w * 4 + r) * GP_S + (c & 7) * 32 + (c >> 3) * 8;
    const unsigned char* gpr = lds + L_GP + (c >> 2) * GP_S + (4 * r + (c & 3)) * 16;
    LWAIT(F_WGT, NHW, 0);
#pragma unroll
    for (int s = 0; s < 5; ++s) {
      v2u t[8];
#pragma unroll
      for (int e = 0; e < 8; ++e) t[e] = *(const v2u*)(lds + L_BG + (160 * w + (s < 4 ? 32 * r + 4 * s + (e & 3) + 16 * (e >> 2) : 128 + 8 * r + e)) * 128 + c * 8);
      frag4(t, BG + 4 * s);
    }
    {
      const v4u ones = {0x3f803f80u, 0x3f803f80u, 0x3f803f80u, 0x3f803f80u};
      f32x4 cs[4] = {zero, zero, zero, zero};
#pragma unroll
      for (int s = 0; s < 5; ++s)
#pragma unroll
        for (int f = 0; f < 4; ++f) cs[f] = mfma(ones, BG[4 * s + f], cs[f]);
      *(f32x2*)gpw = f32x2{cs[0][0], cs[1][0]};
      *(f32x2*)(gpw + 16) = f32x2{cs[2][0], cs[3][0]};
    }
    BAR();
    f32x4 CS = (*(const f32x4*)gpr + *(const f32x4*)(gpr + 4 * GP_S)) + (*(const f32x4*)(gpr + 8 * GP_S) + *(const f32x4*)(gpr + 12 * GP_S));
#pragma unroll
    for (int s = 0; s < 3; ++s) {
      v2u t[8];
#pragma unroll
      for (int e = 0; e < 8; ++e) t[e] = *(const v2u*)(lds + L_F4 + (8 * r + e) * (DM * 2) + (s < 2 ? w + 4 * s : 8 + (w & 1)) * 128 + c * 8);
      frag4(t, BD + 4 * s);
    }
#pragma unroll
    for (int n = 0; n < 20; ++n) asm volatile("" : "+v"(BG[n]));
#pragma unroll
    for (int n = 0; n < 12; ++n) asm volatile("" : "+v"(BD[n]));
    asm volatile("" : "+v"(CS));
    BAR();
    int zvo = (r * 20 + (c >> 3)) * (NFFN * 128) + (c & 7) * 16, sto = L_ST + (c >> 2) * 32 + (lane >> 5) * 16 + w * 4;
    int zso0 = WS_Z + (2 * w * NFFN + fi) * 128, zso2 = w < 2 ? zso0 + 16 * NFFN * 128 : OOB;
    asm volatile("" : "+v"(zvo), "+v"(sto), "+s"(zso0), "+s"(zso2));
    v4u g0, g1, g2;
    {
      const int o0 = (c >> 2) * 2560 + r * 128 + (c & 3) * 16, o1 = (c >> 2) * 2560 + 512 + r * 32 + (c & 1) * 16, hs = WS_H + xs * 10240 + w * 640;
      for (unsigned spin = 0;; ++spin) {
        g0 = ld128_sc1(rws, o0, hs);
        g1 = ld128_sc1(rws, o0 + 64, hs);
        g2 = ld128_sc1(rws, o1, hs);
        SB();
        unsigned bad = tagacc(tagacc(0u, g0[1], tag), g0[3], tag);
        bad = tagacc(tagacc(bad, g1[1], tag), g1[3], tag);
        bad = tagacc(tagacc(bad, g2[1], tag), g2[3], tag);
        if (__builtin_amdgcn_ballot_w64(bad != 0u) == 0ull) break;
        if (spin > SPIN_CAP) break;
        FENCE();
      }
    }
    f32x4 dd[4] = {zero, zero, zero, zero};
#define QB(q, v) dppu<(q) * 0x55>(v)
#define GU_STEP(s, A) { const v4u a_ = A; _Pragma("unroll") for (int f = 0; f < 4; ++f) dd[f] = mfma(a_, BG[4 * (s) + f], dd[f]); }
    GU_STEP(0, (v4u{QB(0, g0[0]), QB(0, g0[2]), QB(0, g1[0]), QB(0, g1[2])}))
    GU_STEP(1, (v4u{QB(1, g0[0]), QB(1, g0[2]), QB(1, g1[0]), QB(1, g1[2])}))
    GU_STEP(2, (v4u{QB(2, g0[0]), QB(2, g0[2]), QB(2, g1[0]), QB(2, g1[2])}))
    GU_STEP(3, (v4u{QB(3, g0[0]), QB(3, g0[2]), QB(3, g1[0]), QB(3, g1[2])}))
    GU_STEP(4, (v4u{QB(0, g2[0]), QB(0, g2[2]), QB(1, g2[0]), QB(1, g2[2])}))
    {
      const unsigned ax = (c & 2) ? g2[2] : g2[0];
      float s1 = dot2(ax, 0x3f803f80u, 0.f), s2 = dot2(ax, ax, 0.f);
      s1 = dot2(g0[0], 0x3f803f80u, s1); s2 = dot2(g0[0], g0[0], s2); s1 = dot2(g0[2], 0x3f803f80u, s1); s2 = dot2(g0[2], g0[2], s2);
      s1 = dot2(g1[0], 0x3f803f80u, s1); s2 = dot2(g1[0], g1[0], s2); s1 = dot2(g1[2], 0x3f803f80u, s1); s2 = dot2(g1[2], g1[2], s2);
      float ss = rs32(s1, s2);
      ss = rs16(ss, ss);
      ss += dppf<XOR1>(ss); ss += dppf<XOR2>(ss);
      *(float*)(lds + sto) = ss;
    }
    __builtin_amdgcn_sched_group_barrier(0x2, 4, 0);
#pragma unroll
    for (int n = 0; n < 20; ++n) { __builtin_amdgcn_sched_group_barrier(0x8, 1, 0); __builtin_amdgcn_sched_group_barrier(0x2, 2, 0); }
    *(f32x2*)gpw = f32x2{dd[0][0], dd[1][0]};
    *(f32x2*)(gpw + 16) = f32x2{dd[2][0], dd[3][0]};
    BAR();
    const f32x4 t1 = *(const f32x4*)(lds + L_ST + (c >> 2) * 32), t2 = *(const f32x4*)(lds + L_ST + (c >> 2) * 32 + 16);
    f32x4 gu[4];
#pragma unroll
    for (int ww = 0; ww < 4; ++ww) gu[ww] = *(const f32x4*)(gpr + ww * 4 * GP_S);
    SB();
    const float e1 = ((t1[0] + t1[1]) + (t1[2] + t1[3])) * (1.0f / DM), e2 = ((t2[0] + t2[1]) + (t2[2] + t2[3])) * (1.0f / DM);
    const float mb = rbf(e1), rden = ln_rden(fmaxf(fmaf(-e1, e1, e2), 0.f));
    const f32x4 GU = (((gu[0] + gu[1]) + (gu[2] + gu[3])) - mb * CS) * rden;
    float av[2];
#pragma unroll
    for (int j = 0; j < 2; ++j) {
      const float g = rbf(GU[j]), u = rbf(GU[2 + j]);
      const float t2x = 2.0f * 0.7978845608028654f * LOG2E * (g + 0.044715f * g * g * g);
      av[j] = rbf(g * __builtin_amdgcn_rcpf(1.0f + __builtin_amdgcn_exp2f(-t2x))) * u;
    }
    const unsigned ap = pk_bf16(av[0], av[1]);
    const v4u af = {dppu<0x00>(ap), dppu<0x55>(ap), dppu<0xAA>(ap), dppu<0xFF>(ap)};
    f32x4 z[12];
#define ZST(s, so) st128_sc1(v4u{pk_bf16(z[4 * (s)][0], z[4 * (s) + 1][0]), tag, pk_bf16(z[4 * (s) + 2][0], z[4 * (s) + 3][0]), tag}, rws, zvo, so)
#pragma unroll
    for (int f = 0; f < 6; ++f) z[f] = mfma(af, BD[f], zero);
    SB();
    ZST(0, zso0);
    SB();
#pragma unroll
    for (int f = 6; f < 10; ++f) z[f] = mfma(af, BD[f], zero);
    SB();
    ZST(1, zso0 + 8 * NFFN * 128);
    SB();
#pragma unroll
    for (int f = 10; f < 12; ++f) z[f] = mfma(af, BD[f], zero);
    ZST(2, zso2);
    return;
  }

  const int w = wv - NHW;
  const unsigned tstart = (unsigned)__builtin_amdgcn_s_memrealtime();
  const int lane = threadIdx.x & 63, r = lane >> 4, c = lane & 15;
  const rsrc_t rk = mk_rsrc(kc, NB * NH * SK * DH * 2), rv = mk_rsrc(vc, NB * NH * SK * DH * 2);
  const int voff = lane * 16;
  if (is_q) { __builtin_amdgcn_s_barrier(); LWAIT(F_WGT, NHW, 1); }
  v4u B[NPRE][8];
  int so = kv0;
#pragma unroll
  for (int t = 0; t < NPRE; ++t, so = NEXT(so))
#pragma unroll
    for (int i = 0; i < 8; ++i) {
      B[t][i] = LD(rk, i, so);
      if (t == 0 && i == 7) { SB(); if (!is_q) __builtin_amdgcn_s_barrier(); SB(); }
      if (8 * t + i >= 7 && 8 * t + i + 1 < 8 * NPRE) VMWAIT(7);
    }
  SB();
  *(f32x2*)(lds + L_SC + w * SC_S + 14 * 128 + lane * 8) = f32x2{-INFINITY, -INFINITY};
  LFLAG(F_AUX + w) = 1u;
  LWAIT(F_QRDY, 1, 1);
  const unsigned tag = __builtin_amdgcn_readfirstlane(LFLAG(F_TAG));
  const v4u q = *(const v4u*)(lds + L_Q + c * 16);

  const int thr0 = __builtin_amdgcn_readfirstlane((half && pair_ffn) ? 0 : is_q ? 1 : 2);
  int thr = thr0;
#define PACE(rem) { \
    const unsigned fw_ = ((unsigned)__builtin_amdgcn_s_memrealtime() + (unsigned)(rem) * TAU) | 1u; \
    const unsigned ot_ = LWORD(L_PACE + (lane & 3) * 4); \
    LWORD(L_PACE + w * 4) = fw_; \
    int dl_ = ot_ == 0u ? 0 : (int)(fw_ - ot_); \
    dl_ += (int)dppu<XOR1>((unsigned)dl_); dl_ += (int)dppu<XOR2>((unsigned)dl_); \
    dl_ = __builtin_amdgcn_readfirstlane(dl_); \
    thr = dl_ > PLEAD ? (thr0 > 0 ? thr0 - 1 : 0) : dl_ < -PLEAD ? thr0 + 1 : thr0; \
    thr = __builtin_amdgcn_readfirstlane(thr + ((int)(fw_ - tstart) < SCHED1 ? 1 : 0) + ((int)(fw_ - tstart) < SCHED2 ? 1 : 0) - ((int)(fw_ - tstart) > SCHED0 && thr > 0 ? 1 : 0)); SB(); }

  unsigned char* sc = lds + L_SC + w * SC_S + r * 32 + (c >> 1) * 4;
  for (int t = 0; t < naux; ++t) so = NEXT(so);
  KSTEP(0, 1, rk, so)
  KSTEP(1, 0, rk, 0)
  KSTEP(2, 0, rk, 0)
  KSTEP(3, 0, rk, 0)
  sc += naux * 128;
  for (int it = NPRE + naux; it < ns - 1; ++it) { so = NEXT(so); KSTEP(0, 2, rk, so) PACE(2 * ns - 1 - it) }
  so = kv0;
  KSTEP(0, 2, rv, so)
  float mw;
  {
    if (naux) LWAIT(F_AUX + w, 2, 1);
    FENCE();
    const unsigned char* sp = lds + L_SC + w * SC_S;
    const f32x4 s0 = *(const f32x4*)(sp + lane * 32), s1 = *(const f32x4*)(sp + lane * 32 + 16);
    const f32x4 s2 = *(const f32x4*)(sp + 2048 + (lane & 7) * 32), s3 = *(const f32x4*)(sp + 2048 + (lane & 7) * 32 + 16);
    float mx = -INFINITY;
#pragma unroll
    for (int j = 0; j < 4; ++j) mx = fmaxf(fmaxf(mx, fmaxf(s0[j], s1[j])), fmaxf(s2[j], s3[j]));
    mx = fmaxf(mx, dppf<ROR8>(mx)); mx = fmaxf(mx, dppf<ROR4>(mx)); mx = fmaxf(mx, dppf<ROR2>(mx)); mx = fmaxf(mx, dppf<ROR1>(mx));
    mw = xrow_max(mx) * LOG2E;
    float p[8], e[8];
#pragma unroll
    for (int j = 0; j < 4; ++j) {
      p[j] = __builtin_amdgcn_exp2f(fmaf(s0[j], LOG2E, -mw)); p[4 + j] = __builtin_amdgcn_exp2f(fmaf(s1[j], LOG2E, -mw));
      e[j] = __builtin_amdgcn_exp2f(fmaf(s2[j], LOG2E, -mw)); e[4 + j] = __builtin_amdgcn_exp2f(fmaf(s3[j], LOG2E, -mw));
    }
    FENCE();
    *(v4u*)(lds + L_SC + w * SC_S + lane * 16) = v4u{pk_bf16(p[0], p[1]), pk_bf16(p[2], p[3]), pk_bf16(p[4], p[5]), pk_bf16(p[6], p[7])};
    *(v4u*)(lds + L_SC + w * SC_S + 1024 + (lane & 7) * 16) = v4u{pk_bf16(e[0], e[1]), pk_bf16(e[2], e[3]), pk_bf16(e[4], e[5]), pk_bf16(e[6], e[7])};
    const float le = ((e[0] + e[1]) + (e[2] + e[3])) + ((e[4] + e[5]) + (e[6] + e[7]));
    float l = ((p[0] + p[1]) + (p[2] + p[3])) + ((p[4] + p[5]) + (p[6] + p[7])) + (lane < 8 ? le : 0.f);
    l += dppf<ROR8>(l); l += dppf<ROR4>(l); l += dppf<ROR2>(l); l += dppf<ROR1>(l);
    l = xrow_sum(l);
    if (lane == 0) *(f32x2*)(lds + L_ML + w * 8) = f32x2{mw, l};
    LADD(F_KDONE);
    SB();
  }
  float acc[8];
#pragma unroll
  for (int e = 0; e < 8; ++e) acc[e] = 0.f;
  const unsigned char* pp = lds + L_SC + w * SC_S + r * 16;
#define VSTEP(MODE, NXT) { \
    const v4u pw = *(const v4u*)pp; pp += 64; \
    _Pragma("unroll") for (int j = 0; j < 8; ++j) { \
      const unsigned u = pw[j >> 1], pl = (j & 1) ? u >> 16 : u & 0xffffu, ph = (j & 1) ? u & 0xffff0000u : u << 16; \
      _Pragma("unroll") for (int d = 0; d < 4; ++d) { acc[2 * d] = dot2(B[0][j][d], pl, acc[2 * d]); acc[2 * d + 1] = dot2(B[0][j][d], ph, acc[2 * d + 1]); } \
      asm volatile("" : "+v"(acc[0]), "+v"(acc[1]), "+v"(acc[2]), "+v"(acc[3]), "+v"(acc[4]), "+v"(acc[5]), "+v"(acc[6]), "+v"(acc[7])); SB(); \
      REFILL(j, MODE, rv, NXT) } }
  for (int it = 0; it < ns - 1; ++it) {
    so = NEXT(so);
    thr = __builtin_amdgcn_readfirstlane(it + BOOST >= ns ? (thr > thr0 ? 1 : 0) : thr);
    VSTEP(2, so)
    PACE(ns - 1 - it)
  }
  if (w == 0) LFLAG(F_LAST) = 1u;
  LWAIT(F_FRAG, 1, 1);
  LWAIT(F_NORM, 1, 1);
#pragma unroll
  for (int n = 0; n < 8; ++n) B[1][n] = *(const v4u*)(lds + L_F4 + (w * 8 + n) * 1024 + lane * 16);
  const f32x2 gl = *(const f32x2*)(lds + L_ML + 32);
  float f = __builtin_amdgcn_exp2f(mw - gl.x) * gl.y;
  int opo = L_OP + w * OP_S + (4 * c + r) * 4, yvo = (lane & 49) ? OOB : c * 8, yso = WS_Y + ((b * 20 + 16 + w) * 64 + head * 2 + half) * 128;
  asm volatile("" : "+v"(f), "+v"(opo), "+v"(yvo), "+s"(yso));
  SB();
  VSTEP(0, 0)
  {
    float t[4];
#pragma unroll
    for (int j = 0; j < 4; ++j) t[j] = rs32(acc[j], acc[j + 4]);
    LWORD(opo) = pk_bf16(rs16(t[0], t[2]) * f, rs16(t[1], t[3]) * f);
    LWORD(L_DONE + w * 4) = 1u;
  }
  {
    v4u a[4];
    WAIT_O(a);
    f32x4 d0 = {0.f, 0.f, 0.f, 0.f}, d1 = {0.f, 0.f, 0.f, 0.f};
#pragma unroll
    for (int s = 0; s < 4; ++s) { d0 = mfma(a[s], B[1][2 * s], d0); d1 = mfma(a[s], B[1][2 * s + 1], d1); }
    const unsigned yp = pk_bf16(xrow_sum(d0[0]), xrow_sum(d1[0]));
    st128_sc1(v4u{yp, tag, dppu<XOR1>(yp), tag}, mk_rsrc(ws, WS_END), yvo, yso);
  }
}

extern "C" void kernel_launch(void* const* d_in, const int* in_sizes, int n_in, void* d_out, int out_size, void* d_ws, size_t ws_size, hipStream_t stream) {
  (void)in_sizes; (void)n_in; (void)out_size; (void)ws_size;
  fused_block<<<NWG, NT, 0, stream>>>((const u16*)d_in[1], (const u16*)d_in[2], d_ws, (const u16*)d_in[0], (const u16*)d_in[3], (const u16*)d_in[4],
                                      (const u16*)d_in[6], (const u16*)d_in[7], (const u16*)d_in[8], (u16*)d_out);
}
```
